```python
import torch

def _softmax(x, axis=-1):
    return torch.softmax(x, dim=axis)
def _gelu(x):
    return torch.nn.functional.gelu(x, approximate="tanh")
def _silu(x):
    return torch.nn.functional.silu(x)
def _sigmoid(x):
    return torch.sigmoid(x)
def _layernorm(x, axis=-1, eps=1e-5):
    m = x.mean(dim=axis, keepdim=True)
    v = x.var(dim=axis, keepdim=True, unbiased=False)
    return (x - m) / torch.sqrt(v + eps)
def _scatter_add(tgt, idx, src, axis=0):
    return tgt.clone().index_add_(axis, idx, src)
def _segment_sum(src, seg, n):
    out = torch.zeros((n,) + src.shape[1:], dtype=src.dtype)
    return out.index_add_(0, seg, src)
def _pad(x, pw):
    flat = tuple(v for pair in reversed(pw) for v in pair)
    return torch.nn.functional.pad(x, flat)

kernel_name = 'sequential_preLN__softmax_full_mha__swiglu_ffn__prompt'

def setup_inputs():

    _w = (lambda a: (lambda t: t.to(torch.bfloat16) if t.is_floating_point() else t)(torch.as_tensor(a)))
    def _A_setup():
        B, S, D = 1, 2048, 512
        return {
            "x": _w((lambda *s: torch.randn(*s, dtype=torch.bfloat16) * 0.05)(B, S, D)),
            "wqkv": _w((lambda *s: torch.randn(*s, dtype=torch.bfloat16) * 0.05)(D, 3 * D)),
            "wo": _w((lambda *s: torch.randn(*s, dtype=torch.bfloat16) * 0.05)(D, D)),
        }
    _A_in = _A_setup()

    def _B_setup():
        B, S, D, F = 1, 2048, 512, 1792
        return {
            "x": _w((lambda *s: torch.randn(*s, dtype=torch.bfloat16) * 0.05)(B, S, D)),
            "w_gate": _w((lambda *s: torch.randn(*s, dtype=torch.bfloat16) * 0.05)(D, F)),
            "w_up": _w((lambda *s: torch.randn(*s, dtype=torch.bfloat16) * 0.05)(D, F)),
            "w_down": _w((lambda *s: torch.randn(*s, dtype=torch.bfloat16) * 0.05)(F, D)),
        }
    _B_in = _B_setup()

    return {
        "A_x": _A_in["x"],
        "A_wqkv": _A_in["wqkv"],
        "A_wo": _A_in["wo"],
        "B_x": _B_in["x"],
        "B_w_gate": _B_in["w_gate"],
        "B_w_up": _B_in["w_up"],
        "B_w_down": _B_in["w_down"],
    }

def reference(A_x, A_wqkv, A_wo, B_x, B_w_gate, B_w_up, B_w_down):
    def _A_ref(x, wqkv, wo):
        B, S, D = x.shape
        H = 32; Dh = D // H
        qkv = (lambda x, *d: x.permute(*d))((x @ wqkv).reshape(B, S, 3, H, Dh), 2, 0, 3, 1, 4)
        q, k, v = qkv[0], qkv[1], qkv[2]
        scale = Dh ** -0.5
        scores = torch.einsum("bhqd,bhkd->bhqk", q, k) * scale
        p = _softmax(scores, axis=-1)
        o = torch.einsum("bhqk,bhkd->bhqd", p, v)
        return (lambda x, *d: x.permute(*d))(o, 0, 2, 1, 3).reshape(B, S, D) @ wo

    def _B_ref(x, w_gate, w_up, w_down):
        g = _silu(x @ w_gate)
        u = x @ w_up
        return (g * u) @ w_down

    x = A_x
    h = x + _A_ref(_layernorm(x, axis=-1), A_wqkv, A_wo)
    h2 = h + _B_ref(_layernorm(h, axis=-1), B_w_gate, B_w_up, B_w_down)
    return h2
```

```cpp
#include <hip/hip_runtime.h>
#include <hip/hip_bf16.h>

typedef short bf16x8 __attribute__((ext_vector_type(8)));
typedef float f32x4 __attribute__((ext_vector_type(4)));
typedef float f32x2 __attribute__((ext_vector_type(2)));
typedef __bf16 bf16x2 __attribute__((ext_vector_type(2)));
typedef float f32x8 __attribute__((ext_vector_type(8)));
typedef float f32x16 __attribute__((ext_vector_type(16)));
typedef unsigned u32x4 __attribute__((ext_vector_type(4)));
typedef unsigned u32x2 __attribute__((ext_vector_type(2)));
typedef __attribute__((address_space(3))) char* lds_ptr;
#define LDS_AS __attribute__((address_space(3)))

#define MFMA16(a, b, c) __builtin_amdgcn_mfma_f32_16x16x32_bf16((a), (b), (c), 0, 0, 0)
#define MFMA32(a, b, c) __builtin_amdgcn_mfma_f32_32x32x16_bf16((a), (b), (c), 0, 0, 0)

__device__ __forceinline__ void lds_bar() { asm volatile("s_waitcnt lgkmcnt(0)\n\ts_barrier" ::: "memory"); }
__device__ __forceinline__ unsigned cvtpk(float lo, float hi) {
    return __builtin_bit_cast(unsigned, __builtin_convertvector((f32x2){lo, hi}, bf16x2));
}
__device__ __forceinline__ bf16x8 ldsr128(lds_ptr p) { return *(const LDS_AS bf16x8*)p; }
__device__ __forceinline__ float bflo(unsigned v) { return __uint_as_float(v << 16); }
__device__ __forceinline__ float bfhi(unsigned v) { return __uint_as_float(v & 0xffff0000u); }
__device__ __forceinline__ float xg_sum(float x) {
    auto s = __builtin_amdgcn_permlane16_swap(__float_as_uint(x), __float_as_uint(x), false, false);
    x = __uint_as_float(s[0]) + __uint_as_float(s[1]);
    auto t = __builtin_amdgcn_permlane32_swap(__float_as_uint(x), __float_as_uint(x), false, false);
    return __uint_as_float(t[0]) + __uint_as_float(t[1]);
}
__device__ __forceinline__ u32x4 swap16(unsigned a0, unsigned a1, unsigned b0, unsigned b1) {
    auto s = __builtin_amdgcn_permlane16_swap(a0, b0, false, false);
    auto t = __builtin_amdgcn_permlane16_swap(a1, b1, false, false);
    return (u32x4){s[0], t[0], s[1], t[1]};
}
#define STMOD " sc0 sc1"
#define ST16(base, voff, imm, val) asm volatile("global_store_dwordx4 %0, %1, %2 offset:%3" STMOD "\n\ts_nop 1" :: "v"(voff), "v"(val), "s"(base), "n"(imm) : "memory")

#define TSA
#define TSB
#define TSAB
#define TSDECL2
#define TSKEEP(t)
#define TSPUT(kid, t)
#define TSDECL
#define TSTAMP(slot)
#define TSFLUSH(kid, tid)
#define TSASM
#define TSOUT
#define BAR0 "s_barrier\n\t" TSASM
#define CODE_PF(d, v) "s_getpc_b64 vcc\n\t.Lpc%=:\n\ts_sub_u32 vcc_lo, vcc_lo, .Lpc%=-code_beg\n\ts_subb_u32 vcc_hi, vcc_hi, 0\n\t" \
                      "v_min_u32 " v ", code_end-code_beg, " v "\n\tglobal_load_dword " d ", " v ", vcc\n\t"
constexpr float LOG2E = 1.4426950408889634f;
constexpr float QSCALE = 0.25f * LOG2E;
constexpr float LN_EPS = 1e-5f;
constexpr int LDS_ALL = 163840;
constexpr unsigned WS_KV = 2u << 20, WS_O = 6u << 20, WS_H = 8u << 20, WS_ACT = 10u << 20, WS_ST = 18u << 20;
constexpr int CSUM = 131072, FLAG2 = CSUM + 16384, FLAG3 = FLAG2 + 32768, FLAG5 = FLAG3 + 32768, FLAGC = FLAG5 + 32768;

#define K3_IDLE \
    "s_barrier\n\t" \
    "s_barrier\n\t" \
    "s_barrier\n\t" \
    "s_barrier\n\t" \
    "s_barrier\n\t" \
    "s_barrier\n\t" \
    "s_barrier\n\t" \
    "s_barrier\n\t" \
    "s_barrier\n\t" \
    "s_barrier\n\t"
#define K3_CONS \
    "ds_read_b128 v[16:19], %[aa] offset:0\n\t" \
    "ds_read_b64_tr_b16 v[20:21], %[b0] offset:0\n\t" \
    "ds_read_b64_tr_b16 v[22:23], %[b0] offset:1024\n\t" \
    "ds_read_b64_tr_b16 v[24:25], %[b1] offset:0\n\t" \
    "ds_read_b64_tr_b16 v[26:27], %[b1] offset:1024\n\t" \
    "ds_read_b64_tr_b16 v[28:29], %[b2] offset:0\n\t" \
    "ds_read_b64_tr_b16 v[30:31], %[b2] offset:1024\n\t" \
    "ds_read_b64_tr_b16 v[32:33], %[b3] offset:0\n\t" \
    "ds_read_b64_tr_b16 v[34:35], %[b3] offset:1024\n\t" \
    "s_barrier\n\t" \
    "ds_read_b128 v[36:39], %[aa] offset:1024\n\t" \
    "ds_read_b64_tr_b16 v[40:41], %[b0] offset:4096\n\t" \
    "ds_read_b64_tr_b16 v[42:43], %[b0] offset:5120\n\t" \
    "ds_read_b64_tr_b16 v[44:45], %[b1] offset:4096\n\t" \
    "ds_read_b64_tr_b16 v[46:47], %[b1] offset:5120\n\t" \
    "ds_read_b64_tr_b16 v[48:49], %[b2] offset:4096\n\t" \
    "ds_read_b64_tr_b16 v[50:51], %[b2] offset:5120\n\t" \
    "ds_read_b64_tr_b16 v[52:53], %[b3] offset:4096\n\t" \
    "ds_read_b64_tr_b16 v[54:55], %[b3] offset:5120\n\t" \
    "s_waitcnt lgkmcnt(9)\n\t" \
    "s_barrier\n\t" \
    "v_mfma_f32_16x16x32_bf16 v[0:3], v[20:23], v[16:19], 0\n\t" \
    "ds_read_b128 v[56:59], %[aa] offset:2048\n\t" \
    "ds_read_b64_tr_b16 v[60:61], %[b0] offset:8192\n\t" \
    "v_mfma_f32_16x16x32_bf16 v[4:7], v[24:27], v[16:19], 0\n\t" \
    "ds_read_b64_tr_b16 v[62:63], %[b0] offset:9216\n\t" \
    "ds_read_b64_tr_b16 v[64:65], %[b1] offset:8192\n\t" \
    "v_mfma_f32_16x16x32_bf16 v[8:11], v[28:31], v[16:19], 0\n\t" \
    "ds_read_b64_tr_b16 v[66:67], %[b1] offset:9216\n\t" \
    "ds_read_b64_tr_b16 v[68:69], %[b2] offset:8192\n\t" \
    "v_mfma_f32_16x16x32_bf16 v[12:15], v[32:35], v[16:19], 0\n\t" \
    "ds_read_b64_tr_b16 v[70:71], %[b2] offset:9216\n\t" \
    "ds_read_b64_tr_b16 v[72:73], %[b3] offset:8192\n\t" \
    "ds_read_b64_tr_b16 v[74:75], %[b3] offset:9216\n\t" \
    "s_waitcnt lgkmcnt(9)\n\t" \
    "v_mfma_f32_16x16x32_bf16 v[0:3], v[40:43], v[36:39], v[0:3]\n\t" \
    "ds_read_b128 v[16:19], %[aa] offset:3072\n\t" \
    "ds_read_b64_tr_b16 v[20:21], %[b0] offset:12288\n\t" \
    "v_mfma_f32_16x16x32_bf16 v[4:7], v[44:47], v[36:39], v[4:7]\n\t" \
    "ds_read_b64_tr_b16 v[22:23], %[b0] offset:13312\n\t" \
    "ds_read_b64_tr_b16 v[24:25], %[b1] offset:12288\n\t" \
    "v_mfma_f32_16x16x32_bf16 v[8:11], v[48:51], v[36:39], v[8:11]\n\t" \
    "ds_read_b64_tr_b16 v[26:27], %[b1] offset:13312\n\t" \
    "ds_read_b64_tr_b16 v[28:29], %[b2] offset:12288\n\t" \
    "v_mfma_f32_16x16x32_bf16 v[12:15], v[52:55], v[36:39], v[12:15]\n\t" \
    "ds_read_b64_tr_b16 v[30:31], %[b2] offset:13312\n\t" \
    "ds_read_b64_tr_b16 v[32:33], %[b3] offset:12288\n\t" \
    "ds_read_b64_tr_b16 v[34:35], %[b3] offset:13312\n\t" \
    "s_waitcnt lgkmcnt(9)\n\t" \
    "s_barrier\n\t" \
    "v_mfma_f32_16x16x32_bf16 v[0:3], v[60:63], v[56:59], v[0:3]\n\t" \
    "ds_read_b128 v[36:39], %[aa] offset:4096\n\t" \
    "ds_read_b64_tr_b16 v[40:41], %[b0] offset:16384\n\t" \
    "v_mfma_f32_16x16x32_bf16 v[4:7], v[64:67], v[56:59], v[4:7]\n\t" \
    "ds_read_b64_tr_b16 v[42:43], %[b0] offset:17408\n\t" \
    "ds_read_b64_tr_b16 v[44:45], %[b1] offset:16384\n\t" \
    "v_mfma_f32_16x16x32_bf16 v[8:11], v[68:71], v[56:59], v[8:11]\n\t" \
    "ds_read_b64_tr_b16 v[46:47], %[b1] offset:17408\n\t" \
    "ds_read_b64_tr_b16 v[48:49], %[b2] offset:16384\n\t" \
    "v_mfma_f32_16x16x32_bf16 v[12:15], v[72:75], v[56:59], v[12:15]\n\t" \
    "ds_read_b64_tr_b16 v[50:51], %[b2] offset:17408\n\t" \
    "ds_read_b64_tr_b16 v[52:53], %[b3] offset:16384\n\t" \
    "ds_read_b64_tr_b16 v[54:55], %[b3] offset:17408\n\t" \
    "s_waitcnt lgkmcnt(9)\n\t" \
    "v_mfma_f32_16x16x32_bf16 v[0:3], v[20:23], v[16:19], v[0:3]\n\t" \
    "ds_read_b128 v[56:59], %[aa] offset:5120\n\t" \
    "ds_read_b64_tr_b16 v[60:61], %[b0] offset:20480\n\t" \
    "v_mfma_f32_16x16x32_bf16 v[4:7], v[24:27], v[16:19], v[4:7]\n\t" \
    "ds_read_b64_tr_b16 v[62:63], %[b0] offset:21504\n\t" \
    "ds_read_b64_tr_b16 v[64:65], %[b1] offset:20480\n\t" \
    "v_mfma_f32_16x16x32_bf16 v[8:11], v[28:31], v[16:19], v[8:11]\n\t" \
    "ds_read_b64_tr_b16 v[66:67], %[b1] offset:21504\n\t" \
    "ds_read_b64_tr_b16 v[68:69], %[b2] offset:20480\n\t" \
    "v_mfma_f32_16x16x32_bf16 v[12:15], v[32:35], v[16:19], v[12:15]\n\t" \
    "ds_read_b64_tr_b16 v[70:71], %[b2] offset:21504\n\t" \
    "ds_read_b64_tr_b16 v[72:73], %[b3] offset:20480\n\t" \
    "ds_read_b64_tr_b16 v[74:75], %[b3] offset:21504\n\t" \
    "s_waitcnt lgkmcnt(9)\n\t" \
    "s_barrier\n\t" \
    "v_mfma_f32_16x16x32_bf16 v[0:3], v[40:43], v[36:39], v[0:3]\n\t" \
    "ds_read_b128 v[16:19], %[aa] offset:6144\n\t" \
    "ds_read_b64_tr_b16 v[20:21], %[b0] offset:24576\n\t" \
    "v_mfma_f32_16x16x32_bf16 v[4:7], v[44:47], v[36:39], v[4:7]\n\t" \
    "ds_read_b64_tr_b16 v[22:23], %[b0] offset:25600\n\t" \
    "ds_read_b64_tr_b16 v[24:25], %[b1] offset:24576\n\t" \
    "v_mfma_f32_16x16x32_bf16 v[8:11], v[48:51], v[36:39], v[8:11]\n\t" \
    "ds_read_b64_tr_b16 v[26:27], %[b1] offset:25600\n\t" \
    "ds_read_b64_tr_b16 v[28:29], %[b2] offset:24576\n\t" \
    "v_mfma_f32_16x16x32_bf16 v[12:15], v[52:55], v[36:39], v[12:15]\n\t" \
    "ds_read_b64_tr_b16 v[30:31], %[b2] offset:25600\n\t" \
    "ds_read_b64_tr_b16 v[32:33], %[b3] offset:24576\n\t" \
    "ds_read_b64_tr_b16 v[34:35], %[b3] offset:25600\n\t" \
    "s_waitcnt lgkmcnt(9)\n\t" \
    "v_mfma_f32_16x16x32_bf16 v[0:3], v[60:63], v[56:59], v[0:3]\n\t" \
    "ds_read_b128 v[36:39], %[aa] offset:7168\n\t" \
    "ds_read_b64_tr_b16 v[40:41], %[b0] offset:28672\n\t" \
    "v_mfma_f32_16x16x32_bf16 v[4:7], v[64:67], v[56:59], v[4:7]\n\t" \
    "ds_read_b64_tr_b16 v[42:43], %[b0] offset:29696\n\t" \
    "ds_read_b64_tr_b16 v[44:45], %[b1] offset:28672\n\t" \
    "v_mfma_f32_16x16x32_bf16 v[8:11], v[68:71], v[56:59], v[8:11]\n\t" \
    "ds_read_b64_tr_b16 v[46:47], %[b1] offset:29696\n\t" \
    "ds_read_b64_tr_b16 v[48:49], %[b2] offset:28672\n\t" \
    "v_mfma_f32_16x16x32_bf16 v[12:15], v[72:75], v[56:59], v[12:15]\n\t" \
    "ds_read_b64_tr_b16 v[50:51], %[b2] offset:29696\n\t" \
    "ds_read_b64_tr_b16 v[52:53], %[b3] offset:28672\n\t" \
    "ds_read_b64_tr_b16 v[54:55], %[b3] offset:29696\n\t" \
    "s_waitcnt lgkmcnt(9)\n\t" \
    "s_barrier\n\t" \
    "v_mfma_f32_16x16x32_bf16 v[0:3], v[20:23], v[16:19], v[0:3]\n\t" \
    "ds_read_b128 v[56:59], %[aa] offset:8192\n\t" \
    "ds_read_b64_tr_b16 v[60:61], %[b0] offset:32768\n\t" \
    "v_mfma_f32_16x16x32_bf16 v[4:7], v[24:27], v[16:19], v[4:7]\n\t" \
    "ds_read_b64_tr_b16 v[62:63], %[b0] offset:33792\n\t" \
    "ds_read_b64_tr_b16 v[64:65], %[b1] offset:32768\n\t" \
    "v_mfma_f32_16x16x32_bf16 v[8:11], v[28:31], v[16:19], v[8:11]\n\t" \
    "ds_read_b64_tr_b16 v[66:67], %[b1] offset:33792\n\t" \
    "ds_read_b64_tr_b16 v[68:69], %[b2] offset:32768\n\t" \
    "v_mfma_f32_16x16x32_bf16 v[12:15], v[32:35], v[16:19], v[12:15]\n\t" \
    "ds_read_b64_tr_b16 v[70:71], %[b2] offset:33792\n\t" \
    "ds_read_b64_tr_b16 v[72:73], %[b3] offset:32768\n\t" \
    "ds_read_b64_tr_b16 v[74:75], %[b3] offset:33792\n\t" \
    "s_waitcnt lgkmcnt(9)\n\t" \
    "v_mfma_f32_16x16x32_bf16 v[0:3], v[40:43], v[36:39], v[0:3]\n\t" \
    "ds_read_b128 v[16:19], %[aa] offset:9216\n\t" \
    "ds_read_b64_tr_b16 v[20:21], %[b0] offset:36864\n\t" \
    "v_mfma_f32_16x16x32_bf16 v[4:7], v[44:47], v[36:39], v[4:7]\n\t" \
    "ds_read_b64_tr_b16 v[22:23], %[b0] offset:37888\n\t" \
    "ds_read_b64_tr_b16 v[24:25], %[b1] offset:36864\n\t" \
    "v_mfma_f32_16x16x32_bf16 v[8:11], v[48:51], v[36:39], v[8:11]\n\t" \
    "ds_read_b64_tr_b16 v[26:27], %[b1] offset:37888\n\t" \
    "ds_read_b64_tr_b16 v[28:29], %[b2] offset:36864\n\t" \
    "v_mfma_f32_16x16x32_bf16 v[12:15], v[52:55], v[36:39], v[12:15]\n\t" \
    "ds_read_b64_tr_b16 v[30:31], %[b2] offset:37888\n\t" \
    "ds_read_b64_tr_b16 v[32:33], %[b3] offset:36864\n\t" \
    "ds_read_b64_tr_b16 v[34:35], %[b3] offset:37888\n\t" \
    "s_waitcnt lgkmcnt(9)\n\t" \
    "s_barrier\n\t" \
    "v_mfma_f32_16x16x32_bf16 v[0:3], v[60:63], v[56:59], v[0:3]\n\t" \
    "ds_read_b128 v[36:39], %[aa] offset:10240\n\t" \
    "ds_read_b64_tr_b16 v[40:41], %[b0] offset:57344\n\t" \
    "v_mfma_f32_16x16x32_bf16 v[4:7], v[64:67], v[56:59], v[4:7]\n\t" \
    "ds_read_b64_tr_b16 v[42:43], %[b0] offset:58368\n\t" \
    "ds_read_b64_tr_b16 v[44:45], %[b1] offset:57344\n\t" \
    "v_mfma_f32_16x16x32_bf16 v[8:11], v[68:71], v[56:59], v[8:11]\n\t" \
    "ds_read_b64_tr_b16 v[46:47], %[b1] offset:58368\n\t" \
    "ds_read_b64_tr_b16 v[48:49], %[b2] offset:57344\n\t" \
    "v_mfma_f32_16x16x32_bf16 v[12:15], v[72:75], v[56:59], v[12:15]\n\t" \
    "ds_read_b64_tr_b16 v[50:51], %[b2] offset:58368\n\t" \
    "ds_read_b64_tr_b16 v[52:53], %[b3] offset:57344\n\t" \
    "ds_read_b64_tr_b16 v[54:55], %[b3] offset:58368\n\t" \
    "s_waitcnt lgkmcnt(9)\n\t" \
    "v_mfma_f32_16x16x32_bf16 v[0:3], v[20:23], v[16:19], v[0:3]\n\t" \
    "ds_read_b128 v[56:59], %[aa] offset:11264\n\t" \
    "ds_read_b64_tr_b16 v[60:61], %[b0] offset:61440\n\t" \
    "v_mfma_f32_16x16x32_bf16 v[4:7], v[24:27], v[16:19], v[4:7]\n\t" \
    "ds_read_b64_tr_b16 v[62:63], %[b0] offset:62464\n\t" \
    "ds_read_b64_tr_b16 v[64:65], %[b1] offset:61440\n\t" \
    "v_mfma_f32_16x16x32_bf16 v[8:11], v[28:31], v[16:19], v[8:11]\n\t" \
    "ds_read_b64_tr_b16 v[66:67], %[b1] offset:62464\n\t" \
    "ds_read_b64_tr_b16 v[68:69], %[b2] offset:61440\n\t" \
    "v_mfma_f32_16x16x32_bf16 v[12:15], v[32:35], v[16:19], v[12:15]\n\t" \
    "ds_read_b64_tr_b16 v[70:71], %[b2] offset:62464\n\t" \
    "ds_read_b64_tr_b16 v[72:73], %[b3] offset:61440\n\t" \
    "ds_read_b64_tr_b16 v[74:75], %[b3] offset:62464\n\t" \
    "s_waitcnt vmcnt(0)\n\t" \
    "v_lshlrev_b32 v84, 16, v76\n\t" \
    "v_and_b32 v76, 0xffff0000, v76\n\t" \
    "v_lshlrev_b32 v85, 16, v77\n\t" \
    "v_and_b32 v77, 0xffff0000, v77\n\t" \
    "v_lshlrev_b32 v86, 16, v78\n\t" \
    "v_and_b32 v78, 0xffff0000, v78\n\t" \
    "v_lshlrev_b32 v87, 16, v79\n\t" \
    "v_and_b32 v79, 0xffff0000, v79\n\t" \
    "v_lshlrev_b32 v88, 16, v80\n\t" \
    "v_and_b32 v80, 0xffff0000, v80\n\t" \
    "v_lshlrev_b32 v89, 16, v81\n\t" \
    "v_and_b32 v81, 0xffff0000, v81\n\t" \
    "v_lshlrev_b32 v90, 16, v82\n\t" \
    "v_and_b32 v82, 0xffff0000, v82\n\t" \
    "v_lshlrev_b32 v91, 16, v83\n\t" \
    "v_and_b32 v83, 0xffff0000, v83\n\t" \
    "v_add_f32 v0, v0, v84\n\t" \
    "v_add_f32 v1, v1, v76\n\t" \
    "v_add_f32 v2, v2, v85\n\t" \
    "v_add_f32 v3, v3, v77\n\t" \
    "v_add_f32 v4, v4, v86\n\t" \
    "v_add_f32 v5, v5, v78\n\t" \
    "v_add_f32 v6, v6, v87\n\t" \
    "v_add_f32 v7, v7, v79\n\t" \
    "v_add_f32 v8, v8, v88\n\t" \
    "v_add_f32 v9, v9, v80\n\t" \
    "v_add_f32 v10, v10, v89\n\t" \
    "v_add_f32 v11, v11, v81\n\t" \
    "v_add_f32 v12, v12, v90\n\t" \
    "v_add_f32 v13, v13, v82\n\t" \
    "v_add_f32 v14, v14, v91\n\t" \
    "v_add_f32 v15, v15, v83\n\t" \
    "s_nop 0\n\t" \
    "s_waitcnt lgkmcnt(9)\n\t" \
    "s_barrier\n\t" \
    "v_mfma_f32_16x16x32_bf16 v[0:3], v[40:43], v[36:39], v[0:3]\n\t" \
    "ds_read_b128 v[16:19], %[aa] offset:12288\n\t" \
    "ds_read_b64_tr_b16 v[20:21], %[b0] offset:49152\n\t" \
    "v_mfma_f32_16x16x32_bf16 v[4:7], v[44:47], v[36:39], v[4:7]\n\t" \
    "ds_read_b64_tr_b16 v[22:23], %[b0] offset:50176\n\t" \
    "ds_read_b64_tr_b16 v[24:25], %[b1] offset:49152\n\t" \
    "v_mfma_f32_16x16x32_bf16 v[8:11], v[48:51], v[36:39], v[8:11]\n\t" \
    "ds_read_b64_tr_b16 v[26:27], %[b1] offset:50176\n\t" \
    "ds_read_b64_tr_b16 v[28:29], %[b2] offset:49152\n\t" \
    "v_mfma_f32_16x16x32_bf16 v[12:15], v[52:55], v[36:39], v[12:15]\n\t" \
    "ds_read_b64_tr_b16 v[30:31], %[b2] offset:50176\n\t" \
    "ds_read_b64_tr_b16 v[32:33], %[b3] offset:49152\n\t" \
    "ds_read_b64_tr_b16 v[34:35], %[b3] offset:50176\n\t" \
    "s_waitcnt lgkmcnt(9)\n\t" \
    "v_mfma_f32_16x16x32_bf16 v[0:3], v[60:63], v[56:59], v[0:3]\n\t" \
    "ds_read_b128 v[36:39], %[aa] offset:13312\n\t" \
    "ds_read_b64_tr_b16 v[40:41], %[b0] offset:53248\n\t" \
    "v_mfma_f32_16x16x32_bf16 v[4:7], v[64:67], v[56:59], v[4:7]\n\t" \
    "ds_read_b64_tr_b16 v[42:43], %[b0] offset:54272\n\t" \
    "ds_read_b64_tr_b16 v[44:45], %[b1] offset:53248\n\t" \
    "v_mfma_f32_16x16x32_bf16 v[8:11], v[68:71], v[56:59], v[8:11]\n\t" \
    "ds_read_b64_tr_b16 v[46:47], %[b1] offset:54272\n\t" \
    "ds_read_b64_tr_b16 v[48:49], %[b2] offset:53248\n\t" \
    "v_mfma_f32_16x16x32_bf16 v[12:15], v[72:75], v[56:59], v[12:15]\n\t" \
    "ds_read_b64_tr_b16 v[50:51], %[b2] offset:54272\n\t" \
    "ds_read_b64_tr_b16 v[52:53], %[b3] offset:53248\n\t" \
    "ds_read_b64_tr_b16 v[54:55], %[b3] offset:54272\n\t" \
    "s_waitcnt lgkmcnt(9)\n\t" \
    "s_barrier\n\t" \
    "v_mfma_f32_16x16x32_bf16 v[0:3], v[20:23], v[16:19], v[0:3]\n\t" \
    "ds_read_b128 v[56:59], %[aa] offset:14336\n\t" \
    "ds_read_b64_tr_b16 v[60:61], %[b0] offset:40960\n\t" \
    "v_mfma_f32_16x16x32_bf16 v[4:7], v[24:27], v[16:19], v[4:7]\n\t" \
    "ds_read_b64_tr_b16 v[62:63], %[b0] offset:41984\n\t" \
    "ds_read_b64_tr_b16 v[64:65], %[b1] offset:40960\n\t" \
    "v_mfma_f32_16x16x32_bf16 v[8:11], v[28:31], v[16:19], v[8:11]\n\t" \
    "ds_read_b64_tr_b16 v[66:67], %[b1] offset:41984\n\t" \
    "ds_read_b64_tr_b16 v[68:69], %[b2] offset:40960\n\t" \
    "v_mfma_f32_16x16x32_bf16 v[12:15], v[32:35], v[16:19], v[12:15]\n\t" \
    "ds_read_b64_tr_b16 v[70:71], %[b2] offset:41984\n\t" \
    "ds_read_b64_tr_b16 v[72:73], %[b3] offset:40960\n\t" \
    "ds_read_b64_tr_b16 v[74:75], %[b3] offset:41984\n\t" \
    "s_waitcnt lgkmcnt(9)\n\t" \
    "s_barrier\n\t" \
    "v_mfma_f32_16x16x32_bf16 v[0:3], v[40:43], v[36:39], v[0:3]\n\t" \
    "ds_read_b128 v[16:19], %[aa] offset:15360\n\t" \
    "ds_read_b64_tr_b16 v[20:21], %[b0] offset:45056\n\t" \
    "v_mfma_f32_16x16x32_bf16 v[4:7], v[44:47], v[36:39], v[4:7]\n\t" \
    "ds_read_b64_tr_b16 v[22:23], %[b0] offset:46080\n\t" \
    "ds_read_b64_tr_b16 v[24:25], %[b1] offset:45056\n\t" \
    "v_mfma_f32_16x16x32_bf16 v[8:11], v[48:51], v[36:39], v[8:11]\n\t" \
    "ds_read_b64_tr_b16 v[26:27], %[b1] offset:46080\n\t" \
    "ds_read_b64_tr_b16 v[28:29], %[b2] offset:45056\n\t" \
    "v_mfma_f32_16x16x32_bf16 v[12:15], v[52:55], v[36:39], v[12:15]\n\t" \
    "ds_read_b64_tr_b16 v[30:31], %[b2] offset:46080\n\t" \
    "ds_read_b64_tr_b16 v[32:33], %[b3] offset:45056\n\t" \
    "ds_read_b64_tr_b16 v[34:35], %[b3] offset:46080\n\t" \
    "s_waitcnt lgkmcnt(9)\n\t" \
    "v_mfma_f32_16x16x32_bf16 v[0:3], v[60:63], v[56:59], v[0:3]\n\t" \
    "v_mfma_f32_16x16x32_bf16 v[4:7], v[64:67], v[56:59], v[4:7]\n\t" \
    "v_mfma_f32_16x16x32_bf16 v[8:11], v[68:71], v[56:59], v[8:11]\n\t" \
    "v_mfma_f32_16x16x32_bf16 v[12:15], v[72:75], v[56:59], v[12:15]\n\t" \
    "s_waitcnt lgkmcnt(6)\n\t" \
    "v_mfma_f32_16x16x32_bf16 v[0:3], v[20:23], v[16:19], v[0:3]\n\t" \
    "s_waitcnt lgkmcnt(4)\n\t" \
    "v_mfma_f32_16x16x32_bf16 v[4:7], v[24:27], v[16:19], v[4:7]\n\t" \
    "s_waitcnt lgkmcnt(2)\n\t" \
    "v_mfma_f32_16x16x32_bf16 v[8:11], v[28:31], v[16:19], v[8:11]\n\t" \
    "s_waitcnt lgkmcnt(0)\n\t" \
    "v_mfma_f32_16x16x32_bf16 v[12:15], v[32:35], v[16:19], v[12:15]\n\t" \
    "s_nop 7\n\t" \
    "s_nop 3\n\t"
#define K5_CONS \
    "ds_read_b128 v[16:19], %[aa] offset:0\n\t" \
    "ds_read_b64_tr_b16 v[20:21], %[b0] offset:0\n\t" \
    "ds_read_b64_tr_b16 v[22:23], %[b0] offset:2048\n\t" \
    "ds_read_b64_tr_b16 v[24:25], %[b1] offset:0\n\t" \
    "ds_read_b64_tr_b16 v[26:27], %[b1] offset:2048\n\t" \
    "ds_read_b64_tr_b16 v[28:29], %[b2] offset:0\n\t" \
    "ds_read_b64_tr_b16 v[30:31], %[b2] offset:2048\n\t" \
    "ds_read_b64_tr_b16 v[32:33], %[b3] offset:0\n\t" \
    "ds_read_b64_tr_b16 v[34:35], %[b3] offset:2048\n\t" \
    "s_barrier\n\t" \
    "ds_read_b128 v[36:39], %[aa] offset:1024\n\t" \
    "ds_read_b64_tr_b16 v[40:41], %[b0] offset:4096\n\t" \
    "ds_read_b64_tr_b16 v[42:43], %[b0] offset:6144\n\t" \
    "ds_read_b64_tr_b16 v[44:45], %[b1] offset:4096\n\t" \
    "ds_read_b64_tr_b16 v[46:47], %[b1] offset:6144\n\t" \
    "ds_read_b64_tr_b16 v[48:49], %[b2] offset:4096\n\t" \
    "ds_read_b64_tr_b16 v[50:51], %[b2] offset:6144\n\t" \
    "ds_read_b64_tr_b16 v[52:53], %[b3] offset:4096\n\t" \
    "ds_read_b64_tr_b16 v[54:55], %[b3] offset:6144\n\t" \
    "s_waitcnt lgkmcnt(9)\n\t" \
    "s_barrier\n\t" \
    "v_mfma_f32_16x16x32_bf16 v[0:3], v[20:23], v[16:19], 0\n\t" \
    "ds_read_b128 v[56:59], %[aa] offset:2048\n\t" \
    "ds_read_b64_tr_b16 v[60:61], %[b0] offset:16384\n\t" \
    "v_mfma_f32_16x16x32_bf16 v[4:7], v[24:27], v[16:19], 0\n\t" \
    "ds_read_b64_tr_b16 v[62:63], %[b0] offset:18432\n\t" \
    "ds_read_b64_tr_b16 v[64:65], %[b1] offset:16384\n\t" \
    "v_mfma_f32_16x16x32_bf16 v[8:11], v[28:31], v[16:19], 0\n\t" \
    "ds_read_b64_tr_b16 v[66:67], %[b1] offset:18432\n\t" \
    "ds_read_b64_tr_b16 v[68:69], %[b2] offset:16384\n\t" \
    "v_mfma_f32_16x16x32_bf16 v[12:15], v[32:35], v[16:19], 0\n\t" \
    "ds_read_b64_tr_b16 v[70:71], %[b2] offset:18432\n\t" \
    "ds_read_b64_tr_b16 v[72:73], %[b3] offset:16384\n\t" \
    "ds_read_b64_tr_b16 v[74:75], %[b3] offset:18432\n\t" \
    "s_waitcnt lgkmcnt(9)\n\t" \
    "v_mfma_f32_16x16x32_bf16 v[0:3], v[40:43], v[36:39], v[0:3]\n\t" \
    "ds_read_b128 v[16:19], %[aa] offset:3072\n\t" \
    "ds_read_b64_tr_b16 v[20:21], %[b0] offset:20480\n\t" \
    "v_mfma_f32_16x16x32_bf16 v[4:7], v[44:47], v[36:39], v[4:7]\n\t" \
    "ds_read_b64_tr_b16 v[22:23], %[b0] offset:22528\n\t" \
    "ds_read_b64_tr_b16 v[24:25], %[b1] offset:20480\n\t" \
    "v_mfma_f32_16x16x32_bf16 v[8:11], v[48:51], v[36:39], v[8:11]\n\t" \
    "ds_read_b64_tr_b16 v[26:27], %[b1] offset:22528\n\t" \
    "ds_read_b64_tr_b16 v[28:29], %[b2] offset:20480\n\t" \
    "v_mfma_f32_16x16x32_bf16 v[12:15], v[52:55], v[36:39], v[12:15]\n\t" \
    "ds_read_b64_tr_b16 v[30:31], %[b2] offset:22528\n\t" \
    "ds_read_b64_tr_b16 v[32:33], %[b3] offset:20480\n\t" \
    "ds_read_b64_tr_b16 v[34:35], %[b3] offset:22528\n\t" \
    "s_waitcnt lgkmcnt(9)\n\t" \
    "s_barrier\n\t" \
    "v_mfma_f32_16x16x32_bf16 v[0:3], v[60:63], v[56:59], v[0:3]\n\t" \
    "ds_read_b128 v[36:39], %[aa] offset:4096\n\t" \
    "ds_read_b64_tr_b16 v[40:41], %[b0] offset:24576\n\t" \
    "v_mfma_f32_16x16x32_bf16 v[4:7], v[64:67], v[56:59], v[4:7]\n\t" \
    "ds_read_b64_tr_b16 v[42:43], %[b0] offset:26624\n\t" \
    "ds_read_b64_tr_b16 v[44:45], %[b1] offset:24576\n\t" \
    "v_mfma_f32_16x16x32_bf16 v[8:11], v[68:71], v[56:59], v[8:11]\n\t" \
    "ds_read_b64_tr_b16 v[46:47], %[b1] offset:26624\n\t" \
    "ds_read_b64_tr_b16 v[48:49], %[b2] offset:24576\n\t" \
    "v_mfma_f32_16x16x32_bf16 v[12:15], v[72:75], v[56:59], v[12:15]\n\t" \
    "ds_read_b64_tr_b16 v[50:51], %[b2] offset:26624\n\t" \
    "ds_read_b64_tr_b16 v[52:53], %[b3] offset:24576\n\t" \
    "ds_read_b64_tr_b16 v[54:55], %[b3] offset:26624\n\t" \
    "s_waitcnt lgkmcnt(9)\n\t" \
    "v_mfma_f32_16x16x32_bf16 v[0:3], v[20:23], v[16:19], v[0:3]\n\t" \
    "ds_read_b128 v[56:59], %[aa] offset:5120\n\t" \
    "ds_read_b64_tr_b16 v[60:61], %[b0] offset:28672\n\t" \
    "v_mfma_f32_16x16x32_bf16 v[4:7], v[24:27], v[16:19], v[4:7]\n\t" \
    "ds_read_b64_tr_b16 v[62:63], %[b0] offset:30720\n\t" \
    "ds_read_b64_tr_b16 v[64:65], %[b1] offset:28672\n\t" \
    "v_mfma_f32_16x16x32_bf16 v[8:11], v[28:31], v[16:19], v[8:11]\n\t" \
    "ds_read_b64_tr_b16 v[66:67], %[b1] offset:30720\n\t" \
    "ds_read_b64_tr_b16 v[68:69], %[b2] offset:28672\n\t" \
    "v_mfma_f32_16x16x32_bf16 v[12:15], v[32:35], v[16:19], v[12:15]\n\t" \
    "ds_read_b64_tr_b16 v[70:71], %[b2] offset:30720\n\t" \
    "ds_read_b64_tr_b16 v[72:73], %[b3] offset:28672\n\t" \
    "ds_read_b64_tr_b16 v[74:75], %[b3] offset:30720\n\t" \
    "s_waitcnt lgkmcnt(9)\n\t" \
    "s_barrier\n\t" \
    "v_mfma_f32_16x16x32_bf16 v[0:3], v[40:43], v[36:39], v[0:3]\n\t" \
    "ds_read_b128 v[16:19], %[aa] offset:6144\n\t" \
    "ds_read_b64_tr_b16 v[20:21], %[b0] offset:40960\n\t" \
    "v_mfma_f32_16x16x32_bf16 v[4:7], v[44:47], v[36:39], v[4:7]\n\t" \
    "ds_read_b64_tr_b16 v[22:23], %[b0] offset:43008\n\t" \
    "ds_read_b64_tr_b16 v[24:25], %[b1] offset:40960\n\t" \
    "v_mfma_f32_16x16x32_bf16 v[8:11], v[48:51], v[36:39], v[8:11]\n\t" \
    "ds_read_b64_tr_b16 v[26:27], %[b1] offset:43008\n\t" \
    "ds_read_b64_tr_b16 v[28:29], %[b2] offset:40960\n\t" \
    "v_mfma_f32_16x16x32_bf16 v[12:15], v[52:55], v[36:39], v[12:15]\n\t" \
    "ds_read_b64_tr_b16 v[30:31], %[b2] offset:43008\n\t" \
    "ds_read_b64_tr_b16 v[32:33], %[b3] offset:40960\n\t" \
    "ds_read_b64_tr_b16 v[34:35], %[b3] offset:43008\n\t" \
    "s_waitcnt lgkmcnt(9)\n\t" \
    "v_mfma_f32_16x16x32_bf16 v[0:3], v[60:63], v[56:59], v[0:3]\n\t" \
    "ds_read_b128 v[36:39], %[aa] offset:7168\n\t" \
    "ds_read_b64_tr_b16 v[40:41], %[b0] offset:45056\n\t" \
    "v_mfma_f32_16x16x32_bf16 v[4:7], v[64:67], v[56:59], v[4:7]\n\t" \
    "ds_read_b64_tr_b16 v[42:43], %[b0] offset:47104\n\t" \
    "ds_read_b64_tr_b16 v[44:45], %[b1] offset:45056\n\t" \
    "v_mfma_f32_16x16x32_bf16 v[8:11], v[68:71], v[56:59], v[8:11]\n\t" \
    "ds_read_b64_tr_b16 v[46:47], %[b1] offset:47104\n\t" \
    "ds_read_b64_tr_b16 v[48:49], %[b2] offset:45056\n\t" \
    "v_mfma_f32_16x16x32_bf16 v[12:15], v[72:75], v[56:59], v[12:15]\n\t" \
    "ds_read_b64_tr_b16 v[50:51], %[b2] offset:47104\n\t" \
    "ds_read_b64_tr_b16 v[52:53], %[b3] offset:45056\n\t" \
    "ds_read_b64_tr_b16 v[54:55], %[b3] offset:47104\n\t" \
    "s_waitcnt lgkmcnt(9)\n\t" \
    "s_barrier\n\t" \
    "v_mfma_f32_16x16x32_bf16 v[0:3], v[20:23], v[16:19], v[0:3]\n\t" \
    "ds_read_b128 v[56:59], %[aa] offset:8192\n\t" \
    "ds_read_b64_tr_b16 v[60:61], %[c0] offset:0\n\t" \
    "v_mfma_f32_16x16x32_bf16 v[4:7], v[24:27], v[16:19], v[4:7]\n\t" \
    "ds_read_b64_tr_b16 v[62:63], %[c0] offset:2048\n\t" \
    "ds_read_b64_tr_b16 v[64:65], %[c1] offset:0\n\t" \
    "v_mfma_f32_16x16x32_bf16 v[8:11], v[28:31], v[16:19], v[8:11]\n\t" \
    "ds_read_b64_tr_b16 v[66:67], %[c1] offset:2048\n\t" \
    "ds_read_b64_tr_b16 v[68:69], %[c2] offset:0\n\t" \
    "v_mfma_f32_16x16x32_bf16 v[12:15], v[32:35], v[16:19], v[12:15]\n\t" \
    "ds_read_b64_tr_b16 v[70:71], %[c2] offset:2048\n\t" \
    "ds_read_b64_tr_b16 v[72:73], %[c3] offset:0\n\t" \
    "ds_read_b64_tr_b16 v[74:75], %[c3] offset:2048\n\t" \
    "s_waitcnt lgkmcnt(9)\n\t" \
    "v_mfma_f32_16x16x32_bf16 v[0:3], v[40:43], v[36:39], v[0:3]\n\t" \
    "ds_read_b128 v[16:19], %[aa] offset:9216\n\t" \
    "ds_read_b64_tr_b16 v[20:21], %[c0] offset:4096\n\t" \
    "v_mfma_f32_16x16x32_bf16 v[4:7], v[44:47], v[36:39], v[4:7]\n\t" \
    "ds_read_b64_tr_b16 v[22:23], %[c0] offset:6144\n\t" \
    "ds_read_b64_tr_b16 v[24:25], %[c1] offset:4096\n\t" \
    "v_mfma_f32_16x16x32_bf16 v[8:11], v[48:51], v[36:39], v[8:11]\n\t" \
    "ds_read_b64_tr_b16 v[26:27], %[c1] offset:6144\n\t" \
    "ds_read_b64_tr_b16 v[28:29], %[c2] offset:4096\n\t" \
    "v_mfma_f32_16x16x32_bf16 v[12:15], v[52:55], v[36:39], v[12:15]\n\t" \
    "ds_read_b64_tr_b16 v[30:31], %[c2] offset:6144\n\t" \
    "ds_read_b64_tr_b16 v[32:33], %[c3] offset:4096\n\t" \
    "ds_read_b64_tr_b16 v[34:35], %[c3] offset:6144\n\t" \
    "s_waitcnt lgkmcnt(9)\n\t" \
    "s_barrier\n\t" \
    "v_mfma_f32_16x16x32_bf16 v[0:3], v[60:63], v[56:59], v[0:3]\n\t" \
    "ds_read_b128 v[36:39], %[aa] offset:10240\n\t" \
    "ds_read_b64_tr_b16 v[40:41], %[c0] offset:16384\n\t" \
    "v_mfma_f32_16x16x32_bf16 v[4:7], v[64:67], v[56:59], v[4:7]\n\t" \
    "ds_read_b64_tr_b16 v[42:43], %[c0] offset:18432\n\t" \
    "ds_read_b64_tr_b16 v[44:45], %[c1] offset:16384\n\t" \
    "v_mfma_f32_16x16x32_bf16 v[8:11], v[68:71], v[56:59], v[8:11]\n\t" \
    "ds_read_b64_tr_b16 v[46:47], %[c1] offset:18432\n\t" \
    "ds_read_b64_tr_b16 v[48:49], %[c2] offset:16384\n\t" \
    "v_mfma_f32_16x16x32_bf16 v[12:15], v[72:75], v[56:59], v[12:15]\n\t" \
    "ds_read_b64_tr_b16 v[50:51], %[c2] offset:18432\n\t" \
    "ds_read_b64_tr_b16 v[52:53], %[c3] offset:16384\n\t" \
    "ds_read_b64_tr_b16 v[54:55], %[c3] offset:18432\n\t" \
    "s_waitcnt lgkmcnt(9)\n\t" \
    "v_mfma_f32_16x16x32_bf16 v[0:3], v[20:23], v[16:19], v[0:3]\n\t" \
    "ds_read_b128 v[56:59], %[aa] offset:11264\n\t" \
    "ds_read_b64_tr_b16 v[60:61], %[c0] offset:20480\n\t" \
    "v_mfma_f32_16x16x32_bf16 v[4:7], v[24:27], v[16:19], v[4:7]\n\t" \
    "ds_read_b64_tr_b16 v[62:63], %[c0] offset:22528\n\t" \
    "ds_read_b64_tr_b16 v[64:65], %[c1] offset:20480\n\t" \
    "v_mfma_f32_16x16x32_bf16 v[8:11], v[28:31], v[16:19], v[8:11]\n\t" \
    "ds_read_b64_tr_b16 v[66:67], %[c1] offset:22528\n\t" \
    "ds_read_b64_tr_b16 v[68:69], %[c2] offset:20480\n\t" \
    "v_mfma_f32_16x16x32_bf16 v[12:15], v[32:35], v[16:19], v[12:15]\n\t" \
    "ds_read_b64_tr_b16 v[70:71], %[c2] offset:22528\n\t" \
    "ds_read_b64_tr_b16 v[72:73], %[c3] offset:20480\n\t" \
    "ds_read_b64_tr_b16 v[74:75], %[c3] offset:22528\n\t" \
    "s_waitcnt lgkmcnt(9)\n\t" \
    "s_barrier\n\t" \
    "v_mfma_f32_16x16x32_bf16 v[0:3], v[40:43], v[36:39], v[0:3]\n\t" \
    "ds_read_b128 v[16:19], %[aa] offset:12288\n\t" \
    "ds_read_b64_tr_b16 v[20:21], %[c0] offset:24576\n\t" \
    "v_mfma_f32_16x16x32_bf16 v[4:7], v[44:47], v[36:39], v[4:7]\n\t" \
    "ds_read_b64_tr_b16 v[22:23], %[c0] offset:26624\n\t" \
    "ds_read_b64_tr_b16 v[24:25], %[c1] offset:24576\n\t" \
    "v_mfma_f32_16x16x32_bf16 v[8:11], v[48:51], v[36:39], v[8:11]\n\t" \
    "ds_read_b64_tr_b16 v[26:27], %[c1] offset:26624\n\t" \
    "ds_read_b64_tr_b16 v[28:29], %[c2] offset:24576\n\t" \
    "v_mfma_f32_16x16x32_bf16 v[12:15], v[52:55], v[36:39], v[12:15]\n\t" \
    "ds_read_b64_tr_b16 v[30:31], %[c2] offset:26624\n\t" \
    "ds_read_b64_tr_b16 v[32:33], %[c3] offset:24576\n\t" \
    "ds_read_b64_tr_b16 v[34:35], %[c3] offset:26624\n\t" \
    "s_waitcnt lgkmcnt(9)\n\t" \
    "v_mfma_f32_16x16x32_bf16 v[0:3], v[60:63], v[56:59], v[0:3]\n\t" \
    "ds_read_b128 v[36:39], %[aa] offset:13312\n\t" \
    "ds_read_b64_tr_b16 v[40:41], %[c0] offset:28672\n\t" \
    "v_mfma_f32_16x16x32_bf16 v[4:7], v[64:67], v[56:59], v[4:7]\n\t" \
    "ds_read_b64_tr_b16 v[42:43], %[c0] offset:30720\n\t" \
    "ds_read_b64_tr_b16 v[44:45], %[c1] offset:28672\n\t" \
    "v_mfma_f32_16x16x32_bf16 v[8:11], v[68:71], v[56:59], v[8:11]\n\t" \
    "ds_read_b64_tr_b16 v[46:47], %[c1] offset:30720\n\t" \
    "ds_read_b64_tr_b16 v[48:49], %[c2] offset:28672\n\t" \
    "v_mfma_f32_16x16x32_bf16 v[12:15], v[72:75], v[56:59], v[12:15]\n\t" \
    "ds_read_b64_tr_b16 v[50:51], %[c2] offset:30720\n\t" \
    "ds_read_b64_tr_b16 v[52:53], %[c3] offset:28672\n\t" \
    "ds_read_b64_tr_b16 v[54:55], %[c3] offset:30720\n\t" \
    "s_waitcnt lgkmcnt(9)\n\t" \
    "s_barrier\n\t" \
    "v_mfma_f32_16x16x32_bf16 v[0:3], v[20:23], v[16:19], v[0:3]\n\t" \
    "ds_read_b128 v[56:59], %[aa] offset:14336\n\t" \
    "ds_read_b64_tr_b16 v[60:61], %[c0] offset:40960\n\t" \
    "v_mfma_f32_16x16x32_bf16 v[4:7], v[24:27], v[16:19], v[4:7]\n\t" \
    "ds_read_b64_tr_b16 v[62:63], %[c0] offset:43008\n\t" \
    "ds_read_b64_tr_b16 v[64:65], %[c1] offset:40960\n\t" \
    "v_mfma_f32_16x16x32_bf16 v[8:11], v[28:31], v[16:19], v[8:11]\n\t" \
    "ds_read_b64_tr_b16 v[66:67], %[c1] offset:43008\n\t" \
    "ds_read_b64_tr_b16 v[68:69], %[c2] offset:40960\n\t" \
    "v_mfma_f32_16x16x32_bf16 v[12:15], v[32:35], v[16:19], v[12:15]\n\t" \
    "ds_read_b64_tr_b16 v[70:71], %[c2] offset:43008\n\t" \
    "ds_read_b64_tr_b16 v[72:73], %[c3] offset:40960\n\t" \
    "ds_read_b64_tr_b16 v[74:75], %[c3] offset:43008\n\t" \
    "s_waitcnt lgkmcnt(9)\n\t" \
    "v_mfma_f32_16x16x32_bf16 v[0:3], v[40:43], v[36:39], v[0:3]\n\t" \
    "ds_read_b128 v[16:19], %[aa] offset:15360\n\t" \
    "ds_read_b64_tr_b16 v[20:21], %[c0] offset:45056\n\t" \
    "v_mfma_f32_16x16x32_bf16 v[4:7], v[44:47], v[36:39], v[4:7]\n\t" \
    "ds_read_b64_tr_b16 v[22:23], %[c0] offset:47104\n\t" \
    "ds_read_b64_tr_b16 v[24:25], %[c1] offset:45056\n\t" \
    "v_mfma_f32_16x16x32_bf16 v[8:11], v[48:51], v[36:39], v[8:11]\n\t" \
    "ds_read_b64_tr_b16 v[26:27], %[c1] offset:47104\n\t" \
    "ds_read_b64_tr_b16 v[28:29], %[c2] offset:45056\n\t" \
    "v_mfma_f32_16x16x32_bf16 v[12:15], v[52:55], v[36:39], v[12:15]\n\t" \
    "ds_read_b64_tr_b16 v[30:31], %[c2] offset:47104\n\t" \
    "ds_read_b64_tr_b16 v[32:33], %[c3] offset:45056\n\t" \
    "ds_read_b64_tr_b16 v[34:35], %[c3] offset:47104\n\t" \
    "s_waitcnt lgkmcnt(9)\n\t" \
    "s_barrier\n\t" \
    "v_mfma_f32_16x16x32_bf16 v[0:3], v[60:63], v[56:59], v[0:3]\n\t" \
    "ds_read_b128 v[36:39], %[aa] offset:0\n\t" \
    "ds_read_b64_tr_b16 v[40:41], %[b0] offset:8192\n\t" \
    "v_mfma_f32_16x16x32_bf16 v[4:7], v[64:67], v[56:59], v[4:7]\n\t" \
    "ds_read_b64_tr_b16 v[42:43], %[b0] offset:10240\n\t" \
    "ds_read_b64_tr_b16 v[44:45], %[b1] offset:8192\n\t" \
    "v_mfma_f32_16x16x32_bf16 v[8:11], v[68:71], v[56:59], v[8:11]\n\t" \
    "ds_read_b64_tr_b16 v[46:47], %[b1] offset:10240\n\t" \
    "ds_read_b64_tr_b16 v[48:49], %[b2] offset:8192\n\t" \
    "v_mfma_f32_16x16x32_bf16 v[12:15], v[72:75], v[56:59], v[12:15]\n\t" \
    "ds_read_b64_tr_b16 v[50:51], %[b2] offset:10240\n\t" \
    "ds_read_b64_tr_b16 v[52:53], %[b3] offset:8192\n\t" \
    "ds_read_b64_tr_b16 v[54:55], %[b3] offset:10240\n\t" \
    "s_waitcnt lgkmcnt(9)\n\t" \
    "v_mfma_f32_16x16x32_bf16 v[0:3], v[20:23], v[16:19], v[0:3]\n\t" \
    "ds_read_b128 v[56:59], %[aa] offset:1024\n\t" \
    "ds_read_b64_tr_b16 v[60:61], %[b0] offset:12288\n\t" \
    "v_mfma_f32_16x16x32_bf16 v[4:7], v[24:27], v[16:19], v[4:7]\n\t" \
    "ds_read_b64_tr_b16 v[62:63], %[b0] offset:14336\n\t" \
    "ds_read_b64_tr_b16 v[64:65], %[b1] offset:12288\n\t" \
    "v_mfma_f32_16x16x32_bf16 v[8:11], v[28:31], v[16:19], v[8:11]\n\t" \
    "ds_read_b64_tr_b16 v[66:67], %[b1] offset:14336\n\t" \
    "ds_read_b64_tr_b16 v[68:69], %[b2] offset:12288\n\t" \
    "v_mfma_f32_16x16x32_bf16 v[12:15], v[32:35], v[16:19], v[12:15]\n\t" \
    "ds_read_b64_tr_b16 v[70:71], %[b2] offset:14336\n\t" \
    "ds_read_b64_tr_b16 v[72:73], %[b3] offset:12288\n\t" \
    "ds_read_b64_tr_b16 v[74:75], %[b3] offset:14336\n\t" \
    "s_waitcnt lgkmcnt(9)\n\t" \
    "s_barrier\n\t" \
    "v_mfma_f32_16x16x32_bf16 v[0:3], v[40:43], v[36:39], v[0:3]\n\t" \
    "ds_read_b128 v[16:19], %[aa] offset:2048\n\t" \
    "ds_read_b64_tr_b16 v[20:21], %[b0] offset:32768\n\t" \
    "v_mfma_f32_16x16x32_bf16 v[4:7], v[44:47], v[36:39], v[4:7]\n\t" \
    "ds_read_b64_tr_b16 v[22:23], %[b0] offset:34816\n\t" \
    "ds_read_b64_tr_b16 v[24:25], %[b1] offset:32768\n\t" \
    "v_mfma_f32_16x16x32_bf16 v[8:11], v[48:51], v[36:39], v[8:11]\n\t" \
    "ds_read_b64_tr_b16 v[26:27], %[b1] offset:34816\n\t" \
    "ds_read_b64_tr_b16 v[28:29], %[b2] offset:32768\n\t" \
    "v_mfma_f32_16x16x32_bf16 v[12:15], v[52:55], v[36:39], v[12:15]\n\t" \
    "ds_read_b64_tr_b16 v[30:31], %[b2] offset:34816\n\t" \
    "ds_read_b64_tr_b16 v[32:33], %[b3] offset:32768\n\t" \
    "ds_read_b64_tr_b16 v[34:35], %[b3] offset:34816\n\t" \
    "s_waitcnt lgkmcnt(9)\n\t" \
    "v_mfma_f32_16x16x32_bf16 v[0:3], v[60:63], v[56:59], v[0:3]\n\t" \
    "ds_read_b128 v[36:39], %[aa] offset:3072\n\t" \
    "ds_read_b64_tr_b16 v[40:41], %[b0] offset:36864\n\t" \
    "v_mfma_f32_16x16x32_bf16 v[4:7], v[64:67], v[56:59], v[4:7]\n\t" \
    "ds_read_b64_tr_b16 v[42:43], %[b0] offset:38912\n\t" \
    "ds_read_b64_tr_b16 v[44:45], %[b1] offset:36864\n\t" \
    "v_mfma_f32_16x16x32_bf16 v[8:11], v[68:71], v[56:59], v[8:11]\n\t" \
    "ds_read_b64_tr_b16 v[46:47], %[b1] offset:38912\n\t" \
    "ds_read_b64_tr_b16 v[48:49], %[b2] offset:36864\n\t" \
    "v_mfma_f32_16x16x32_bf16 v[12:15], v[72:75], v[56:59], v[12:15]\n\t" \
    "ds_read_b64_tr_b16 v[50:51], %[b2] offset:38912\n\t" \
    "ds_read_b64_tr_b16 v[52:53], %[b3] offset:36864\n\t" \
    "ds_read_b64_tr_b16 v[54:55], %[b3] offset:38912\n\t" \
    "s_waitcnt lgkmcnt(9)\n\t" \
    "s_barrier\n\t" \
    "v_mfma_f32_16x16x32_bf16 v[0:3], v[20:23], v[16:19], v[0:3]\n\t" \
    "ds_read_b128 v[56:59], %[aa] offset:4096\n\t" \
    "ds_read_b64_tr_b16 v[60:61], %[c0] offset:8192\n\t" \
    "v_mfma_f32_16x16x32_bf16 v[4:7], v[24:27], v[16:19], v[4:7]\n\t" \
    "ds_read_b64_tr_b16 v[62:63], %[c0] offset:10240\n\t" \
    "ds_read_b64_tr_b16 v[64:65], %[c1] offset:8192\n\t" \
    "v_mfma_f32_16x16x32_bf16 v[8:11], v[28:31], v[16:19], v[8:11]\n\t" \
    "ds_read_b64_tr_b16 v[66:67], %[c1] offset:10240\n\t" \
    "ds_read_b64_tr_b16 v[68:69], %[c2] offset:8192\n\t" \
    "v_mfma_f32_16x16x32_bf16 v[12:15], v[32:35], v[16:19], v[12:15]\n\t" \
    "ds_read_b64_tr_b16 v[70:71], %[c2] offset:10240\n\t" \
    "ds_read_b64_tr_b16 v[72:73], %[c3] offset:8192\n\t" \
    "ds_read_b64_tr_b16 v[74:75], %[c3] offset:10240\n\t" \
    "s_waitcnt lgkmcnt(9)\n\t" \
    "v_mfma_f32_16x16x32_bf16 v[0:3], v[40:43], v[36:39], v[0:3]\n\t" \
    "ds_read_b128 v[16:19], %[aa] offset:5120\n\t" \
    "ds_read_b64_tr_b16 v[20:21], %[c0] offset:12288\n\t" \
    "v_mfma_f32_16x16x32_bf16 v[4:7], v[44:47], v[36:39], v[4:7]\n\t" \
    "ds_read_b64_tr_b16 v[22:23], %[c0] offset:14336\n\t" \
    "ds_read_b64_tr_b16 v[24:25], %[c1] offset:12288\n\t" \
    "v_mfma_f32_16x16x32_bf16 v[8:11], v[48:51], v[36:39], v[8:11]\n\t" \
    "ds_read_b64_tr_b16 v[26:27], %[c1] offset:14336\n\t" \
    "ds_read_b64_tr_b16 v[28:29], %[c2] offset:12288\n\t" \
    "v_mfma_f32_16x16x32_bf16 v[12:15], v[52:55], v[36:39], v[12:15]\n\t" \
    "ds_read_b64_tr_b16 v[30:31], %[c2] offset:14336\n\t" \
    "ds_read_b64_tr_b16 v[32:33], %[c3] offset:12288\n\t" \
    "ds_read_b64_tr_b16 v[34:35], %[c3] offset:14336\n\t" \
    "s_waitcnt lgkmcnt(9)\n\t" \
    "s_barrier\n\t" \
    "v_mfma_f32_16x16x32_bf16 v[0:3], v[60:63], v[56:59], v[0:3]\n\t" \
    "ds_read_b128 v[36:39], %[aa] offset:6144\n\t" \
    "ds_read_b64_tr_b16 v[40:41], %[c0] offset:32768\n\t" \
    "v_mfma_f32_16x16x32_bf16 v[4:7], v[64:67], v[56:59], v[4:7]\n\t" \
    "ds_read_b64_tr_b16 v[42:43], %[c0] offset:34816\n\t" \
    "ds_read_b64_tr_b16 v[44:45], %[c1] offset:32768\n\t" \
    "v_mfma_f32_16x16x32_bf16 v[8:11], v[68:71], v[56:59], v[8:11]\n\t" \
    "ds_read_b64_tr_b16 v[46:47], %[c1] offset:34816\n\t" \
    "ds_read_b64_tr_b16 v[48:49], %[c2] offset:32768\n\t" \
    "v_mfma_f32_16x16x32_bf16 v[12:15], v[72:75], v[56:59], v[12:15]\n\t" \
    "ds_read_b64_tr_b16 v[50:51], %[c2] offset:34816\n\t" \
    "ds_read_b64_tr_b16 v[52:53], %[c3] offset:32768\n\t" \
    "ds_read_b64_tr_b16 v[54:55], %[c3] offset:34816\n\t" \
    "s_waitcnt lgkmcnt(9)\n\t" \
    "v_mfma_f32_16x16x32_bf16 v[0:3], v[20:23], v[16:19], v[0:3]\n\t" \
    "ds_read_b128 v[56:59], %[aa] offset:7168\n\t" \
    "ds_read_b64_tr_b16 v[60:61], %[c0] offset:36864\n\t" \
    "v_mfma_f32_16x16x32_bf16 v[4:7], v[24:27], v[16:19], v[4:7]\n\t" \
    "ds_read_b64_tr_b16 v[62:63], %[c0] offset:38912\n\t" \
    "ds_read_b64_tr_b16 v[64:65], %[c1] offset:36864\n\t" \
    "v_mfma_f32_16x16x32_bf16 v[8:11], v[28:31], v[16:19], v[8:11]\n\t" \
    "ds_read_b64_tr_b16 v[66:67], %[c1] offset:38912\n\t" \
    "ds_read_b64_tr_b16 v[68:69], %[c2] offset:36864\n\t" \
    "v_mfma_f32_16x16x32_bf16 v[12:15], v[32:35], v[16:19], v[12:15]\n\t" \
    "ds_read_b64_tr_b16 v[70:71], %[c2] offset:38912\n\t" \
    "ds_read_b64_tr_b16 v[72:73], %[c3] offset:36864\n\t" \
    "ds_read_b64_tr_b16 v[74:75], %[c3] offset:38912\n\t" \
    "s_waitcnt lgkmcnt(9)\n\t" \
    "s_barrier\n\t" \
    "v_mfma_f32_16x16x32_bf16 v[0:3], v[40:43], v[36:39], v[0:3]\n\t" \
    "ds_read_b128 v[16:19], %[aa] offset:8192\n\t" \
    "ds_read_b64_tr_b16 v[20:21], %[b0] offset:0\n\t" \
    "v_mfma_f32_16x16x32_bf16 v[4:7], v[44:47], v[36:39], v[4:7]\n\t" \
    "ds_read_b64_tr_b16 v[22:23], %[b0] offset:2048\n\t" \
    "ds_read_b64_tr_b16 v[24:25], %[b1] offset:0\n\t" \
    "v_mfma_f32_16x16x32_bf16 v[8:11], v[48:51], v[36:39], v[8:11]\n\t" \
    "ds_read_b64_tr_b16 v[26:27], %[b1] offset:2048\n\t" \
    "ds_read_b64_tr_b16 v[28:29], %[b2] offset:0\n\t" \
    "v_mfma_f32_16x16x32_bf16 v[12:15], v[52:55], v[36:39], v[12:15]\n\t" \
    "ds_read_b64_tr_b16 v[30:31], %[b2] offset:2048\n\t" \
    "ds_read_b64_tr_b16 v[32:33], %[b3] offset:0\n\t" \
    "ds_read_b64_tr_b16 v[34:35], %[b3] offset:2048\n\t" \
    "s_waitcnt lgkmcnt(9)\n\t" \
    "v_mfma_f32_16x16x32_bf16 v[0:3], v[60:63], v[56:59], v[0:3]\n\t" \
    "ds_read_b128 v[36:39], %[aa] offset:9216\n\t" \
    "ds_read_b64_tr_b16 v[40:41], %[b0] offset:4096\n\t" \
    "v_mfma_f32_16x16x32_bf16 v[4:7], v[64:67], v[56:59], v[4:7]\n\t" \
    "ds_read_b64_tr_b16 v[42:43], %[b0] offset:6144\n\t" \
    "ds_read_b64_tr_b16 v[44:45], %[b1] offset:4096\n\t" \
    "v_mfma_f32_16x16x32_bf16 v[8:11], v[68:71], v[56:59], v[8:11]\n\t" \
    "ds_read_b64_tr_b16 v[46:47], %[b1] offset:6144\n\t" \
    "ds_read_b64_tr_b16 v[48:49], %[b2] offset:4096\n\t" \
    "v_mfma_f32_16x16x32_bf16 v[12:15], v[72:75], v[56:59], v[12:15]\n\t" \
    "ds_read_b64_tr_b16 v[50:51], %[b2] offset:6144\n\t" \
    "ds_read_b64_tr_b16 v[52:53], %[b3] offset:4096\n\t" \
    "ds_read_b64_tr_b16 v[54:55], %[b3] offset:6144\n\t" \
    "s_waitcnt vmcnt(0)\n\t" \
    "v_lshlrev_b32 v84, 16, v76\n\t" \
    "v_and_b32 v76, 0xffff0000, v76\n\t" \
    "v_lshlrev_b32 v85, 16, v77\n\t" \
    "v_and_b32 v77, 0xffff0000, v77\n\t" \
    "v_lshlrev_b32 v86, 16, v78\n\t" \
    "v_and_b32 v78, 0xffff0000, v78\n\t" \
    "v_lshlrev_b32 v87, 16, v79\n\t" \
    "v_and_b32 v79, 0xffff0000, v79\n\t" \
    "v_lshlrev_b32 v88, 16, v80\n\t" \
    "v_and_b32 v80, 0xffff0000, v80\n\t" \
    "v_lshlrev_b32 v89, 16, v81\n\t" \
    "v_and_b32 v81, 0xffff0000, v81\n\t" \
    "v_lshlrev_b32 v90, 16, v82\n\t" \
    "v_and_b32 v82, 0xffff0000, v82\n\t" \
    "v_lshlrev_b32 v91, 16, v83\n\t" \
    "v_and_b32 v83, 0xffff0000, v83\n\t" \
    "v_add_f32 v0, v0, v84\n\t" \
    "v_add_f32 v1, v1, v76\n\t" \
    "v_add_f32 v2, v2, v85\n\t" \
    "v_add_f32 v3, v3, v77\n\t" \
    "v_add_f32 v4, v4, v86\n\t" \
    "v_add_f32 v5, v5, v78\n\t" \
    "v_add_f32 v6, v6, v87\n\t" \
    "v_add_f32 v7, v7, v79\n\t" \
    "v_add_f32 v8, v8, v88\n\t" \
    "v_add_f32 v9, v9, v80\n\t" \
    "v_add_f32 v10, v10, v89\n\t" \
    "v_add_f32 v11, v11, v81\n\t" \
    "v_add_f32 v12, v12, v90\n\t" \
    "v_add_f32 v13, v13, v82\n\t" \
    "v_add_f32 v14, v14, v91\n\t" \
    "v_add_f32 v15, v15, v83\n\t" \
    "s_nop 0\n\t" \
    "s_waitcnt lgkmcnt(9)\n\t" \
    "s_barrier\n\t" \
    "v_mfma_f32_16x16x32_bf16 v[0:3], v[20:23], v[16:19], v[0:3]\n\t" \
    "ds_read_b128 v[56:59], %[aa] offset:10240\n\t" \
    "ds_read_b64_tr_b16 v[60:61], %[b0] offset:16384\n\t" \
    "v_mfma_f32_16x16x32_bf16 v[4:7], v[24:27], v[16:19], v[4:7]\n\t" \
    "ds_read_b64_tr_b16 v[62:63], %[b0] offset:18432\n\t" \
    "ds_read_b64_tr_b16 v[64:65], %[b1] offset:16384\n\t" \
    "v_mfma_f32_16x16x32_bf16 v[8:11], v[28:31], v[16:19], v[8:11]\n\t" \
    "ds_read_b64_tr_b16 v[66:67], %[b1] offset:18432\n\t" \
    "ds_read_b64_tr_b16 v[68:69], %[b2] offset:16384\n\t" \
    "v_mfma_f32_16x16x32_bf16 v[12:15], v[32:35], v[16:19], v[12:15]\n\t" \
    "ds_read_b64_tr_b16 v[70:71], %[b2] offset:18432\n\t" \
    "ds_read_b64_tr_b16 v[72:73], %[b3] offset:16384\n\t" \
    "ds_read_b64_tr_b16 v[74:75], %[b3] offset:18432\n\t" \
    "s_waitcnt lgkmcnt(9)\n\t" \
    "v_mfma_f32_16x16x32_bf16 v[0:3], v[40:43], v[36:39], v[0:3]\n\t" \
    "ds_read_b128 v[16:19], %[aa] offset:11264\n\t" \
    "ds_read_b64_tr_b16 v[20:21], %[b0] offset:20480\n\t" \
    "v_mfma_f32_16x16x32_bf16 v[4:7], v[44:47], v[36:39], v[4:7]\n\t" \
    "ds_read_b64_tr_b16 v[22:23], %[b0] offset:22528\n\t" \
    "ds_read_b64_tr_b16 v[24:25], %[b1] offset:20480\n\t" \
    "v_mfma_f32_16x16x32_bf16 v[8:11], v[48:51], v[36:39], v[8:11]\n\t" \
    "ds_read_b64_tr_b16 v[26:27], %[b1] offset:22528\n\t" \
    "ds_read_b64_tr_b16 v[28:29], %[b2] offset:20480\n\t" \
    "v_mfma_f32_16x16x32_bf16 v[12:15], v[52:55], v[36:39], v[12:15]\n\t" \
    "ds_read_b64_tr_b16 v[30:31], %[b2] offset:22528\n\t" \
    "ds_read_b64_tr_b16 v[32:33], %[b3] offset:20480\n\t" \
    "ds_read_b64_tr_b16 v[34:35], %[b3] offset:22528\n\t" \
    "s_waitcnt lgkmcnt(9)\n\t" \
    "s_barrier\n\t" \
    "v_mfma_f32_16x16x32_bf16 v[0:3], v[60:63], v[56:59], v[0:3]\n\t" \
    "ds_read_b128 v[36:39], %[aa] offset:12288\n\t" \
    "ds_read_b64_tr_b16 v[40:41], %[b0] offset:24576\n\t" \
    "v_mfma_f32_16x16x32_bf16 v[4:7], v[64:67], v[56:59], v[4:7]\n\t" \
    "ds_read_b64_tr_b16 v[42:43], %[b0] offset:26624\n\t" \
    "ds_read_b64_tr_b16 v[44:45], %[b1] offset:24576\n\t" \
    "v_mfma_f32_16x16x32_bf16 v[8:11], v[68:71], v[56:59], v[8:11]\n\t" \
    "ds_read_b64_tr_b16 v[46:47], %[b1] offset:26624\n\t" \
    "ds_read_b64_tr_b16 v[48:49], %[b2] offset:24576\n\t" \
    "v_mfma_f32_16x16x32_bf16 v[12:15], v[72:75], v[56:59], v[12:15]\n\t" \
    "ds_read_b64_tr_b16 v[50:51], %[b2] offset:26624\n\t" \
    "ds_read_b64_tr_b16 v[52:53], %[b3] offset:24576\n\t" \
    "ds_read_b64_tr_b16 v[54:55], %[b3] offset:26624\n\t" \
    "s_waitcnt lgkmcnt(9)\n\t" \
    "v_mfma_f32_16x16x32_bf16 v[0:3], v[20:23], v[16:19], v[0:3]\n\t" \
    "ds_read_b128 v[56:59], %[aa] offset:13312\n\t" \
    "ds_read_b64_tr_b16 v[60:61], %[b0] offset:28672\n\t" \
    "v_mfma_f32_16x16x32_bf16 v[4:7], v[24:27], v[16:19], v[4:7]\n\t" \
    "ds_read_b64_tr_b16 v[62:63], %[b0] offset:30720\n\t" \
    "ds_read_b64_tr_b16 v[64:65], %[b1] offset:28672\n\t" \
    "v_mfma_f32_16x16x32_bf16 v[8:11], v[28:31], v[16:19], v[8:11]\n\t" \
    "ds_read_b64_tr_b16 v[66:67], %[b1] offset:30720\n\t" \
    "ds_read_b64_tr_b16 v[68:69], %[b2] offset:28672\n\t" \
    "v_mfma_f32_16x16x32_bf16 v[12:15], v[32:35], v[16:19], v[12:15]\n\t" \
    "ds_read_b64_tr_b16 v[70:71], %[b2] offset:30720\n\t" \
    "ds_read_b64_tr_b16 v[72:73], %[b3] offset:28672\n\t" \
    "ds_read_b64_tr_b16 v[74:75], %[b3] offset:30720\n\t" \
    "s_waitcnt lgkmcnt(9)\n\t" \
    "s_barrier\n\t" \
    "v_mfma_f32_16x16x32_bf16 v[0:3], v[40:43], v[36:39], v[0:3]\n\t" \
    "ds_read_b128 v[16:19], %[aa] offset:14336\n\t" \
    "ds_read_b64_tr_b16 v[20:21], %[b0] offset:40960\n\t" \
    "v_mfma_f32_16x16x32_bf16 v[4:7], v[44:47], v[36:39], v[4:7]\n\t" \
    "ds_read_b64_tr_b16 v[22:23], %[b0] offset:43008\n\t" \
    "ds_read_b64_tr_b16 v[24:25], %[b1] offset:40960\n\t" \
    "v_mfma_f32_16x16x32_bf16 v[8:11], v[48:51], v[36:39], v[8:11]\n\t" \
    "ds_read_b64_tr_b16 v[26:27], %[b1] offset:43008\n\t" \
    "ds_read_b64_tr_b16 v[28:29], %[b2] offset:40960\n\t" \
    "v_mfma_f32_16x16x32_bf16 v[12:15], v[52:55], v[36:39], v[12:15]\n\t" \
    "ds_read_b64_tr_b16 v[30:31], %[b2] offset:43008\n\t" \
    "ds_read_b64_tr_b16 v[32:33], %[b3] offset:40960\n\t" \
    "ds_read_b64_tr_b16 v[34:35], %[b3] offset:43008\n\t" \
    "s_waitcnt lgkmcnt(9)\n\t" \
    "v_mfma_f32_16x16x32_bf16 v[0:3], v[60:63], v[56:59], v[0:3]\n\t" \
    "ds_read_b128 v[36:39], %[aa] offset:15360\n\t" \
    "ds_read_b64_tr_b16 v[40:41], %[b0] offset:45056\n\t" \
    "v_mfma_f32_16x16x32_bf16 v[4:7], v[64:67], v[56:59], v[4:7]\n\t" \
    "ds_read_b64_tr_b16 v[42:43], %[b0] offset:47104\n\t" \
    "ds_read_b64_tr_b16 v[44:45], %[b1] offset:45056\n\t" \
    "v_mfma_f32_16x16x32_bf16 v[8:11], v[68:71], v[56:59], v[8:11]\n\t" \
    "ds_read_b64_tr_b16 v[46:47], %[b1] offset:47104\n\t" \
    "ds_read_b64_tr_b16 v[48:49], %[b2] offset:45056\n\t" \
    "v_mfma_f32_16x16x32_bf16 v[12:15], v[72:75], v[56:59], v[12:15]\n\t" \
    "ds_read_b64_tr_b16 v[50:51], %[b2] offset:47104\n\t" \
    "ds_read_b64_tr_b16 v[52:53], %[b3] offset:45056\n\t" \
    "ds_read_b64_tr_b16 v[54:55], %[b3] offset:47104\n\t" \
    "s_waitcnt lgkmcnt(9)\n\t" \
    "s_barrier\n\t" \
    "v_mfma_f32_16x16x32_bf16 v[0:3], v[20:23], v[16:19], v[0:3]\n\t" \
    "ds_read_b128 v[56:59], %[aa] offset:0\n\t" \
    "ds_read_b64_tr_b16 v[60:61], %[c0] offset:0\n\t" \
    "v_mfma_f32_16x16x32_bf16 v[4:7], v[24:27], v[16:19], v[4:7]\n\t" \
    "ds_read_b64_tr_b16 v[62:63], %[c0] offset:2048\n\t" \
    "ds_read_b64_tr_b16 v[64:65], %[c1] offset:0\n\t" \
    "v_mfma_f32_16x16x32_bf16 v[8:11], v[28:31], v[16:19], v[8:11]\n\t" \
    "ds_read_b64_tr_b16 v[66:67], %[c1] offset:2048\n\t" \
    "ds_read_b64_tr_b16 v[68:69], %[c2] offset:0\n\t" \
    "v_mfma_f32_16x16x32_bf16 v[12:15], v[32:35], v[16:19], v[12:15]\n\t" \
    "ds_read_b64_tr_b16 v[70:71], %[c2] offset:2048\n\t" \
    "ds_read_b64_tr_b16 v[72:73], %[c3] offset:0\n\t" \
    "ds_read_b64_tr_b16 v[74:75], %[c3] offset:2048\n\t" \
    "s_waitcnt lgkmcnt(9)\n\t" \
    "v_mfma_f32_16x16x32_bf16 v[0:3], v[40:43], v[36:39], v[0:3]\n\t" \
    "ds_read_b128 v[16:19], %[aa] offset:1024\n\t" \
    "ds_read_b64_tr_b16 v[20:21], %[c0] offset:4096\n\t" \
    "v_mfma_f32_16x16x32_bf16 v[4:7], v[44:47], v[36:39], v[4:7]\n\t" \
    "ds_read_b64_tr_b16 v[22:23], %[c0] offset:6144\n\t" \
    "ds_read_b64_tr_b16 v[24:25], %[c1] offset:4096\n\t" \
    "v_mfma_f32_16x16x32_bf16 v[8:11], v[48:51], v[36:39], v[8:11]\n\t" \
    "ds_read_b64_tr_b16 v[26:27], %[c1] offset:6144\n\t" \
    "ds_read_b64_tr_b16 v[28:29], %[c2] offset:4096\n\t" \
    "v_mfma_f32_16x16x32_bf16 v[12:15], v[52:55], v[36:39], v[12:15]\n\t" \
    "ds_read_b64_tr_b16 v[30:31], %[c2] offset:6144\n\t" \
    "ds_read_b64_tr_b16 v[32:33], %[c3] offset:4096\n\t" \
    "ds_read_b64_tr_b16 v[34:35], %[c3] offset:6144\n\t" \
    "s_waitcnt lgkmcnt(9)\n\t" \
    "s_barrier\n\t" \
    "v_mfma_f32_16x16x32_bf16 v[0:3], v[60:63], v[56:59], v[0:3]\n\t" \
    "ds_read_b128 v[36:39], %[aa] offset:2048\n\t" \
    "ds_read_b64_tr_b16 v[40:41], %[c0] offset:16384\n\t" \
    "v_mfma_f32_16x16x32_bf16 v[4:7], v[64:67], v[56:59], v[4:7]\n\t" \
    "ds_read_b64_tr_b16 v[42:43], %[c0] offset:18432\n\t" \
    "ds_read_b64_tr_b16 v[44:45], %[c1] offset:16384\n\t" \
    "v_mfma_f32_16x16x32_bf16 v[8:11], v[68:71], v[56:59], v[8:11]\n\t" \
    "ds_read_b64_tr_b16 v[46:47], %[c1] offset:18432\n\t" \
    "ds_read_b64_tr_b16 v[48:49], %[c2] offset:16384\n\t" \
    "v_mfma_f32_16x16x32_bf16 v[12:15], v[72:75], v[56:59], v[12:15]\n\t" \
    "ds_read_b64_tr_b16 v[50:51], %[c2] offset:18432\n\t" \
    "ds_read_b64_tr_b16 v[52:53], %[c3] offset:16384\n\t" \
    "ds_read_b64_tr_b16 v[54:55], %[c3] offset:18432\n\t" \
    "s_waitcnt lgkmcnt(9)\n\t" \
    "v_mfma_f32_16x16x32_bf16 v[0:3], v[20:23], v[16:19], v[0:3]\n\t" \
    "ds_read_b128 v[56:59], %[aa] offset:3072\n\t" \
    "ds_read_b64_tr_b16 v[60:61], %[c0] offset:20480\n\t" \
    "v_mfma_f32_16x16x32_bf16 v[4:7], v[24:27], v[16:19], v[4:7]\n\t" \
    "ds_read_b64_tr_b16 v[62:63], %[c0] offset:22528\n\t" \
    "ds_read_b64_tr_b16 v[64:65], %[c1] offset:20480\n\t" \
    "v_mfma_f32_16x16x32_bf16 v[8:11], v[28:31], v[16:19], v[8:11]\n\t" \
    "ds_read_b64_tr_b16 v[66:67], %[c1] offset:22528\n\t" \
    "ds_read_b64_tr_b16 v[68:69], %[c2] offset:20480\n\t" \
    "v_mfma_f32_16x16x32_bf16 v[12:15], v[32:35], v[16:19], v[12:15]\n\t" \
    "ds_read_b64_tr_b16 v[70:71], %[c2] offset:22528\n\t" \
    "ds_read_b64_tr_b16 v[72:73], %[c3] offset:20480\n\t" \
    "ds_read_b64_tr_b16 v[74:75], %[c3] offset:22528\n\t" \
    "s_waitcnt lgkmcnt(9)\n\t" \
    "s_barrier\n\t" \
    "v_mfma_f32_16x16x32_bf16 v[0:3], v[40:43], v[36:39], v[0:3]\n\t" \
    "ds_read_b128 v[16:19], %[aa] offset:4096\n\t" \
    "ds_read_b64_tr_b16 v[20:21], %[c0] offset:24576\n\t" \
    "v_mfma_f32_16x16x32_bf16 v[4:7], v[44:47], v[36:39], v[4:7]\n\t" \
    "ds_read_b64_tr_b16 v[22:23], %[c0] offset:26624\n\t" \
    "ds_read_b64_tr_b16 v[24:25], %[c1] offset:24576\n\t" \
    "v_mfma_f32_16x16x32_bf16 v[8:11], v[48:51], v[36:39], v[8:11]\n\t" \
    "ds_read_b64_tr_b16 v[26:27], %[c1] offset:26624\n\t" \
    "ds_read_b64_tr_b16 v[28:29], %[c2] offset:24576\n\t" \
    "v_mfma_f32_16x16x32_bf16 v[12:15], v[52:55], v[36:39], v[12:15]\n\t" \
    "ds_read_b64_tr_b16 v[30:31], %[c2] offset:26624\n\t" \
    "ds_read_b64_tr_b16 v[32:33], %[c3] offset:24576\n\t" \
    "ds_read_b64_tr_b16 v[34:35], %[c3] offset:26624\n\t" \
    "s_waitcnt lgkmcnt(9)\n\t" \
    "v_mfma_f32_16x16x32_bf16 v[0:3], v[60:63], v[56:59], v[0:3]\n\t" \
    "ds_read_b128 v[36:39], %[aa] offset:5120\n\t" \
    "ds_read_b64_tr_b16 v[40:41], %[c0] offset:28672\n\t" \
    "v_mfma_f32_16x16x32_bf16 v[4:7], v[64:67], v[56:59], v[4:7]\n\t" \
    "ds_read_b64_tr_b16 v[42:43], %[c0] offset:30720\n\t" \
    "ds_read_b64_tr_b16 v[44:45], %[c1] offset:28672\n\t" \
    "v_mfma_f32_16x16x32_bf16 v[8:11], v[68:71], v[56:59], v[8:11]\n\t" \
    "ds_read_b64_tr_b16 v[46:47], %[c1] offset:30720\n\t" \
    "ds_read_b64_tr_b16 v[48:49], %[c2] offset:28672\n\t" \
    "v_mfma_f32_16x16x32_bf16 v[12:15], v[72:75], v[56:59], v[12:15]\n\t" \
    "ds_read_b64_tr_b16 v[50:51], %[c2] offset:30720\n\t" \
    "ds_read_b64_tr_b16 v[52:53], %[c3] offset:28672\n\t" \
    "ds_read_b64_tr_b16 v[54:55], %[c3] offset:30720\n\t" \
    "s_waitcnt lgkmcnt(9)\n\t" \
    "s_barrier\n\t" \
    "v_mfma_f32_16x16x32_bf16 v[0:3], v[20:23], v[16:19], v[0:3]\n\t" \
    "ds_read_b128 v[56:59], %[aa] offset:6144\n\t" \
    "ds_read_b64_tr_b16 v[60:61], %[c0] offset:40960\n\t" \
    "v_mfma_f32_16x16x32_bf16 v[4:7], v[24:27], v[16:19], v[4:7]\n\t" \
    "ds_read_b64_tr_b16 v[62:63], %[c0] offset:43008\n\t" \
    "ds_read_b64_tr_b16 v[64:65], %[c1] offset:40960\n\t" \
    "v_mfma_f32_16x16x32_bf16 v[8:11], v[28:31], v[16:19], v[8:11]\n\t" \
    "ds_read_b64_tr_b16 v[66:67], %[c1] offset:43008\n\t" \
    "ds_read_b64_tr_b16 v[68:69], %[c2] offset:40960\n\t" \
    "v_mfma_f32_16x16x32_bf16 v[12:15], v[32:35], v[16:19], v[12:15]\n\t" \
    "ds_read_b64_tr_b16 v[70:71], %[c2] offset:43008\n\t" \
    "ds_read_b64_tr_b16 v[72:73], %[c3] offset:40960\n\t" \
    "ds_read_b64_tr_b16 v[74:75], %[c3] offset:43008\n\t" \
    "s_waitcnt lgkmcnt(9)\n\t" \
    "v_mfma_f32_16x16x32_bf16 v[0:3], v[40:43], v[36:39], v[0:3]\n\t" \
    "ds_read_b128 v[16:19], %[aa] offset:7168\n\t" \
    "ds_read_b64_tr_b16 v[20:21], %[c0] offset:45056\n\t" \
    "v_mfma_f32_16x16x32_bf16 v[4:7], v[44:47], v[36:39], v[4:7]\n\t" \
    "ds_read_b64_tr_b16 v[22:23], %[c0] offset:47104\n\t" \
    "ds_read_b64_tr_b16 v[24:25], %[c1] offset:45056\n\t" \
    "v_mfma_f32_16x16x32_bf16 v[8:11], v[48:51], v[36:39], v[8:11]\n\t" \
    "ds_read_b64_tr_b16 v[26:27], %[c1] offset:47104\n\t" \
    "ds_read_b64_tr_b16 v[28:29], %[c2] offset:45056\n\t" \
    "v_mfma_f32_16x16x32_bf16 v[12:15], v[52:55], v[36:39], v[12:15]\n\t" \
    "ds_read_b64_tr_b16 v[30:31], %[c2] offset:47104\n\t" \
    "ds_read_b64_tr_b16 v[32:33], %[c3] offset:45056\n\t" \
    "ds_read_b64_tr_b16 v[34:35], %[c3] offset:47104\n\t" \
    "s_waitcnt lgkmcnt(9)\n\t" \
    "s_barrier\n\t" \
    "v_mfma_f32_16x16x32_bf16 v[0:3], v[60:63], v[56:59], v[0:3]\n\t" \
    "ds_read_b128 v[36:39], %[aa] offset:8192\n\t" \
    "ds_read_b64_tr_b16 v[40:41], %[b0] offset:8192\n\t" \
    "v_mfma_f32_16x16x32_bf16 v[4:7], v[64:67], v[56:59], v[4:7]\n\t" \
    "ds_read_b64_tr_b16 v[42:43], %[b0] offset:10240\n\t" \
    "ds_read_b64_tr_b16 v[44:45], %[b1] offset:8192\n\t" \
    "v_mfma_f32_16x16x32_bf16 v[8:11], v[68:71], v[56:59], v[8:11]\n\t" \
    "ds_read_b64_tr_b16 v[46:47], %[b1] offset:10240\n\t" \
    "ds_read_b64_tr_b16 v[48:49], %[b2] offset:8192\n\t" \
    "v_mfma_f32_16x16x32_bf16 v[12:15], v[72:75], v[56:59], v[12:15]\n\t" \
    "ds_read_b64_tr_b16 v[50:51], %[b2] offset:10240\n\t" \
    "ds_read_b64_tr_b16 v[52:53], %[b3] offset:8192\n\t" \
    "ds_read_b64_tr_b16 v[54:55], %[b3] offset:10240\n\t" \
    "s_waitcnt lgkmcnt(9)\n\t" \
    "v_mfma_f32_16x16x32_bf16 v[0:3], v[20:23], v[16:19], v[0:3]\n\t" \
    "ds_read_b128 v[56:59], %[aa] offset:9216\n\t" \
    "ds_read_b64_tr_b16 v[60:61], %[b0] offset:12288\n\t" \
    "v_mfma_f32_16x16x32_bf16 v[4:7], v[24:27], v[16:19], v[4:7]\n\t" \
    "ds_read_b64_tr_b16 v[62:63], %[b0] offset:14336\n\t" \
    "ds_read_b64_tr_b16 v[64:65], %[b1] offset:12288\n\t" \
    "v_mfma_f32_16x16x32_bf16 v[8:11], v[28:31], v[16:19], v[8:11]\n\t" \
    "ds_read_b64_tr_b16 v[66:67], %[b1] offset:14336\n\t" \
    "ds_read_b64_tr_b16 v[68:69], %[b2] offset:12288\n\t" \
    "v_mfma_f32_16x16x32_bf16 v[12:15], v[32:35], v[16:19], v[12:15]\n\t" \
    "ds_read_b64_tr_b16 v[70:71], %[b2] offset:14336\n\t" \
    "ds_read_b64_tr_b16 v[72:73], %[b3] offset:12288\n\t" \
    "ds_read_b64_tr_b16 v[74:75], %[b3] offset:14336\n\t" \
    "s_waitcnt lgkmcnt(9)\n\t" \
    "s_barrier\n\t" \
    "v_mfma_f32_16x16x32_bf16 v[0:3], v[40:43], v[36:39], v[0:3]\n\t" \
    "ds_read_b128 v[16:19], %[aa] offset:10240\n\t" \
    "ds_read_b64_tr_b16 v[20:21], %[b0] offset:32768\n\t" \
    "v_mfma_f32_16x16x32_bf16 v[4:7], v[44:47], v[36:39], v[4:7]\n\t" \
    "ds_read_b64_tr_b16 v[22:23], %[b0] offset:34816\n\t" \
    "ds_read_b64_tr_b16 v[24:25], %[b1] offset:32768\n\t" \
    "v_mfma_f32_16x16x32_bf16 v[8:11], v[48:51], v[36:39], v[8:11]\n\t" \
    "ds_read_b64_tr_b16 v[26:27], %[b1] offset:34816\n\t" \
    "ds_read_b64_tr_b16 v[28:29], %[b2] offset:32768\n\t" \
    "v_mfma_f32_16x16x32_bf16 v[12:15], v[52:55], v[36:39], v[12:15]\n\t" \
    "ds_read_b64_tr_b16 v[30:31], %[b2] offset:34816\n\t" \
    "ds_read_b64_tr_b16 v[32:33], %[b3] offset:32768\n\t" \
    "ds_read_b64_tr_b16 v[34:35], %[b3] offset:34816\n\t" \
    "s_waitcnt lgkmcnt(9)\n\t" \
    "v_mfma_f32_16x16x32_bf16 v[0:3], v[60:63], v[56:59], v[0:3]\n\t" \
    "ds_read_b128 v[36:39], %[aa] offset:11264\n\t" \
    "ds_read_b64_tr_b16 v[40:41], %[b0] offset:36864\n\t" \
    "v_mfma_f32_16x16x32_bf16 v[4:7], v[64:67], v[56:59], v[4:7]\n\t" \
    "ds_read_b64_tr_b16 v[42:43], %[b0] offset:38912\n\t" \
    "ds_read_b64_tr_b16 v[44:45], %[b1] offset:36864\n\t" \
    "v_mfma_f32_16x16x32_bf16 v[8:11], v[68:71], v[56:59], v[8:11]\n\t" \
    "ds_read_b64_tr_b16 v[46:47], %[b1] offset:38912\n\t" \
    "ds_read_b64_tr_b16 v[48:49], %[b2] offset:36864\n\t" \
    "v_mfma_f32_16x16x32_bf16 v[12:15], v[72:75], v[56:59], v[12:15]\n\t" \
    "ds_read_b64_tr_b16 v[50:51], %[b2] offset:38912\n\t" \
    "ds_read_b64_tr_b16 v[52:53], %[b3] offset:36864\n\t" \
    "ds_read_b64_tr_b16 v[54:55], %[b3] offset:38912\n\t" \
    "s_waitcnt lgkmcnt(9)\n\t" \
    "s_barrier\n\t" \
    "v_mfma_f32_16x16x32_bf16 v[0:3], v[20:23], v[16:19], v[0:3]\n\t" \
    "ds_read_b128 v[56:59], %[aa] offset:12288\n\t" \
    "ds_read_b64_tr_b16 v[60:61], %[c0] offset:8192\n\t" \
    "v_mfma_f32_16x16x32_bf16 v[4:7], v[24:27], v[16:19], v[4:7]\n\t" \
    "ds_read_b64_tr_b16 v[62:63], %[c0] offset:10240\n\t" \
    "ds_read_b64_tr_b16 v[64:65], %[c1] offset:8192\n\t" \
    "v_mfma_f32_16x16x32_bf16 v[8:11], v[28:31], v[16:19], v[8:11]\n\t" \
    "ds_read_b64_tr_b16 v[66:67], %[c1] offset:10240\n\t" \
    "ds_read_b64_tr_b16 v[68:69], %[c2] offset:8192\n\t" \
    "v_mfma_f32_16x16x32_bf16 v[12:15], v[32:35], v[16:19], v[12:15]\n\t" \
    "ds_read_b64_tr_b16 v[70:71], %[c2] offset:10240\n\t" \
    "ds_read_b64_tr_b16 v[72:73], %[c3] offset:8192\n\t" \
    "ds_read_b64_tr_b16 v[74:75], %[c3] offset:10240\n\t" \
    "s_waitcnt lgkmcnt(9)\n\t" \
    "v_mfma_f32_16x16x32_bf16 v[0:3], v[40:43], v[36:39], v[0:3]\n\t" \
    "ds_read_b128 v[16:19], %[aa] offset:13312\n\t" \
    "ds_read_b64_tr_b16 v[20:21], %[c0] offset:12288\n\t" \
    "v_mfma_f32_16x16x32_bf16 v[4:7], v[44:47], v[36:39], v[4:7]\n\t" \
    "ds_read_b64_tr_b16 v[22:23], %[c0] offset:14336\n\t" \
    "ds_read_b64_tr_b16 v[24:25], %[c1] offset:12288\n\t" \
    "v_mfma_f32_16x16x32_bf16 v[8:11], v[48:51], v[36:39], v[8:11]\n\t" \
    "ds_read_b64_tr_b16 v[26:27], %[c1] offset:14336\n\t" \
    "ds_read_b64_tr_b16 v[28:29], %[c2] offset:12288\n\t" \
    "v_mfma_f32_16x16x32_bf16 v[12:15], v[52:55], v[36:39], v[12:15]\n\t" \
    "ds_read_b64_tr_b16 v[30:31], %[c2] offset:14336\n\t" \
    "ds_read_b64_tr_b16 v[32:33], %[c3] offset:12288\n\t" \
    "ds_read_b64_tr_b16 v[34:35], %[c3] offset:14336\n\t" \
    "s_waitcnt lgkmcnt(9)\n\t" \
    "s_barrier\n\t" \
    "v_mfma_f32_16x16x32_bf16 v[0:3], v[60:63], v[56:59], v[0:3]\n\t" \
    "ds_read_b128 v[36:39], %[aa] offset:14336\n\t" \
    "ds_read_b64_tr_b16 v[40:41], %[c0] offset:32768\n\t" \
    "v_mfma_f32_16x16x32_bf16 v[4:7], v[64:67], v[56:59], v[4:7]\n\t" \
    "ds_read_b64_tr_b16 v[42:43], %[c0] offset:34816\n\t" \
    "ds_read_b64_tr_b16 v[44:45], %[c1] offset:32768\n\t" \
    "v_mfma_f32_16x16x32_bf16 v[8:11], v[68:71], v[56:59], v[8:11]\n\t" \
    "ds_read_b64_tr_b16 v[46:47], %[c1] offset:34816\n\t" \
    "ds_read_b64_tr_b16 v[48:49], %[c2] offset:32768\n\t" \
    "v_mfma_f32_16x16x32_bf16 v[12:15], v[72:75], v[56:59], v[12:15]\n\t" \
    "ds_read_b64_tr_b16 v[50:51], %[c2] offset:34816\n\t" \
    "ds_read_b64_tr_b16 v[52:53], %[c3] offset:32768\n\t" \
    "ds_read_b64_tr_b16 v[54:55], %[c3] offset:34816\n\t" \
    "s_waitcnt lgkmcnt(9)\n\t" \
    "v_mfma_f32_16x16x32_bf16 v[0:3], v[20:23], v[16:19], v[0:3]\n\t" \
    "ds_read_b128 v[56:59], %[aa] offset:15360\n\t" \
    "ds_read_b64_tr_b16 v[60:61], %[c0] offset:36864\n\t" \
    "v_mfma_f32_16x16x32_bf16 v[4:7], v[24:27], v[16:19], v[4:7]\n\t" \
    "ds_read_b64_tr_b16 v[62:63], %[c0] offset:38912\n\t" \
    "ds_read_b64_tr_b16 v[64:65], %[c1] offset:36864\n\t" \
    "v_mfma_f32_16x16x32_bf16 v[8:11], v[28:31], v[16:19], v[8:11]\n\t" \
    "ds_read_b64_tr_b16 v[66:67], %[c1] offset:38912\n\t" \
    "ds_read_b64_tr_b16 v[68:69], %[c2] offset:36864\n\t" \
    "v_mfma_f32_16x16x32_bf16 v[12:15], v[32:35], v[16:19], v[12:15]\n\t" \
    "ds_read_b64_tr_b16 v[70:71], %[c2] offset:38912\n\t" \
    "ds_read_b64_tr_b16 v[72:73], %[c3] offset:36864\n\t" \
    "ds_read_b64_tr_b16 v[74:75], %[c3] offset:38912\n\t" \
    "s_waitcnt lgkmcnt(9)\n\t" \
    "s_barrier\n\t" \
    "v_mfma_f32_16x16x32_bf16 v[0:3], v[40:43], v[36:39], v[0:3]\n\t" \
    "ds_read_b128 v[16:19], %[aa] offset:0\n\t" \
    "ds_read_b64_tr_b16 v[20:21], %[b0] offset:0\n\t" \
    "v_mfma_f32_16x16x32_bf16 v[4:7], v[44:47], v[36:39], v[4:7]\n\t" \
    "ds_read_b64_tr_b16 v[22:23], %[b0] offset:2048\n\t" \
    "ds_read_b64_tr_b16 v[24:25], %[b1] offset:0\n\t" \
    "v_mfma_f32_16x16x32_bf16 v[8:11], v[48:51], v[36:39], v[8:11]\n\t" \
    "ds_read_b64_tr_b16 v[26:27], %[b1] offset:2048\n\t" \
    "ds_read_b64_tr_b16 v[28:29], %[b2] offset:0\n\t" \
    "v_mfma_f32_16x16x32_bf16 v[12:15], v[52:55], v[36:39], v[12:15]\n\t" \
    "ds_read_b64_tr_b16 v[30:31], %[b2] offset:2048\n\t" \
    "ds_read_b64_tr_b16 v[32:33], %[b3] offset:0\n\t" \
    "ds_read_b64_tr_b16 v[34:35], %[b3] offset:2048\n\t" \
    "s_waitcnt lgkmcnt(9)\n\t" \
    "v_mfma_f32_16x16x32_bf16 v[0:3], v[60:63], v[56:59], v[0:3]\n\t" \
    "ds_read_b128 v[36:39], %[aa] offset:1024\n\t" \
    "ds_read_b64_tr_b16 v[40:41], %[b0] offset:4096\n\t" \
    "v_mfma_f32_16x16x32_bf16 v[4:7], v[64:67], v[56:59], v[4:7]\n\t" \
    "ds_read_b64_tr_b16 v[42:43], %[b0] offset:6144\n\t" \
    "ds_read_b64_tr_b16 v[44:45], %[b1] offset:4096\n\t" \
    "v_mfma_f32_16x16x32_bf16 v[8:11], v[68:71], v[56:59], v[8:11]\n\t" \
    "ds_read_b64_tr_b16 v[46:47], %[b1] offset:6144\n\t" \
    "ds_read_b64_tr_b16 v[48:49], %[b2] offset:4096\n\t" \
    "v_mfma_f32_16x16x32_bf16 v[12:15], v[72:75], v[56:59], v[12:15]\n\t" \
    "ds_read_b64_tr_b16 v[50:51], %[b2] offset:6144\n\t" \
    "ds_read_b64_tr_b16 v[52:53], %[b3] offset:4096\n\t" \
    "ds_read_b64_tr_b16 v[54:55], %[b3] offset:6144\n\t" \
    "s_waitcnt lgkmcnt(9)\n\t" \
    "s_barrier\n\t" \
    "v_mfma_f32_16x16x32_bf16 v[0:3], v[20:23], v[16:19], v[0:3]\n\t" \
    "ds_read_b128 v[56:59], %[aa] offset:2048\n\t" \
    "ds_read_b64_tr_b16 v[60:61], %[b0] offset:16384\n\t" \
    "v_mfma_f32_16x16x32_bf16 v[4:7], v[24:27], v[16:19], v[4:7]\n\t" \
    "ds_read_b64_tr_b16 v[62:63], %[b0] offset:18432\n\t" \
    "ds_read_b64_tr_b16 v[64:65], %[b1] offset:16384\n\t" \
    "v_mfma_f32_16x16x32_bf16 v[8:11], v[28:31], v[16:19], v[8:11]\n\t" \
    "ds_read_b64_tr_b16 v[66:67], %[b1] offset:18432\n\t" \
    "ds_read_b64_tr_b16 v[68:69], %[b2] offset:16384\n\t" \
    "v_mfma_f32_16x16x32_bf16 v[12:15], v[32:35], v[16:19], v[12:15]\n\t" \
    "ds_read_b64_tr_b16 v[70:71], %[b2] offset:18432\n\t" \
    "ds_read_b64_tr_b16 v[72:73], %[b3] offset:16384\n\t" \
    "ds_read_b64_tr_b16 v[74:75], %[b3] offset:18432\n\t" \
    "s_waitcnt lgkmcnt(9)\n\t" \
    "v_mfma_f32_16x16x32_bf16 v[0:3], v[40:43], v[36:39], v[0:3]\n\t" \
    "ds_read_b128 v[16:19], %[aa] offset:3072\n\t" \
    "ds_read_b64_tr_b16 v[20:21], %[b0] offset:20480\n\t" \
    "v_mfma_f32_16x16x32_bf16 v[4:7], v[44:47], v[36:39], v[4:7]\n\t" \
    "ds_read_b64_tr_b16 v[22:23], %[b0] offset:22528\n\t" \
    "ds_read_b64_tr_b16 v[24:25], %[b1] offset:20480\n\t" \
    "v_mfma_f32_16x16x32_bf16 v[8:11], v[48:51], v[36:39], v[8:11]\n\t" \
    "ds_read_b64_tr_b16 v[26:27], %[b1] offset:22528\n\t" \
    "ds_read_b64_tr_b16 v[28:29], %[b2] offset:20480\n\t" \
    "v_mfma_f32_16x16x32_bf16 v[12:15], v[52:55], v[36:39], v[12:15]\n\t" \
    "ds_read_b64_tr_b16 v[30:31], %[b2] offset:22528\n\t" \
    "ds_read_b64_tr_b16 v[32:33], %[b3] offset:20480\n\t" \
    "ds_read_b64_tr_b16 v[34:35], %[b3] offset:22528\n\t" \
    "s_waitcnt lgkmcnt(9)\n\t" \
    "s_barrier\n\t" \
    "v_mfma_f32_16x16x32_bf16 v[0:3], v[60:63], v[56:59], v[0:3]\n\t" \
    "ds_read_b128 v[36:39], %[aa] offset:4096\n\t" \
    "ds_read_b64_tr_b16 v[40:41], %[b0] offset:24576\n\t" \
    "v_mfma_f32_16x16x32_bf16 v[4:7], v[64:67], v[56:59], v[4:7]\n\t" \
    "ds_read_b64_tr_b16 v[42:43], %[b0] offset:26624\n\t" \
    "ds_read_b64_tr_b16 v[44:45], %[b1] offset:24576\n\t" \
    "v_mfma_f32_16x16x32_bf16 v[8:11], v[68:71], v[56:59], v[8:11]\n\t" \
    "ds_read_b64_tr_b16 v[46:47], %[b1] offset:26624\n\t" \
    "ds_read_b64_tr_b16 v[48:49], %[b2] offset:24576\n\t" \
    "v_mfma_f32_16x16x32_bf16 v[12:15], v[72:75], v[56:59], v[12:15]\n\t" \
    "ds_read_b64_tr_b16 v[50:51], %[b2] offset:26624\n\t" \
    "ds_read_b64_tr_b16 v[52:53], %[b3] offset:24576\n\t" \
    "ds_read_b64_tr_b16 v[54:55], %[b3] offset:26624\n\t" \
    "s_waitcnt lgkmcnt(9)\n\t" \
    "v_mfma_f32_16x16x32_bf16 v[0:3], v[20:23], v[16:19], v[0:3]\n\t" \
    "ds_read_b128 v[56:59], %[aa] offset:5120\n\t" \
    "ds_read_b64_tr_b16 v[60:61], %[b0] offset:28672\n\t" \
    "v_mfma_f32_16x16x32_bf16 v[4:7], v[24:27], v[16:19], v[4:7]\n\t" \
    "ds_read_b64_tr_b16 v[62:63], %[b0] offset:30720\n\t" \
    "ds_read_b64_tr_b16 v[64:65], %[b1] offset:28672\n\t" \
    "v_mfma_f32_16x16x32_bf16 v[8:11], v[28:31], v[16:19], v[8:11]\n\t" \
    "ds_read_b64_tr_b16 v[66:67], %[b1] offset:30720\n\t" \
    "ds_read_b64_tr_b16 v[68:69], %[b2] offset:28672\n\t" \
    "v_mfma_f32_16x16x32_bf16 v[12:15], v[32:35], v[16:19], v[12:15]\n\t" \
    "ds_read_b64_tr_b16 v[70:71], %[b2] offset:30720\n\t" \
    "ds_read_b64_tr_b16 v[72:73], %[b3] offset:28672\n\t" \
    "ds_read_b64_tr_b16 v[74:75], %[b3] offset:30720\n\t" \
    "s_waitcnt lgkmcnt(9)\n\t" \
    "s_barrier\n\t" \
    "v_mfma_f32_16x16x32_bf16 v[0:3], v[40:43], v[36:39], v[0:3]\n\t" \
    "ds_read_b128 v[16:19], %[aa] offset:6144\n\t" \
    "ds_read_b64_tr_b16 v[20:21], %[b0] offset:40960\n\t" \
    "v_mfma_f32_16x16x32_bf16 v[4:7], v[44:47], v[36:39], v[4:7]\n\t" \
    "ds_read_b64_tr_b16 v[22:23], %[b0] offset:43008\n\t" \
    "ds_read_b64_tr_b16 v[24:25], %[b1] offset:40960\n\t" \
    "v_mfma_f32_16x16x32_bf16 v[8:11], v[48:51], v[36:39], v[8:11]\n\t" \
    "ds_read_b64_tr_b16 v[26:27], %[b1] offset:43008\n\t" \
    "ds_read_b64_tr_b16 v[28:29], %[b2] offset:40960\n\t" \
    "v_mfma_f32_16x16x32_bf16 v[12:15], v[52:55], v[36:39], v[12:15]\n\t" \
    "ds_read_b64_tr_b16 v[30:31], %[b2] offset:43008\n\t" \
    "ds_read_b64_tr_b16 v[32:33], %[b3] offset:40960\n\t" \
    "ds_read_b64_tr_b16 v[34:35], %[b3] offset:43008\n\t" \
    "s_waitcnt lgkmcnt(9)\n\t" \
    "s_barrier\n\t" \
    "v_mfma_f32_16x16x32_bf16 v[0:3], v[60:63], v[56:59], v[0:3]\n\t" \
    "ds_read_b128 v[36:39], %[aa] offset:7168\n\t" \
    "ds_read_b64_tr_b16 v[40:41], %[b0] offset:45056\n\t" \
    "v_mfma_f32_16x16x32_bf16 v[4:7], v[64:67], v[56:59], v[4:7]\n\t" \
    "ds_read_b64_tr_b16 v[42:43], %[b0] offset:47104\n\t" \
    "ds_read_b64_tr_b16 v[44:45], %[b1] offset:45056\n\t" \
    "v_mfma_f32_16x16x32_bf16 v[8:11], v[68:71], v[56:59], v[8:11]\n\t" \
    "ds_read_b64_tr_b16 v[46:47], %[b1] offset:47104\n\t" \
    "ds_read_b64_tr_b16 v[48:49], %[b2] offset:45056\n\t" \
    "v_mfma_f32_16x16x32_bf16 v[12:15], v[72:75], v[56:59], v[12:15]\n\t" \
    "ds_read_b64_tr_b16 v[50:51], %[b2] offset:47104\n\t" \
    "ds_read_b64_tr_b16 v[52:53], %[b3] offset:45056\n\t" \
    "ds_read_b64_tr_b16 v[54:55], %[b3] offset:47104\n\t" \
    "s_waitcnt lgkmcnt(9)\n\t" \
    "v_mfma_f32_16x16x32_bf16 v[0:3], v[20:23], v[16:19], v[0:3]\n\t" \
    "v_mfma_f32_16x16x32_bf16 v[4:7], v[24:27], v[16:19], v[4:7]\n\t" \
    "v_mfma_f32_16x16x32_bf16 v[8:11], v[28:31], v[16:19], v[8:11]\n\t" \
    "v_mfma_f32_16x16x32_bf16 v[12:15], v[32:35], v[16:19], v[12:15]\n\t" \
    "s_waitcnt lgkmcnt(6)\n\t" \
    "v_mfma_f32_16x16x32_bf16 v[0:3], v[40:43], v[36:39], v[0:3]\n\t" \
    "s_waitcnt lgkmcnt(4)\n\t" \
    "v_mfma_f32_16x16x32_bf16 v[4:7], v[44:47], v[36:39], v[4:7]\n\t" \
    "s_waitcnt lgkmcnt(2)\n\t" \
    "v_mfma_f32_16x16x32_bf16 v[8:11], v[48:51], v[36:39], v[8:11]\n\t" \
    "s_waitcnt lgkmcnt(0)\n\t" \
    "v_mfma_f32_16x16x32_bf16 v[12:15], v[52:55], v[36:39], v[12:15]\n\t" \
    "s_nop 7\n\t" \
    "s_nop 3\n\t"
#define K3_PRE \
    "global_load_dwordx2 v[76:77], %[ro], %[rs] offset:0\n\t" \
    "global_load_dwordx2 v[78:79], %[ro], %[rs] offset:32\n\t" \
    "global_load_dwordx2 v[80:81], %[ro], %[rs] offset:64\n\t" \
    "global_load_dwordx2 v[82:83], %[ro], %[rs] offset:96\n\t"
#define K5_PRE \
    "global_load_dwordx4 v[76:79], %[ro], %[rs] offset:0 sc0 sc1\n\t" \
    "global_load_dwordx4 v[80:83], %[ro], %[rs] offset:1024 sc0 sc1\n\t"
#define G64_CLOB "v16", "v17", "v18", "v19", "v20", "v21", "v22", "v23", "v24", "v25", "v26", "v27", "v28", "v29", "v30", "v31", "v32", "v33", "v34", "v35", "v36", "v37", "v38", "v39", "v40", "v41", "v42", "v43", "v44", "v45", "v46", "v47", "v48", "v49", "v50", "v51", "v52", "v53", "v54", "v55", "v56", "v57", "v58", "v59", "v60", "v61", "v62", "v63", "v64", "v65", "v66", "v67", "v68", "v69", "v70", "v71", "v72", "v73", "v74", "v75", "v76", "v77", "v78", "v79", "v80", "v81", "v82", "v83", "v84", "v85", "v86", "v87", "v88", "v89", "v90", "v91"
#define K4_SP \
    "s_sleep 8\n\t" \
    "v_mbcnt_lo_u32_b32 v64, -1, 0\n\t" \
    "s_mul_i32 m0, %[bi], 12\n\t" \
    "v_mbcnt_hi_u32_b32 v64, -1, v64\n\t" \
    "s_add_u32 m0, m0, %[w]\n\t" \
    "v_lshlrev_b32 v64, 7, v64\n\t" \
    "s_sub_u32 m0, m0, 336\n\t" \
    "s_lshl_b32 m0, m0, 13\n\t" \
    "s_nop 0\n\t" \
    "v_add_u32 v64, m0, v64\n\t" \
    "s_cmp_lt_u32 m0, 0x40000\n\t" \
    "global_load_dword v65, v64, %[sp]\n\t" \
    "v_add_u32 v64, 0x60000, v64\n\t" \
    "global_load_dword v65, v64, %[sp]\n\t" \
    "v_add_u32 v64, 0x60000, v64\n\t" \
    "global_load_dword v65, v64, %[sp]\n\t" \
    "v_add_u32 v64, 0x60000, v64\n\t" \
    "global_load_dword v65, v64, %[sp]\n\t" \
    "v_add_u32 v64, 0x60000, v64\n\t" \
    "s_cbranch_scc0 .Lsq%=\n\t" \
    "global_load_dword v65, v64, %[sp]\n\t" \
    ".Lsq%=:\n\t" \
    "s_waitcnt vmcnt(0)\n\t"
#define K4_PROD \
    "s_barrier\n\t" \
    "s_sleep 8\n\t" \
    "s_add_u32 m0, %[lb], 0\n\t" \
    "s_nop 0\n\t" \
    "global_load_lds_dwordx4 %[vb], %[sg] offset:0\n\t" \
    "s_add_u32 m0, %[lb], 49024\n\t" \
    "s_nop 0\n\t" \
    "global_load_lds_dwordx4 %[vb], %[sg] offset:128\n\t" \
    "s_add_u32 m0, %[lb], 24576\n\t" \
    "s_nop 0\n\t" \
    "global_load_lds_dwordx4 %[vb], %[su] offset:0\n\t" \
    "s_add_u32 m0, %[lb], 73600\n\t" \
    "s_nop 0\n\t" \
    "global_load_lds_dwordx4 %[vb], %[su] offset:128\n\t" \
    "s_add_u32 m0, %[lb], 4096\n\t" \
    "v_add_u32 %[vb], 0x1c000, %[vb]\n\t" \
    "global_load_lds_dwordx4 %[vb], %[sg] offset:0\n\t" \
    "s_add_u32 m0, %[lb], 53120\n\t" \
    "s_nop 0\n\t" \
    "global_load_lds_dwordx4 %[vb], %[sg] offset:128\n\t" \
    "s_add_u32 m0, %[lb], 28672\n\t" \
    "s_nop 0\n\t" \
    "global_load_lds_dwordx4 %[vb], %[su] offset:0\n\t" \
    "s_add_u32 m0, %[lb], 77696\n\t" \
    "s_nop 0\n\t" \
    "global_load_lds_dwordx4 %[vb], %[su] offset:128\n\t" \
    "s_add_u32 m0, %[lb], 8192\n\t" \
    "v_add_u32 %[vb], 0x1c000, %[vb]\n\t" \
    "global_load_lds_dwordx4 %[vb], %[sg] offset:0\n\t" \
    "s_add_u32 m0, %[lb], 57216\n\t" \
    "s_nop 0\n\t" \
    "global_load_lds_dwordx4 %[vb], %[sg] offset:128\n\t" \
    "s_add_u32 m0, %[lb], 32768\n\t" \
    "s_nop 0\n\t" \
    "global_load_lds_dwordx4 %[vb], %[su] offset:0\n\t" \
    "s_add_u32 m0, %[lb], 81792\n\t" \
    "s_nop 0\n\t" \
    "global_load_lds_dwordx4 %[vb], %[su] offset:128\n\t" \
    "s_add_u32 m0, %[lb], 12288\n\t" \
    "v_add_u32 %[vb], 0x1c000, %[vb]\n\t" \
    "global_load_lds_dwordx4 %[vb], %[sg] offset:0\n\t" \
    "s_add_u32 m0, %[lb], 61312\n\t" \
    "s_nop 0\n\t" \
    "global_load_lds_dwordx4 %[vb], %[sg] offset:128\n\t" \
    "s_add_u32 m0, %[lb], 36864\n\t" \
    "s_nop 0\n\t" \
    "global_load_lds_dwordx4 %[vb], %[su] offset:0\n\t" \
    "s_add_u32 m0, %[lb], 85888\n\t" \
    "s_nop 0\n\t" \
    "global_load_lds_dwordx4 %[vb], %[su] offset:128\n\t" \
    "s_add_u32 m0, %[lb], 16384\n\t" \
    "v_add_u32 %[vb], 0x1c000, %[vb]\n\t" \
    "global_load_lds_dwordx4 %[vb], %[sg] offset:0\n\t" \
    "s_add_u32 m0, %[lb], 65408\n\t" \
    "s_nop 0\n\t" \
    "global_load_lds_dwordx4 %[vb], %[sg] offset:128\n\t" \
    "s_add_u32 m0, %[lb], 40960\n\t" \
    "s_nop 0\n\t" \
    "global_load_lds_dwordx4 %[vb], %[su] offset:0\n\t" \
    "s_add_u32 m0, %[lb], 89984\n\t" \
    "s_nop 0\n\t" \
    "global_load_lds_dwordx4 %[vb], %[su] offset:128\n\t" \
    "s_add_u32 m0, %[lb], 20480\n\t" \
    "v_add_u32 %[vb], 0x1c000, %[vb]\n\t" \
    "global_load_lds_dwordx4 %[vb], %[sg] offset:0\n\t" \
    "s_add_u32 m0, %[lb], 69504\n\t" \
    "s_nop 0\n\t" \
    "global_load_lds_dwordx4 %[vb], %[sg] offset:128\n\t" \
    "s_add_u32 m0, %[lb], 45056\n\t" \
    "s_nop 0\n\t" \
    "global_load_lds_dwordx4 %[vb], %[su] offset:0\n\t" \
    "s_add_u32 m0, %[lb], 94080\n\t" \
    "s_nop 0\n\t" \
    "global_load_lds_dwordx4 %[vb], %[su] offset:128\n\t" \
    "s_barrier\n\t" \
    "s_add_u32 m0, %[la], 4096\n\t" \
    "s_nop 0\n\t" \
    "global_load_lds_dwordx4 %[va0], %[sa] offset:-4096 sc0 sc1\n\t" \
    "s_add_u32 m0, %[la], 12288\n\t" \
    "s_nop 0\n\t" \
    "global_load_lds_dwordx4 %[va1], %[sa] offset:-4096 sc0 sc1\n\t" \
    "s_add_u32 m0, %[la], 4096\n\t" \
    "s_nop 0\n\t" \
    "global_load_lds_dwordx4 %[va0], %[sa] offset:-3072 sc0 sc1\n\t" \
    "s_add_u32 m0, %[la], 12288\n\t" \
    "s_nop 0\n\t" \
    "global_load_lds_dwordx4 %[va1], %[sa] offset:-3072 sc0 sc1\n\t" \
    "s_add_u32 m0, %[la], 4096\n\t" \
    "s_nop 0\n\t" \
    "global_load_lds_dwordx4 %[va0], %[sa] offset:-2048 sc0 sc1\n\t" \
    "s_add_u32 m0, %[la], 12288\n\t" \
    "s_nop 0\n\t" \
    "global_load_lds_dwordx4 %[va1], %[sa] offset:-2048 sc0 sc1\n\t" \
    "s_add_u32 m0, %[la], 4096\n\t" \
    "s_nop 0\n\t" \
    "global_load_lds_dwordx4 %[va0], %[sa] offset:-1024 sc0 sc1\n\t" \
    "s_add_u32 m0, %[la], 12288\n\t" \
    "s_nop 0\n\t" \
    "global_load_lds_dwordx4 %[va1], %[sa] offset:-1024 sc0 sc1\n\t" \
    "s_add_u32 m0, %[la], 4096\n\t" \
    "s_nop 0\n\t" \
    "global_load_lds_dwordx4 %[va0], %[sa] offset:0 sc0 sc1\n\t" \
    "s_add_u32 m0, %[la], 12288\n\t" \
    "s_nop 0\n\t" \
    "global_load_lds_dwordx4 %[va1], %[sa] offset:0 sc0 sc1\n\t" \
    "s_add_u32 m0, %[la], 4096\n\t" \
    "s_nop 0\n\t" \
    "global_load_lds_dwordx4 %[va0], %[sa] offset:1024 sc0 sc1\n\t" \
    "s_add_u32 m0, %[la], 12288\n\t" \
    "s_nop 0\n\t" \
    "global_load_lds_dwordx4 %[va1], %[sa] offset:1024 sc0 sc1\n\t" \
    "s_add_u32 m0, %[la], 4096\n\t" \
    "s_nop 0\n\t" \
    "global_load_lds_dwordx4 %[va0], %[sa] offset:2048 sc0 sc1\n\t" \
    "s_add_u32 m0, %[la], 12288\n\t" \
    "s_nop 0\n\t" \
    "global_load_lds_dwordx4 %[va1], %[sa] offset:2048 sc0 sc1\n\t" \
    "s_waitcnt vmcnt(12)\n\t" \
    "s_barrier\n\t" \
    "s_add_u32 m0, %[la], 4096\n\t" \
    "s_nop 0\n\t" \
    "global_load_lds_dwordx4 %[va0], %[sa] offset:3072 sc0 sc1\n\t" \
    "s_add_u32 m0, %[la], 12288\n\t" \
    "s_nop 0\n\t" \
    "global_load_lds_dwordx4 %[va1], %[sa] offset:3072 sc0 sc1\n\t" \
    "s_waitcnt vmcnt(12)\n\t" \
    "s_barrier\n\t" \
    "s_add_u32 m0, %[lb], 0\n\t" \
    "v_add_u32 %[vb], 0x1c000, %[vb]\n\t" \
    "global_load_lds_dwordx4 %[vb], %[sg] offset:0\n\t" \
    "s_add_u32 m0, %[lb], 49024\n\t" \
    "s_nop 0\n\t" \
    "global_load_lds_dwordx4 %[vb], %[sg] offset:128\n\t" \
    "s_waitcnt vmcnt(12)\n\t" \
    "s_barrier\n\t" \
    "s_add_u32 m0, %[lb], 24576\n\t" \
    "s_nop 0\n\t" \
    "global_load_lds_dwordx4 %[vb], %[su] offset:0\n\t" \
    "s_add_u32 m0, %[lb], 73600\n\t" \
    "s_nop 0\n\t" \
    "global_load_lds_dwordx4 %[vb], %[su] offset:128\n\t" \
    "s_waitcnt vmcnt(12)\n\t" \
    "s_barrier\n\t" \
    "s_add_u32 m0, %[la], 4096\n\t" \
    "v_add_u32 %[va0], 8192, %[va0]\n\t" \
    "global_load_lds_dwordx4 %[va0], %[sa] offset:-4096 sc0 sc1\n\t" \
    "s_add_u32 m0, %[la], 12288\n\t" \
    "v_add_u32 %[va1], 8192, %[va1]\n\t" \
    "global_load_lds_dwordx4 %[va1], %[sa] offset:-4096 sc0 sc1\n\t" \
    "s_add_u32 m0, %[lb], 4096\n\t" \
    "v_add_u32 %[vb], 0x1c000, %[vb]\n\t" \
    "global_load_lds_dwordx4 %[vb], %[sg] offset:0\n\t" \
    "s_add_u32 m0, %[lb], 53120\n\t" \
    "s_nop 0\n\t" \
    "global_load_lds_dwordx4 %[vb], %[sg] offset:128\n\t" \
    "s_waitcnt vmcnt(12)\n\t" \
    "s_barrier\n\t" \
    "s_add_u32 m0, %[lb], 28672\n\t" \
    "s_nop 0\n\t" \
    "global_load_lds_dwordx4 %[vb], %[su] offset:0\n\t" \
    "s_add_u32 m0, %[lb], 77696\n\t" \
    "s_nop 0\n\t" \
    "global_load_lds_dwordx4 %[vb], %[su] offset:128\n\t" \
    "s_add_u32 m0, %[la], 4096\n\t" \
    "s_nop 0\n\t" \
    "global_load_lds_dwordx4 %[va0], %[sa] offset:-3072 sc0 sc1\n\t" \
    "s_add_u32 m0, %[la], 12288\n\t" \
    "s_nop 0\n\t" \
    "global_load_lds_dwordx4 %[va1], %[sa] offset:-3072 sc0 sc1\n\t" \
    "s_add_u32 m0, %[lb], 8192\n\t" \
    "v_add_u32 %[vb], 0x1c000, %[vb]\n\t" \
    "global_load_lds_dwordx4 %[vb], %[sg] offset:0\n\t" \
    "s_add_u32 m0, %[lb], 57216\n\t" \
    "s_nop 0\n\t" \
    "global_load_lds_dwordx4 %[vb], %[sg] offset:128\n\t" \
    "s_add_u32 m0, %[lb], 32768\n\t" \
    "s_nop 0\n\t" \
    "global_load_lds_dwordx4 %[vb], %[su] offset:0\n\t" \
    "s_add_u32 m0, %[lb], 81792\n\t" \
    "s_nop 0\n\t" \
    "global_load_lds_dwordx4 %[vb], %[su] offset:128\n\t" \
    "s_add_u32 m0, %[la], 4096\n\t" \
    "s_nop 0\n\t" \
    "global_load_lds_dwordx4 %[va0], %[sa] offset:-2048 sc0 sc1\n\t" \
    "s_add_u32 m0, %[la], 12288\n\t" \
    "s_nop 0\n\t" \
    "global_load_lds_dwordx4 %[va1], %[sa] offset:-2048 sc0 sc1\n\t" \
    "global_load_dwordx2 v[64:65], %[vh], %[sh] offset:0 sc0 sc1\n\t" \
    "global_load_dwordx2 v[66:67], %[vh], %[sh] offset:128 sc0 sc1\n\t" \
    "global_load_dwordx2 v[68:69], %[vh], %[sh] offset:256 sc0 sc1\n\t" \
    "global_load_dwordx2 v[70:71], %[vh], %[sh] offset:384 sc0 sc1\n\t" \
    "s_waitcnt vmcnt(12)\n\t" \
    "s_barrier\n\t" \
    "global_load_dword v72, %[vc], %[sh] sc0 sc1\n\t" \
    "s_add_u32 m0, %[lb], 12288\n\t" \
    "v_add_u32 %[vb], 0x1c000, %[vb]\n\t" \
    "global_load_lds_dwordx4 %[vb], %[sg] offset:0\n\t" \
    "s_add_u32 m0, %[lb], 61312\n\t" \
    "s_nop 0\n\t" \
    "global_load_lds_dwordx4 %[vb], %[sg] offset:128\n\t" \
    "s_add_u32 m0, %[lb], 36864\n\t" \
    "s_nop 0\n\t" \
    "global_load_lds_dwordx4 %[vb], %[su] offset:0\n\t" \
    "s_add_u32 m0, %[lb], 85888\n\t" \
    "s_nop 0\n\t" \
    "global_load_lds_dwordx4 %[vb], %[su] offset:128\n\t" \
    "s_add_u32 m0, %[la], 4096\n\t" \
    "s_nop 0\n\t" \
    "global_load_lds_dwordx4 %[va0], %[sa] offset:-1024 sc0 sc1\n\t" \
    "s_add_u32 m0, %[la], 12288\n\t" \
    "s_nop 0\n\t" \
    "global_load_lds_dwordx4 %[va1], %[sa] offset:-1024 sc0 sc1\n\t" \
    "s_add_u32 m0, %[lb], 16384\n\t" \
    "v_add_u32 %[vb], 0x1c000, %[vb]\n\t" \
    "global_load_lds_dwordx4 %[vb], %[sg] offset:0\n\t" \
    "s_add_u32 m0, %[lb], 65408\n\t" \
    "s_nop 0\n\t" \
    "global_load_lds_dwordx4 %[vb], %[sg] offset:128\n\t" \
    "s_add_u32 m0, %[lb], 40960\n\t" \
    "s_nop 0\n\t" \
    "global_load_lds_dwordx4 %[vb], %[su] offset:0\n\t" \
    "s_add_u32 m0, %[lb], 89984\n\t" \
    "s_nop 0\n\t" \
    "global_load_lds_dwordx4 %[vb], %[su] offset:128\n\t" \
    "s_add_u32 m0, %[la], 4096\n\t" \
    "s_nop 0\n\t" \
    "global_load_lds_dwordx4 %[va0], %[sa] offset:0 sc0 sc1\n\t" \
    "s_add_u32 m0, %[la], 12288\n\t" \
    "s_nop 0\n\t" \
    "global_load_lds_dwordx4 %[va1], %[sa] offset:0 sc0 sc1\n\t" \
    "s_add_u32 m0, %[lb], 20480\n\t" \
    "v_add_u32 %[vb], 0x1c000, %[vb]\n\t" \
    "global_load_lds_dwordx4 %[vb], %[sg] offset:0\n\t" \
    "s_add_u32 m0, %[lb], 69504\n\t" \
    "s_nop 0\n\t" \
    "global_load_lds_dwordx4 %[vb], %[sg] offset:128\n\t" \
    "s_add_u32 m0, %[lb], 45056\n\t" \
    "s_nop 0\n\t" \
    "global_load_lds_dwordx4 %[vb], %[su] offset:0\n\t" \
    "s_add_u32 m0, %[lb], 94080\n\t" \
    "s_nop 0\n\t" \
    "global_load_lds_dwordx4 %[vb], %[su] offset:128\n\t" \
    "s_waitcnt vmcnt(12)\n\t" \
    "s_barrier\n\t" \
    "s_add_u32 m0, %[la], 4096\n\t" \
    "s_nop 0\n\t" \
    "global_load_lds_dwordx4 %[va0], %[sa] offset:1024 sc0 sc1\n\t" \
    "s_add_u32 m0, %[la], 12288\n\t" \
    "s_nop 0\n\t" \
    "global_load_lds_dwordx4 %[va1], %[sa] offset:1024 sc0 sc1\n\t" \
    "s_add_u32 m0, %[lb], 0\n\t" \
    "v_add_u32 %[vb], 0x1c000, %[vb]\n\t" \
    "global_load_lds_dwordx4 %[vb], %[sg] offset:0\n\t" \
    "s_add_u32 m0, %[lb], 49024\n\t" \
    "s_nop 0\n\t" \
    "global_load_lds_dwordx4 %[vb], %[sg] offset:128\n\t" \
    "s_add_u32 m0, %[lb], 24576\n\t" \
    "s_nop 0\n\t" \
    "global_load_lds_dwordx4 %[vb], %[su] offset:0\n\t" \
    "s_add_u32 m0, %[lb], 73600\n\t" \
    "s_nop 0\n\t" \
    "global_load_lds_dwordx4 %[vb], %[su] offset:128\n\t" \
    "s_add_u32 m0, %[la], 4096\n\t" \
    "s_nop 0\n\t" \
    "global_load_lds_dwordx4 %[va0], %[sa] offset:2048 sc0 sc1\n\t" \
    "s_add_u32 m0, %[la], 12288\n\t" \
    "s_nop 0\n\t" \
    "global_load_lds_dwordx4 %[va1], %[sa] offset:2048 sc0 sc1\n\t" \
    "s_add_u32 m0, %[lb], 4096\n\t" \
    "v_add_u32 %[vb], 0x1c000, %[vb]\n\t" \
    "global_load_lds_dwordx4 %[vb], %[sg] offset:0\n\t" \
    "s_add_u32 m0, %[lb], 53120\n\t" \
    "s_nop 0\n\t" \
    "global_load_lds_dwordx4 %[vb], %[sg] offset:128\n\t" \
    "s_add_u32 m0, %[lb], 28672\n\t" \
    "s_nop 0\n\t" \
    "global_load_lds_dwordx4 %[vb], %[su] offset:0\n\t" \
    "s_add_u32 m0, %[lb], 77696\n\t" \
    "s_nop 0\n\t" \
    "global_load_lds_dwordx4 %[vb], %[su] offset:128\n\t" \
    "s_waitcnt vmcnt(12)\n\t" \
    "s_barrier\n\t" \
    "s_add_u32 m0, %[la], 4096\n\t" \
    "s_nop 0\n\t" \
    "global_load_lds_dwordx4 %[va0], %[sa] offset:3072 sc0 sc1\n\t" \
    "s_add_u32 m0, %[la], 12288\n\t" \
    "s_nop 0\n\t" \
    "global_load_lds_dwordx4 %[va1], %[sa] offset:3072 sc0 sc1\n\t" \
    "s_add_u32 m0, %[lb], 8192\n\t" \
    "v_add_u32 %[vb], 0x1c000, %[vb]\n\t" \
    "global_load_lds_dwordx4 %[vb], %[sg] offset:0\n\t" \
    "s_add_u32 m0, %[lb], 57216\n\t" \
    "s_nop 0\n\t" \
    "global_load_lds_dwordx4 %[vb], %[sg] offset:128\n\t" \
    "s_add_u32 m0, %[lb], 32768\n\t" \
    "s_nop 0\n\t" \
    "global_load_lds_dwordx4 %[vb], %[su] offset:0\n\t" \
    "s_add_u32 m0, %[lb], 81792\n\t" \
    "s_nop 0\n\t" \
    "global_load_lds_dwordx4 %[vb], %[su] offset:128\n\t" \
    "s_add_u32 m0, %[lb], 12288\n\t" \
    "v_add_u32 %[vb], 0x1c000, %[vb]\n\t" \
    "global_load_lds_dwordx4 %[vb], %[sg] offset:0\n\t" \
    "s_add_u32 m0, %[lb], 61312\n\t" \
    "s_nop 0\n\t" \
    "global_load_lds_dwordx4 %[vb], %[sg] offset:128\n\t" \
    "s_add_u32 m0, %[lb], 36864\n\t" \
    "s_nop 0\n\t" \
    "global_load_lds_dwordx4 %[vb], %[su] offset:0\n\t" \
    "s_add_u32 m0, %[lb], 85888\n\t" \
    "s_nop 0\n\t" \
    "global_load_lds_dwordx4 %[vb], %[su] offset:128\n\t" \
    "v_pk_add_f32 v[64:65], v[64:65], v[66:67]\n\t" \
    "v_pk_add_f32 v[68:69], v[68:69], v[70:71]\n\t" \
    "ds_write_b32 %[wc], v72\n\t" \
    "v_pk_add_f32 v[64:65], v[64:65], v[68:69]\n\t" \
    "s_nop 1\n\t" \
    "v_add_f32_dpp v64, v64, v64 quad_perm:[1,0,3,2] row_mask:0xf bank_mask:0xf\n\t" \
    "v_add_f32_dpp v65, v65, v65 quad_perm:[1,0,3,2] row_mask:0xf bank_mask:0xf\n\t" \
    "v_mul_f32 v64, 0xbb000000, v64\n\t" \
    "v_mul_f32 v65, 0x3b000000, v65\n\t" \
    "v_fma_f32 v67, -v64, v64, v65\n\t" \
    "v_max_f32 v67, 0, v67\n\t" \
    "v_add_f32 v67, 0x3727c5ac, v67\n\t" \
    "v_rsq_f32 v65, v67\n\t" \
    "s_nop 0\n\t" \
    "v_mul_f32 v65, 0xbfb8aa3b, v65\n\t" \
    "v_mul_f32 v66, v65, v67\n\t" \
    "ds_write_b128 %[wh], v[64:67]\n\t" \
    "s_waitcnt lgkmcnt(0)\n\t" \
    "s_waitcnt vmcnt(10)\n\t" \
    "s_barrier\n\t" \
    "s_waitcnt vmcnt(0)\n\t" \
    "s_barrier\n\t"
#define K3_PROD \
    "s_waitcnt vmcnt(0)\n\t" \
    "s_barrier\n\t" \
    "s_sleep 8\n\t" \
    "s_add_u32 m0, %[lb], 0\n\t" \
    "s_nop 0\n\t" \
    "global_load_lds_dwordx4 %[vb], %[sb] offset:0\n\t" \
    "s_add_u32 m0, %[lb], 4096\n\t" \
    "v_add_u32 %[vb], 0x8000, %[vb]\n\t" \
    "global_load_lds_dwordx4 %[vb], %[sb] offset:0\n\t" \
    "s_add_u32 m0, %[lb], 8192\n\t" \
    "v_add_u32 %[vb], 0x8000, %[vb]\n\t" \
    "global_load_lds_dwordx4 %[vb], %[sb] offset:0\n\t" \
    "s_add_u32 m0, %[lb], 12288\n\t" \
    "v_add_u32 %[vb], 0x8000, %[vb]\n\t" \
    "global_load_lds_dwordx4 %[vb], %[sb] offset:0\n\t" \
    "s_add_u32 m0, %[lb], 16384\n\t" \
    "v_add_u32 %[vb], 0x8000, %[vb]\n\t" \
    "global_load_lds_dwordx4 %[vb], %[sb] offset:0\n\t" \
    "s_add_u32 m0, %[lb], 20480\n\t" \
    "v_add_u32 %[vb], 0x8000, %[vb]\n\t" \
    "global_load_lds_dwordx4 %[vb], %[sb] offset:0\n\t" \
    "s_add_u32 m0, %[lb], 24576\n\t" \
    "v_add_u32 %[vb], 0x8000, %[vb]\n\t" \
    "global_load_lds_dwordx4 %[vb], %[sb] offset:0\n\t" \
    "s_add_u32 m0, %[lb], 28672\n\t" \
    "v_add_u32 %[vb], 0x8000, %[vb]\n\t" \
    "global_load_lds_dwordx4 %[vb], %[sb] offset:0\n\t" \
    "s_add_u32 m0, %[lb], 32768\n\t" \
    "v_add_u32 %[vb], 0x8000, %[vb]\n\t" \
    "global_load_lds_dwordx4 %[vb], %[sb] offset:0\n\t" \
    "s_add_u32 m0, %[lb], 36864\n\t" \
    "v_add_u32 %[vb], 0x8000, %[vb]\n\t" \
    "global_load_lds_dwordx4 %[vb], %[sb] offset:0\n\t" \
    "s_add_u32 m0, %[lb], 57344\n\t" \
    "v_add_u32 %[vb], 0x8000, %[vb]\n\t" \
    "global_load_lds_dwordx4 %[vb], %[sb] offset:0\n\t" \
    "s_add_u32 m0, %[lb], 61440\n\t" \
    "v_add_u32 %[vb], 0x8000, %[vb]\n\t" \
    "global_load_lds_dwordx4 %[vb], %[sb] offset:0\n\t" \
    "s_add_u32 m0, %[lb], 49152\n\t" \
    "v_add_u32 %[vb], 0x8000, %[vb]\n\t" \
    "global_load_lds_dwordx4 %[vb], %[sb] offset:0\n\t" \
    "s_add_u32 m0, %[lb], 53248\n\t" \
    "v_add_u32 %[vb], 0x8000, %[vb]\n\t" \
    "global_load_lds_dwordx4 %[vb], %[sb] offset:0\n\t" \
    "s_add_u32 m0, %[lb], 40960\n\t" \
    "v_add_u32 %[vb], 0x8000, %[vb]\n\t" \
    "global_load_lds_dwordx4 %[vb], %[sb] offset:0\n\t" \
    "s_add_u32 m0, %[lb], 45056\n\t" \
    "v_add_u32 %[vb], 0x8000, %[vb]\n\t" \
    "global_load_lds_dwordx4 %[vb], %[sb] offset:0\n\t" \
    "s_barrier\n\t" \
    "s_add_u32 m0, %[la], 4096\n\t" \
    "s_nop 0\n\t" \
    "global_load_lds_dwordx4 %[va], %[sa] offset:-4096 sc0 sc1\n\t" \
    "s_add_u32 m0, %[la], 4096\n\t" \
    "s_nop 0\n\t" \
    "global_load_lds_dwordx4 %[va], %[sa] offset:-3072 sc0 sc1\n\t" \
    "s_add_u32 m0, %[la], 4096\n\t" \
    "s_nop 0\n\t" \
    "global_load_lds_dwordx4 %[va], %[sa] offset:-2048 sc0 sc1\n\t" \
    "s_add_u32 m0, %[la], 4096\n\t" \
    "s_nop 0\n\t" \
    "global_load_lds_dwordx4 %[va], %[sa] offset:-1024 sc0 sc1\n\t" \
    "s_add_u32 m0, %[la], 4096\n\t" \
    "s_nop 0\n\t" \
    "global_load_lds_dwordx4 %[va], %[sa] offset:0 sc0 sc1\n\t" \
    "s_add_u32 m0, %[la], 4096\n\t" \
    "s_nop 0\n\t" \
    "global_load_lds_dwordx4 %[va], %[sa] offset:1024 sc0 sc1\n\t" \
    "s_add_u32 m0, %[la], 4096\n\t" \
    "s_nop 0\n\t" \
    "global_load_lds_dwordx4 %[va], %[sa] offset:2048 sc0 sc1\n\t" \
    "s_add_u32 m0, %[la], 4096\n\t" \
    "s_nop 0\n\t" \
    "global_load_lds_dwordx4 %[va], %[sa] offset:3072 sc0 sc1\n\t" \
    "s_add_u32 m0, %[la], 12288\n\t" \
    "v_add_u32 %[va], 8192, %[va]\n\t" \
    "global_load_lds_dwordx4 %[va], %[sa] offset:-4096 sc0 sc1\n\t" \
    "s_add_u32 m0, %[la], 12288\n\t" \
    "s_nop 0\n\t" \
    "global_load_lds_dwordx4 %[va], %[sa] offset:-3072 sc0 sc1\n\t" \
    "s_add_u32 m0, %[la], 12288\n\t" \
    "s_nop 0\n\t" \
    "global_load_lds_dwordx4 %[va], %[sa] offset:-2048 sc0 sc1\n\t" \
    "s_add_u32 m0, %[la], 12288\n\t" \
    "s_nop 0\n\t" \
    "global_load_lds_dwordx4 %[va], %[sa] offset:-1024 sc0 sc1\n\t" \
    "s_add_u32 m0, %[la], 12288\n\t" \
    "s_nop 0\n\t" \
    "global_load_lds_dwordx4 %[va], %[sa] offset:0 sc0 sc1\n\t" \
    "s_add_u32 m0, %[la], 12288\n\t" \
    "s_nop 0\n\t" \
    "global_load_lds_dwordx4 %[va], %[sa] offset:1024 sc0 sc1\n\t" \
    "s_add_u32 m0, %[la], 12288\n\t" \
    "s_nop 0\n\t" \
    "global_load_lds_dwordx4 %[va], %[sa] offset:2048 sc0 sc1\n\t" \
    "s_add_u32 m0, %[la], 12288\n\t" \
    "s_nop 0\n\t" \
    "global_load_lds_dwordx4 %[va], %[sa] offset:3072 sc0 sc1\n\t" \
    "s_waitcnt vmcnt(15)\n\t" \
    "s_barrier\n\t" \
    "s_waitcnt vmcnt(14)\n\t" \
    "s_barrier\n\t" \
    "s_waitcnt vmcnt(12)\n\t" \
    "s_barrier\n\t" \
    "s_waitcnt vmcnt(10)\n\t" \
    "s_barrier\n\t" \
    "s_waitcnt vmcnt(8)\n\t" \
    "s_barrier\n\t" \
    "s_waitcnt vmcnt(6)\n\t" \
    "s_barrier\n\t" \
    "s_waitcnt vmcnt(4)\n\t" \
    "s_barrier\n\t" \
    "s_waitcnt vmcnt(2)\n\t" \
    "s_barrier\n\t" \
    "s_waitcnt vmcnt(1)\n\t" \
    "s_barrier\n\t" \
    "s_waitcnt vmcnt(0)\n\t" \
    "s_barrier\n\t"
#define K5_PROD \
    "s_add_u32 m0, %[lb], 0\n\t" \
    "s_nop 0\n\t" \
    "global_load_lds_dwordx4 %[vb], %[sb] offset:0\n\t" \
    "s_add_u32 m0, %[lb], 4096\n\t" \
    "v_add_u32 %[vb], 0x8000, %[vb]\n\t" \
    "global_load_lds_dwordx4 %[vb], %[sb] offset:0\n\t" \
    "s_add_u32 m0, %[lb], 16384\n\t" \
    "v_add_u32 %[vb], 0x8000, %[vb]\n\t" \
    "global_load_lds_dwordx4 %[vb], %[sb] offset:0\n\t" \
    "s_add_u32 m0, %[lb], 20480\n\t" \
    "v_add_u32 %[vb], 0x8000, %[vb]\n\t" \
    "global_load_lds_dwordx4 %[vb], %[sb] offset:0\n\t" \
    "s_add_u32 m0, %[lb], 24576\n\t" \
    "v_add_u32 %[vb], 0x8000, %[vb]\n\t" \
    "global_load_lds_dwordx4 %[vb], %[sb] offset:0\n\t" \
    "s_add_u32 m0, %[lb], 28672\n\t" \
    "v_add_u32 %[vb], 0x8000, %[vb]\n\t" \
    "global_load_lds_dwordx4 %[vb], %[sb] offset:0\n\t" \
    "s_add_u32 m0, %[lb], 40960\n\t" \
    "v_add_u32 %[vb], 0x8000, %[vb]\n\t" \
    "global_load_lds_dwordx4 %[vb], %[sb] offset:0\n\t" \
    "s_add_u32 m0, %[lb], 45056\n\t" \
    "v_add_u32 %[vb], 0x8000, %[vb]\n\t" \
    "global_load_lds_dwordx4 %[vb], %[sb] offset:0\n\t" \
    "s_add_u32 m0, %[lb], 49152\n\t" \
    "v_add_u32 %[vb], 0x8000, %[vb]\n\t" \
    "global_load_lds_dwordx4 %[vb], %[sb] offset:0\n\t" \
    "s_add_u32 m0, %[lb], 53248\n\t" \
    "v_add_u32 %[vb], 0x8000, %[vb]\n\t" \
    "global_load_lds_dwordx4 %[vb], %[sb] offset:0\n\t" \
    "s_add_u32 m0, %[lb], 65536\n\t" \
    "v_add_u32 %[vb], 0x8000, %[vb]\n\t" \
    "global_load_lds_dwordx4 %[vb], %[sb] offset:0\n\t" \
    "s_add_u32 m0, %[lb], 69632\n\t" \
    "v_add_u32 %[vb], 0x8000, %[vb]\n\t" \
    "global_load_lds_dwordx4 %[vb], %[sb] offset:0\n\t" \
    "s_add_u32 m0, %[lb], 73728\n\t" \
    "v_add_u32 %[vb], 0x8000, %[vb]\n\t" \
    "global_load_lds_dwordx4 %[vb], %[sb] offset:0\n\t" \
    "s_add_u32 m0, %[lb], 77824\n\t" \
    "v_add_u32 %[vb], 0x8000, %[vb]\n\t" \
    "global_load_lds_dwordx4 %[vb], %[sb] offset:0\n\t" \
    "s_add_u32 m0, %[lb], 90112\n\t" \
    "v_add_u32 %[vb], 0x8000, %[vb]\n\t" \
    "global_load_lds_dwordx4 %[vb], %[sb] offset:0\n\t" \
    "s_add_u32 m0, %[lb], 94208\n\t" \
    "v_add_u32 %[vb], 0x8000, %[vb]\n\t" \
    "global_load_lds_dwordx4 %[vb], %[sb] offset:0\n\t" \
    "s_barrier\n\t" \
    "s_sleep 8\n\t" \
    "s_add_u32 m0, %[lb], 8192\n\t" \
    "v_add_u32 %[vb], 0x8000, %[vb]\n\t" \
    "global_load_lds_dwordx4 %[vb], %[sb] offset:0\n\t" \
    "s_add_u32 m0, %[lb], 12288\n\t" \
    "v_add_u32 %[vb], 0x8000, %[vb]\n\t" \
    "global_load_lds_dwordx4 %[vb], %[sb] offset:0\n\t" \
    "s_add_u32 m0, %[lb], 32768\n\t" \
    "v_add_u32 %[vb], 0x8000, %[vb]\n\t" \
    "global_load_lds_dwordx4 %[vb], %[sb] offset:0\n\t" \
    "s_add_u32 m0, %[lb], 36864\n\t" \
    "v_add_u32 %[vb], 0x8000, %[vb]\n\t" \
    "global_load_lds_dwordx4 %[vb], %[sb] offset:0\n\t" \
    "s_add_u32 m0, %[lb], 57344\n\t" \
    "v_add_u32 %[vb], 0x8000, %[vb]\n\t" \
    "global_load_lds_dwordx4 %[vb], %[sb] offset:0\n\t" \
    "s_add_u32 m0, %[lb], 61440\n\t" \
    "v_add_u32 %[vb], 0x8000, %[vb]\n\t" \
    "global_load_lds_dwordx4 %[vb], %[sb] offset:0\n\t" \
    "s_add_u32 m0, %[lb], 81920\n\t" \
    "v_add_u32 %[vb], 0x8000, %[vb]\n\t" \
    "global_load_lds_dwordx4 %[vb], %[sb] offset:0\n\t" \
    "s_add_u32 m0, %[lb], 86016\n\t" \
    "v_add_u32 %[vb], 0x8000, %[vb]\n\t" \
    "global_load_lds_dwordx4 %[vb], %[sb] offset:0\n\t" \
    "s_barrier\n\t" \
    "s_add_u32 m0, %[la], 4096\n\t" \
    "s_nop 0\n\t" \
    "global_load_lds_dwordx4 %[va], %[sa] offset:-4096 sc0 sc1\n\t" \
    "s_add_u32 m0, %[la], 4096\n\t" \
    "s_nop 0\n\t" \
    "global_load_lds_dwordx4 %[va], %[sa] offset:-3072 sc0 sc1\n\t" \
    "s_add_u32 m0, %[la], 4096\n\t" \
    "s_nop 0\n\t" \
    "global_load_lds_dwordx4 %[va], %[sa] offset:-2048 sc0 sc1\n\t" \
    "s_add_u32 m0, %[la], 4096\n\t" \
    "s_nop 0\n\t" \
    "global_load_lds_dwordx4 %[va], %[sa] offset:-1024 sc0 sc1\n\t" \
    "s_add_u32 m0, %[la], 4096\n\t" \
    "s_nop 0\n\t" \
    "global_load_lds_dwordx4 %[va], %[sa] offset:0 sc0 sc1\n\t" \
    "s_add_u32 m0, %[la], 4096\n\t" \
    "s_nop 0\n\t" \
    "global_load_lds_dwordx4 %[va], %[sa] offset:1024 sc0 sc1\n\t" \
    "s_add_u32 m0, %[la], 4096\n\t" \
    "s_nop 0\n\t" \
    "global_load_lds_dwordx4 %[va], %[sa] offset:2048 sc0 sc1\n\t" \
    "s_add_u32 m0, %[la], 4096\n\t" \
    "s_nop 0\n\t" \
    "global_load_lds_dwordx4 %[va], %[sa] offset:3072 sc0 sc1\n\t" \
    "s_add_u32 m0, %[la], 12288\n\t" \
    "v_add_u32 %[va], 8192, %[va]\n\t" \
    "global_load_lds_dwordx4 %[va], %[sa] offset:-4096 sc0 sc1\n\t" \
    "s_add_u32 m0, %[la], 12288\n\t" \
    "s_nop 0\n\t" \
    "global_load_lds_dwordx4 %[va], %[sa] offset:-3072 sc0 sc1\n\t" \
    "s_add_u32 m0, %[la], 12288\n\t" \
    "s_nop 0\n\t" \
    "global_load_lds_dwordx4 %[va], %[sa] offset:-2048 sc0 sc1\n\t" \
    "s_add_u32 m0, %[la], 12288\n\t" \
    "s_nop 0\n\t" \
    "global_load_lds_dwordx4 %[va], %[sa] offset:-1024 sc0 sc1\n\t" \
    "s_add_u32 m0, %[la], 12288\n\t" \
    "s_nop 0\n\t" \
    "global_load_lds_dwordx4 %[va], %[sa] offset:0 sc0 sc1\n\t" \
    "s_waitcnt vmcnt(12)\n\t" \
    "s_barrier\n\t" \
    "s_add_u32 m0, %[la], 12288\n\t" \
    "s_nop 0\n\t" \
    "global_load_lds_dwordx4 %[va], %[sa] offset:1024 sc0 sc1\n\t" \
    "s_waitcnt vmcnt(12)\n\t" \
    "s_barrier\n\t" \
    "s_add_u32 m0, %[la], 12288\n\t" \
    "s_nop 0\n\t" \
    "global_load_lds_dwordx4 %[va], %[sa] offset:2048 sc0 sc1\n\t" \
    "s_add_u32 m0, %[la], 12288\n\t" \
    "s_nop 0\n\t" \
    "global_load_lds_dwordx4 %[va], %[sa] offset:3072 sc0 sc1\n\t" \
    "s_waitcnt vmcnt(12)\n\t" \
    "s_barrier\n\t" \
    "s_add_u32 m0, %[la], 4096\n\t" \
    "v_add_u32 %[va], 8192, %[va]\n\t" \
    "global_load_lds_dwordx4 %[va], %[sa] offset:-4096 sc0 sc1\n\t" \
    "s_add_u32 m0, %[lb], 0\n\t" \
    "v_add_u32 %[vb], 0x8000, %[vb]\n\t" \
    "global_load_lds_dwordx4 %[vb], %[sb] offset:0\n\t" \
    "s_waitcnt vmcnt(12)\n\t" \
    "s_barrier\n\t" \
    "s_add_u32 m0, %[la], 4096\n\t" \
    "s_nop 0\n\t" \
    "global_load_lds_dwordx4 %[va], %[sa] offset:-3072 sc0 sc1\n\t" \
    "s_add_u32 m0, %[lb], 4096\n\t" \
    "v_add_u32 %[vb], 0x8000, %[vb]\n\t" \
    "global_load_lds_dwordx4 %[vb], %[sb] offset:0\n\t" \
    "s_waitcnt vmcnt(12)\n\t" \
    "s_barrier\n\t" \
    "s_add_u32 m0, %[la], 4096\n\t" \
    "s_nop 0\n\t" \
    "global_load_lds_dwordx4 %[va], %[sa] offset:-2048 sc0 sc1\n\t" \
    "s_add_u32 m0, %[lb], 16384\n\t" \
    "v_add_u32 %[vb], 0x8000, %[vb]\n\t" \
    "global_load_lds_dwordx4 %[vb], %[sb] offset:0\n\t" \
    "s_waitcnt vmcnt(12)\n\t" \
    "s_barrier\n\t" \
    "s_add_u32 m0, %[la], 4096\n\t" \
    "s_nop 0\n\t" \
    "global_load_lds_dwordx4 %[va], %[sa] offset:-1024 sc0 sc1\n\t" \
    "s_add_u32 m0, %[lb], 20480\n\t" \
    "v_add_u32 %[vb], 0x8000, %[vb]\n\t" \
    "global_load_lds_dwordx4 %[vb], %[sb] offset:0\n\t" \
    "s_waitcnt vmcnt(12)\n\t" \
    "s_barrier\n\t" \
    "s_add_u32 m0, %[la], 4096\n\t" \
    "s_nop 0\n\t" \
    "global_load_lds_dwordx4 %[va], %[sa] offset:0 sc0 sc1\n\t" \
    "s_add_u32 m0, %[lb], 24576\n\t" \
    "v_add_u32 %[vb], 0x8000, %[vb]\n\t" \
    "global_load_lds_dwordx4 %[vb], %[sb] offset:0\n\t" \
    "s_waitcnt vmcnt(12)\n\t" \
    "s_barrier\n\t" \
    "s_add_u32 m0, %[la], 4096\n\t" \
    "s_nop 0\n\t" \
    "global_load_lds_dwordx4 %[va], %[sa] offset:1024 sc0 sc1\n\t" \
    "s_add_u32 m0, %[lb], 28672\n\t" \
    "v_add_u32 %[vb], 0x8000, %[vb]\n\t" \
    "global_load_lds_dwordx4 %[vb], %[sb] offset:0\n\t" \
    "s_waitcnt vmcnt(12)\n\t" \
    "s_barrier\n\t" \
    "s_add_u32 m0, %[la], 4096\n\t" \
    "s_nop 0\n\t" \
    "global_load_lds_dwordx4 %[va], %[sa] offset:2048 sc0 sc1\n\t" \
    "s_add_u32 m0, %[lb], 40960\n\t" \
    "v_add_u32 %[vb], 0x8000, %[vb]\n\t" \
    "global_load_lds_dwordx4 %[vb], %[sb] offset:0\n\t" \
    "s_add_u32 m0, %[la], 4096\n\t" \
    "s_nop 0\n\t" \
    "global_load_lds_dwordx4 %[va], %[sa] offset:3072 sc0 sc1\n\t" \
    "s_waitcnt vmcnt(12)\n\t" \
    "s_barrier\n\t" \
    "s_add_u32 m0, %[lb], 45056\n\t" \
    "v_add_u32 %[vb], 0x8000, %[vb]\n\t" \
    "global_load_lds_dwordx4 %[vb], %[sb] offset:0\n\t" \
    "s_add_u32 m0, %[la], 12288\n\t" \
    "v_add_u32 %[va], 8192, %[va]\n\t" \
    "global_load_lds_dwordx4 %[va], %[sa] offset:-4096 sc0 sc1\n\t" \
    "s_add_u32 m0, %[lb], 49152\n\t" \
    "v_add_u32 %[vb], 0x8000, %[vb]\n\t" \
    "global_load_lds_dwordx4 %[vb], %[sb] offset:0\n\t" \
    "s_add_u32 m0, %[la], 12288\n\t" \
    "s_nop 0\n\t" \
    "global_load_lds_dwordx4 %[va], %[sa] offset:-3072 sc0 sc1\n\t" \
    "s_waitcnt vmcnt(12)\n\t" \
    "s_barrier\n\t" \
    "s_add_u32 m0, %[lb], 53248\n\t" \
    "v_add_u32 %[vb], 0x8000, %[vb]\n\t" \
    "global_load_lds_dwordx4 %[vb], %[sb] offset:0\n\t" \
    "s_add_u32 m0, %[la], 12288\n\t" \
    "s_nop 0\n\t" \
    "global_load_lds_dwordx4 %[va], %[sa] offset:-2048 sc0 sc1\n\t" \
    "s_add_u32 m0, %[lb], 65536\n\t" \
    "v_add_u32 %[vb], 0x8000, %[vb]\n\t" \
    "global_load_lds_dwordx4 %[vb], %[sb] offset:0\n\t" \
    "s_add_u32 m0, %[la], 12288\n\t" \
    "s_nop 0\n\t" \
    "global_load_lds_dwordx4 %[va], %[sa] offset:-1024 sc0 sc1\n\t" \
    "s_waitcnt vmcnt(12)\n\t" \
    "s_barrier\n\t" \
    "s_add_u32 m0, %[lb], 69632\n\t" \
    "v_add_u32 %[vb], 0x8000, %[vb]\n\t" \
    "global_load_lds_dwordx4 %[vb], %[sb] offset:0\n\t" \
    "s_add_u32 m0, %[la], 12288\n\t" \
    "s_nop 0\n\t" \
    "global_load_lds_dwordx4 %[va], %[sa] offset:0 sc0 sc1\n\t" \
    "s_add_u32 m0, %[lb], 73728\n\t" \
    "v_add_u32 %[vb], 0x8000, %[vb]\n\t" \
    "global_load_lds_dwordx4 %[vb], %[sb] offset:0\n\t" \
    "s_add_u32 m0, %[la], 12288\n\t" \
    "s_nop 0\n\t" \
    "global_load_lds_dwordx4 %[va], %[sa] offset:1024 sc0 sc1\n\t" \
    "s_waitcnt vmcnt(12)\n\t" \
    "s_barrier\n\t" \
    "s_add_u32 m0, %[lb], 77824\n\t" \
    "v_add_u32 %[vb], 0x8000, %[vb]\n\t" \
    "global_load_lds_dwordx4 %[vb], %[sb] offset:0\n\t" \
    "s_add_u32 m0, %[la], 12288\n\t" \
    "s_nop 0\n\t" \
    "global_load_lds_dwordx4 %[va], %[sa] offset:2048 sc0 sc1\n\t" \
    "s_add_u32 m0, %[lb], 90112\n\t" \
    "v_add_u32 %[vb], 0x8000, %[vb]\n\t" \
    "global_load_lds_dwordx4 %[vb], %[sb] offset:0\n\t" \
    "s_add_u32 m0, %[la], 12288\n\t" \
    "s_nop 0\n\t" \
    "global_load_lds_dwordx4 %[va], %[sa] offset:3072 sc0 sc1\n\t" \
    "s_waitcnt vmcnt(12)\n\t" \
    "s_barrier\n\t" \
    "s_add_u32 m0, %[lb], 94208\n\t" \
    "v_add_u32 %[vb], 0x8000, %[vb]\n\t" \
    "global_load_lds_dwordx4 %[vb], %[sb] offset:0\n\t" \
    "s_add_u32 m0, %[la], 4096\n\t" \
    "v_add_u32 %[va], 8192, %[va]\n\t" \
    "global_load_lds_dwordx4 %[va], %[sa] offset:-4096 sc0 sc1\n\t" \
    "s_add_u32 m0, %[lb], 8192\n\t" \
    "v_add_u32 %[vb], 0x8000, %[vb]\n\t" \
    "global_load_lds_dwordx4 %[vb], %[sb] offset:0\n\t" \
    "s_add_u32 m0, %[la], 4096\n\t" \
    "s_nop 0\n\t" \
    "global_load_lds_dwordx4 %[va], %[sa] offset:-3072 sc0 sc1\n\t" \
    "s_waitcnt vmcnt(12)\n\t" \
    "s_barrier\n\t" \
    "s_add_u32 m0, %[lb], 12288\n\t" \
    "v_add_u32 %[vb], 0x8000, %[vb]\n\t" \
    "global_load_lds_dwordx4 %[vb], %[sb] offset:0\n\t" \
    "s_add_u32 m0, %[la], 4096\n\t" \
    "s_nop 0\n\t" \
    "global_load_lds_dwordx4 %[va], %[sa] offset:-2048 sc0 sc1\n\t" \
    "s_add_u32 m0, %[lb], 32768\n\t" \
    "v_add_u32 %[vb], 0x8000, %[vb]\n\t" \
    "global_load_lds_dwordx4 %[vb], %[sb] offset:0\n\t" \
    "s_add_u32 m0, %[la], 4096\n\t" \
    "s_nop 0\n\t" \
    "global_load_lds_dwordx4 %[va], %[sa] offset:-1024 sc0 sc1\n\t" \
    "s_waitcnt vmcnt(12)\n\t" \
    "s_barrier\n\t" \
    "s_add_u32 m0, %[lb], 36864\n\t" \
    "v_add_u32 %[vb], 0x8000, %[vb]\n\t" \
    "global_load_lds_dwordx4 %[vb], %[sb] offset:0\n\t" \
    "s_add_u32 m0, %[la], 4096\n\t" \
    "s_nop 0\n\t" \
    "global_load_lds_dwordx4 %[va], %[sa] offset:0 sc0 sc1\n\t" \
    "s_add_u32 m0, %[lb], 57344\n\t" \
    "v_add_u32 %[vb], 0x8000, %[vb]\n\t" \
    "global_load_lds_dwordx4 %[vb], %[sb] offset:0\n\t" \
    "s_add_u32 m0, %[la], 4096\n\t" \
    "s_nop 0\n\t" \
    "global_load_lds_dwordx4 %[va], %[sa] offset:1024 sc0 sc1\n\t" \
    "s_waitcnt vmcnt(12)\n\t" \
    "s_barrier\n\t" \
    "s_add_u32 m0, %[lb], 61440\n\t" \
    "v_add_u32 %[vb], 0x8000, %[vb]\n\t" \
    "global_load_lds_dwordx4 %[vb], %[sb] offset:0\n\t" \
    "s_add_u32 m0, %[la], 4096\n\t" \
    "s_nop 0\n\t" \
    "global_load_lds_dwordx4 %[va], %[sa] offset:2048 sc0 sc1\n\t" \
    "s_add_u32 m0, %[lb], 81920\n\t" \
    "v_add_u32 %[vb], 0x8000, %[vb]\n\t" \
    "global_load_lds_dwordx4 %[vb], %[sb] offset:0\n\t" \
    "s_add_u32 m0, %[la], 4096\n\t" \
    "s_nop 0\n\t" \
    "global_load_lds_dwordx4 %[va], %[sa] offset:3072 sc0 sc1\n\t" \
    "s_waitcnt vmcnt(12)\n\t" \
    "s_barrier\n\t" \
    "s_add_u32 m0, %[lb], 86016\n\t" \
    "v_add_u32 %[vb], 0x8000, %[vb]\n\t" \
    "global_load_lds_dwordx4 %[vb], %[sb] offset:0\n\t" \
    "s_add_u32 m0, %[la], 12288\n\t" \
    "v_add_u32 %[va], 8192, %[va]\n\t" \
    "global_load_lds_dwordx4 %[va], %[sa] offset:-4096 sc0 sc1\n\t" \
    "s_add_u32 m0, %[lb], 0\n\t" \
    "v_add_u32 %[vb], 0x8000, %[vb]\n\t" \
    "global_load_lds_dwordx4 %[vb], %[sb] offset:0\n\t" \
    "s_add_u32 m0, %[la], 12288\n\t" \
    "s_nop 0\n\t" \
    "global_load_lds_dwordx4 %[va], %[sa] offset:-3072 sc0 sc1\n\t" \
    "s_waitcnt vmcnt(12)\n\t" \
    "s_barrier\n\t" \
    "s_add_u32 m0, %[lb], 4096\n\t" \
    "v_add_u32 %[vb], 0x8000, %[vb]\n\t" \
    "global_load_lds_dwordx4 %[vb], %[sb] offset:0\n\t" \
    "s_add_u32 m0, %[la], 12288\n\t" \
    "s_nop 0\n\t" \
    "global_load_lds_dwordx4 %[va], %[sa] offset:-2048 sc0 sc1\n\t" \
    "s_add_u32 m0, %[lb], 16384\n\t" \
    "v_add_u32 %[vb], 0x8000, %[vb]\n\t" \
    "global_load_lds_dwordx4 %[vb], %[sb] offset:0\n\t" \
    "s_add_u32 m0, %[la], 12288\n\t" \
    "s_nop 0\n\t" \
    "global_load_lds_dwordx4 %[va], %[sa] offset:-1024 sc0 sc1\n\t" \
    "s_waitcnt vmcnt(12)\n\t" \
    "s_barrier\n\t" \
    "s_add_u32 m0, %[lb], 20480\n\t" \
    "v_add_u32 %[vb], 0x8000, %[vb]\n\t" \
    "global_load_lds_dwordx4 %[vb], %[sb] offset:0\n\t" \
    "s_add_u32 m0, %[la], 12288\n\t" \
    "s_nop 0\n\t" \
    "global_load_lds_dwordx4 %[va], %[sa] offset:0 sc0 sc1\n\t" \
    "s_add_u32 m0, %[lb], 24576\n\t" \
    "v_add_u32 %[vb], 0x8000, %[vb]\n\t" \
    "global_load_lds_dwordx4 %[vb], %[sb] offset:0\n\t" \
    "s_add_u32 m0, %[la], 12288\n\t" \
    "s_nop 0\n\t" \
    "global_load_lds_dwordx4 %[va], %[sa] offset:1024 sc0 sc1\n\t" \
    "s_waitcnt vmcnt(12)\n\t" \
    "s_barrier\n\t" \
    "s_add_u32 m0, %[lb], 28672\n\t" \
    "v_add_u32 %[vb], 0x8000, %[vb]\n\t" \
    "global_load_lds_dwordx4 %[vb], %[sb] offset:0\n\t" \
    "s_add_u32 m0, %[la], 12288\n\t" \
    "s_nop 0\n\t" \
    "global_load_lds_dwordx4 %[va], %[sa] offset:2048 sc0 sc1\n\t" \
    "s_add_u32 m0, %[lb], 40960\n\t" \
    "v_add_u32 %[vb], 0x8000, %[vb]\n\t" \
    "global_load_lds_dwordx4 %[vb], %[sb] offset:0\n\t" \
    "s_add_u32 m0, %[la], 12288\n\t" \
    "s_nop 0\n\t" \
    "global_load_lds_dwordx4 %[va], %[sa] offset:3072 sc0 sc1\n\t" \
    "s_waitcnt vmcnt(12)\n\t" \
    "s_barrier\n\t" \
    "s_add_u32 m0, %[lb], 45056\n\t" \
    "v_add_u32 %[vb], 0x8000, %[vb]\n\t" \
    "global_load_lds_dwordx4 %[vb], %[sb] offset:0\n\t" \
    "s_add_u32 m0, %[la], 4096\n\t" \
    "v_add_u32 %[va], 8192, %[va]\n\t" \
    "global_load_lds_dwordx4 %[va], %[sa] offset:-4096 sc0 sc1\n\t" \
    "s_add_u32 m0, %[la], 4096\n\t" \
    "s_nop 0\n\t" \
    "global_load_lds_dwordx4 %[va], %[sa] offset:-3072 sc0 sc1\n\t" \
    "s_add_u32 m0, %[la], 4096\n\t" \
    "s_nop 0\n\t" \
    "global_load_lds_dwordx4 %[va], %[sa] offset:-2048 sc0 sc1\n\t" \
    "s_waitcnt vmcnt(12)\n\t" \
    "s_barrier\n\t" \
    "s_add_u32 m0, %[la], 4096\n\t" \
    "s_nop 0\n\t" \
    "global_load_lds_dwordx4 %[va], %[sa] offset:-1024 sc0 sc1\n\t" \
    "s_add_u32 m0, %[la], 4096\n\t" \
    "s_nop 0\n\t" \
    "global_load_lds_dwordx4 %[va], %[sa] offset:0 sc0 sc1\n\t" \
    "s_add_u32 m0, %[la], 4096\n\t" \
    "s_nop 0\n\t" \
    "global_load_lds_dwordx4 %[va], %[sa] offset:1024 sc0 sc1\n\t" \
    "s_add_u32 m0, %[la], 4096\n\t" \
    "s_nop 0\n\t" \
    "global_load_lds_dwordx4 %[va], %[sa] offset:2048 sc0 sc1\n\t" \
    "s_waitcnt vmcnt(12)\n\t" \
    "s_barrier\n\t" \
    "s_add_u32 m0, %[la], 4096\n\t" \
    "s_nop 0\n\t" \
    "global_load_lds_dwordx4 %[va], %[sa] offset:3072 sc0 sc1\n\t" \
    "s_waitcnt vmcnt(9)\n\t" \
    "s_barrier\n\t" \
    "s_waitcnt vmcnt(6)\n\t" \
    "s_barrier\n\t" \
    "s_waitcnt vmcnt(4)\n\t" \
    "s_barrier\n\t" \
    "s_waitcnt vmcnt(2)\n\t" \
    "s_barrier\n\t" \
    "s_waitcnt vmcnt(1)\n\t" \
    "s_barrier\n\t" \
    "s_waitcnt vmcnt(0)\n\t" \
    "s_barrier\n\t" \
    "s_waitcnt vmcnt(0)\n\t" \
    "s_endpgm\n\t"
#define SYNC16 \
    "s_waitcnt vmcnt(0)\n\t" \
    "" TSA "\n\t" \
    "s_cmp_eq_u32 %[w], 8\n\t" \
    "s_cselect_b64 exec, 1, -1\n\t" \
    "s_barrier\n\t" \
    "s_cbranch_scc0 .Ln%=\n\t" \
    "global_store_dword %[vs], %[v1], %[sh] sc0 sc1\n\t" \
    "s_mov_b64 exec, -1\n\t" \
    ".Ln%=:\n\t" \
    "" TSB "\n\t" \
    "s_cmp_lg_u32 %[w], 9\n\t" \
    "s_cbranch_scc1 .Ly%=\n\t" \
    "s_bfm_b64 exec, 16, 0\n\t" \
    "s_sleep 8\n\t" \
    ".Lpoll%=:\n\t" \
    "s_sleep 2\n\t" \
    "global_load_dword %[vt], %[vf], %[sh] sc0 sc1\n\t" \
    "s_waitcnt vmcnt(0)\n\t" \
    "v_cmp_eq_u32 vcc, 0, %[vt]\n\t" \
    "s_cbranch_vccnz .Lpoll%=\n\t" \
    ".Ly%=:\n\t" \
    "s_mov_b64 exec, -1\n\t" \
    "s_barrier\n\t"
#define SYNC32 \
    "s_waitcnt vmcnt(0)\n\t" \
    "" TSA "\n\t" \
    "s_cmp_eq_u32 %[w], 8\n\t" \
    "s_cselect_b64 exec, 1, -1\n\t" \
    "s_barrier\n\t" \
    "s_cbranch_scc0 .Ln%=\n\t" \
    "global_store_dword %[vs], %[v1], %[sh] sc0 sc1\n\t" \
    "s_mov_b64 exec, -1\n\t" \
    ".Ln%=:\n\t" \
    "" TSB "\n\t" \
    "s_cmp_lg_u32 %[w], 9\n\t" \
    "s_cbranch_scc1 .Ly%=\n\t" \
    "s_bfm_b64 exec, 32, 0\n\t" \
    "s_sleep 8\n\t" \
    ".Lpoll%=:\n\t" \
    "s_sleep 2\n\t" \
    "global_load_dword %[vt], %[vf], %[sh] sc0 sc1\n\t" \
    "s_waitcnt vmcnt(0)\n\t" \
    "v_cmp_eq_u32 vcc, 0, %[vt]\n\t" \
    "s_cbranch_vccnz .Lpoll%=\n\t" \
    ".Ly%=:\n\t" \
    "s_mov_b64 exec, -1\n\t" \
    "s_barrier\n\t"
#define K4P_CLOB "v64", "v65", "v66", "v67", "v68", "v69", "v70", "v71", "v72"
#define K4_CONS \
    "ds_read_b64_tr_b16 v[80:81], %[b0] offset:0\n\t" \
    "ds_read_b64_tr_b16 v[82:83], %[b0] offset:2048\n\t" \
    "ds_read_b64_tr_b16 v[84:85], %[b1] offset:0\n\t" \
    "ds_read_b64_tr_b16 v[86:87], %[b1] offset:2048\n\t" \
    "ds_read_b64_tr_b16 v[88:89], %[b0] offset:24576\n\t" \
    "ds_read_b64_tr_b16 v[90:91], %[b0] offset:26624\n\t" \
    "ds_read_b64_tr_b16 v[92:93], %[b1] offset:24576\n\t" \
    "ds_read_b64_tr_b16 v[94:95], %[b1] offset:26624\n\t" \
    "ds_read_b128 v[64:67], %[aa] offset:0\n\t" \
    "ds_read_b128 v[68:71], %[aa] offset:8192\n\t" \
    "ds_read_b128 v[72:75], %[aa] offset:16384\n\t" \
    "ds_read_b128 v[76:79], %[aa] offset:24576\n\t" \
    "s_waitcnt lgkmcnt(0)\n\t" \
    "s_barrier\n\t" \
    "v_mfma_f32_16x16x32_bf16 v[0:3], v[80:83], v[64:67], 0\n\t" \
    "ds_read_b64_tr_b16 v[112:113], %[b0] offset:4096\n\t" \
    "v_mfma_f32_16x16x32_bf16 v[4:7], v[84:87], v[64:67], 0\n\t" \
    "ds_read_b64_tr_b16 v[114:115], %[b0] offset:6144\n\t" \
    "v_mfma_f32_16x16x32_bf16 v[8:11], v[88:91], v[64:67], 0\n\t" \
    "ds_read_b64_tr_b16 v[116:117], %[b1] offset:4096\n\t" \
    "v_mfma_f32_16x16x32_bf16 v[12:15], v[92:95], v[64:67], 0\n\t" \
    "ds_read_b64_tr_b16 v[118:119], %[b1] offset:6144\n\t" \
    "v_mfma_f32_16x16x32_bf16 v[16:19], v[80:83], v[68:71], 0\n\t" \
    "ds_read_b64_tr_b16 v[120:121], %[b0] offset:28672\n\t" \
    "v_mfma_f32_16x16x32_bf16 v[20:23], v[84:87], v[68:71], 0\n\t" \
    "ds_read_b64_tr_b16 v[122:123], %[b0] offset:30720\n\t" \
    "v_mfma_f32_16x16x32_bf16 v[24:27], v[88:91], v[68:71], 0\n\t" \
    "ds_read_b64_tr_b16 v[124:125], %[b1] offset:28672\n\t" \
    "v_mfma_f32_16x16x32_bf16 v[28:31], v[92:95], v[68:71], 0\n\t" \
    "ds_read_b64_tr_b16 v[126:127], %[b1] offset:30720\n\t" \
    "v_mfma_f32_16x16x32_bf16 v[32:35], v[80:83], v[72:75], 0\n\t" \
    "ds_read_b128 v[96:99], %[aa] offset:1024\n\t" \
    "v_mfma_f32_16x16x32_bf16 v[36:39], v[84:87], v[72:75], 0\n\t" \
    "ds_read_b128 v[100:103], %[aa] offset:9216\n\t" \
    "v_mfma_f32_16x16x32_bf16 v[40:43], v[88:91], v[72:75], 0\n\t" \
    "ds_read_b128 v[104:107], %[aa] offset:17408\n\t" \
    "v_mfma_f32_16x16x32_bf16 v[44:47], v[92:95], v[72:75], 0\n\t" \
    "ds_read_b128 v[108:111], %[aa] offset:25600\n\t" \
    "v_mfma_f32_16x16x32_bf16 v[48:51], v[80:83], v[76:79], 0\n\t" \
    "v_mfma_f32_16x16x32_bf16 v[52:55], v[84:87], v[76:79], 0\n\t" \
    "v_mfma_f32_16x16x32_bf16 v[56:59], v[88:91], v[76:79], 0\n\t" \
    "v_mfma_f32_16x16x32_bf16 v[60:63], v[92:95], v[76:79], 0\n\t" \
    "s_waitcnt lgkmcnt(0)\n\t" \
    "s_barrier\n\t" \
    "v_mfma_f32_16x16x32_bf16 v[0:3], v[112:115], v[96:99], v[0:3]\n\t" \
    "ds_read_b64_tr_b16 v[80:81], %[b0] offset:8192\n\t" \
    "v_mfma_f32_16x16x32_bf16 v[4:7], v[116:119], v[96:99], v[4:7]\n\t" \
    "ds_read_b64_tr_b16 v[82:83], %[b0] offset:10240\n\t" \
    "v_mfma_f32_16x16x32_bf16 v[8:11], v[120:123], v[96:99], v[8:11]\n\t" \
    "ds_read_b64_tr_b16 v[84:85], %[b1] offset:8192\n\t" \
    "v_mfma_f32_16x16x32_bf16 v[12:15], v[124:127], v[96:99], v[12:15]\n\t" \
    "ds_read_b64_tr_b16 v[86:87], %[b1] offset:10240\n\t" \
    "v_mfma_f32_16x16x32_bf16 v[16:19], v[112:115], v[100:103], v[16:19]\n\t" \
    "ds_read_b64_tr_b16 v[88:89], %[b0] offset:32768\n\t" \
    "v_mfma_f32_16x16x32_bf16 v[20:23], v[116:119], v[100:103], v[20:23]\n\t" \
    "ds_read_b64_tr_b16 v[90:91], %[b0] offset:34816\n\t" \
    "v_mfma_f32_16x16x32_bf16 v[24:27], v[120:123], v[100:103], v[24:27]\n\t" \
    "ds_read_b64_tr_b16 v[92:93], %[b1] offset:32768\n\t" \
    "v_mfma_f32_16x16x32_bf16 v[28:31], v[124:127], v[100:103], v[28:31]\n\t" \
    "ds_read_b64_tr_b16 v[94:95], %[b1] offset:34816\n\t" \
    "v_mfma_f32_16x16x32_bf16 v[32:35], v[112:115], v[104:107], v[32:35]\n\t" \
    "ds_read_b128 v[64:67], %[aa] offset:2048\n\t" \
    "v_mfma_f32_16x16x32_bf16 v[36:39], v[116:119], v[104:107], v[36:39]\n\t" \
    "ds_read_b128 v[68:71], %[aa] offset:10240\n\t" \
    "v_mfma_f32_16x16x32_bf16 v[40:43], v[120:123], v[104:107], v[40:43]\n\t" \
    "ds_read_b128 v[72:75], %[aa] offset:18432\n\t" \
    "v_mfma_f32_16x16x32_bf16 v[44:47], v[124:127], v[104:107], v[44:47]\n\t" \
    "ds_read_b128 v[76:79], %[aa] offset:26624\n\t" \
    "v_mfma_f32_16x16x32_bf16 v[48:51], v[112:115], v[108:111], v[48:51]\n\t" \
    "v_mfma_f32_16x16x32_bf16 v[52:55], v[116:119], v[108:111], v[52:55]\n\t" \
    "v_mfma_f32_16x16x32_bf16 v[56:59], v[120:123], v[108:111], v[56:59]\n\t" \
    "v_mfma_f32_16x16x32_bf16 v[60:63], v[124:127], v[108:111], v[60:63]\n\t" \
    "s_waitcnt lgkmcnt(0)\n\t" \
    "s_barrier\n\t" \
    "v_mfma_f32_16x16x32_bf16 v[0:3], v[80:83], v[64:67], v[0:3]\n\t" \
    "ds_read_b64_tr_b16 v[112:113], %[b0] offset:12288\n\t" \
    "v_mfma_f32_16x16x32_bf16 v[4:7], v[84:87], v[64:67], v[4:7]\n\t" \
    "ds_read_b64_tr_b16 v[114:115], %[b0] offset:14336\n\t" \
    "v_mfma_f32_16x16x32_bf16 v[8:11], v[88:91], v[64:67], v[8:11]\n\t" \
    "ds_read_b64_tr_b16 v[116:117], %[b1] offset:12288\n\t" \
    "v_mfma_f32_16x16x32_bf16 v[12:15], v[92:95], v[64:67], v[12:15]\n\t" \
    "ds_read_b64_tr_b16 v[118:119], %[b1] offset:14336\n\t" \
    "v_mfma_f32_16x16x32_bf16 v[16:19], v[80:83], v[68:71], v[16:19]\n\t" \
    "ds_read_b64_tr_b16 v[120:121], %[b0] offset:36864\n\t" \
    "v_mfma_f32_16x16x32_bf16 v[20:23], v[84:87], v[68:71], v[20:23]\n\t" \
    "ds_read_b64_tr_b16 v[122:123], %[b0] offset:38912\n\t" \
    "v_mfma_f32_16x16x32_bf16 v[24:27], v[88:91], v[68:71], v[24:27]\n\t" \
    "ds_read_b64_tr_b16 v[124:125], %[b1] offset:36864\n\t" \
    "v_mfma_f32_16x16x32_bf16 v[28:31], v[92:95], v[68:71], v[28:31]\n\t" \
    "ds_read_b64_tr_b16 v[126:127], %[b1] offset:38912\n\t" \
    "v_mfma_f32_16x16x32_bf16 v[32:35], v[80:83], v[72:75], v[32:35]\n\t" \
    "ds_read_b128 v[96:99], %[aa] offset:3072\n\t" \
    "v_mfma_f32_16x16x32_bf16 v[36:39], v[84:87], v[72:75], v[36:39]\n\t" \
    "ds_read_b128 v[100:103], %[aa] offset:11264\n\t" \
    "v_mfma_f32_16x16x32_bf16 v[40:43], v[88:91], v[72:75], v[40:43]\n\t" \
    "ds_read_b128 v[104:107], %[aa] offset:19456\n\t" \
    "v_mfma_f32_16x16x32_bf16 v[44:47], v[92:95], v[72:75], v[44:47]\n\t" \
    "ds_read_b128 v[108:111], %[aa] offset:27648\n\t" \
    "v_mfma_f32_16x16x32_bf16 v[48:51], v[80:83], v[76:79], v[48:51]\n\t" \
    "v_mfma_f32_16x16x32_bf16 v[52:55], v[84:87], v[76:79], v[52:55]\n\t" \
    "v_mfma_f32_16x16x32_bf16 v[56:59], v[88:91], v[76:79], v[56:59]\n\t" \
    "v_mfma_f32_16x16x32_bf16 v[60:63], v[92:95], v[76:79], v[60:63]\n\t" \
    "s_waitcnt lgkmcnt(0)\n\t" \
    "s_barrier\n\t" \
    "v_mfma_f32_16x16x32_bf16 v[0:3], v[112:115], v[96:99], v[0:3]\n\t" \
    "ds_read_b64_tr_b16 v[80:81], %[b0] offset:16384\n\t" \
    "v_mfma_f32_16x16x32_bf16 v[4:7], v[116:119], v[96:99], v[4:7]\n\t" \
    "ds_read_b64_tr_b16 v[82:83], %[b0] offset:18432\n\t" \
    "v_mfma_f32_16x16x32_bf16 v[8:11], v[120:123], v[96:99], v[8:11]\n\t" \
    "ds_read_b64_tr_b16 v[84:85], %[b1] offset:16384\n\t" \
    "v_mfma_f32_16x16x32_bf16 v[12:15], v[124:127], v[96:99], v[12:15]\n\t" \
    "ds_read_b64_tr_b16 v[86:87], %[b1] offset:18432\n\t" \
    "v_mfma_f32_16x16x32_bf16 v[16:19], v[112:115], v[100:103], v[16:19]\n\t" \
    "ds_read_b64_tr_b16 v[88:89], %[b0] offset:40960\n\t" \
    "v_mfma_f32_16x16x32_bf16 v[20:23], v[116:119], v[100:103], v[20:23]\n\t" \
    "ds_read_b64_tr_b16 v[90:91], %[b0] offset:43008\n\t" \
    "v_mfma_f32_16x16x32_bf16 v[24:27], v[120:123], v[100:103], v[24:27]\n\t" \
    "ds_read_b64_tr_b16 v[92:93], %[b1] offset:40960\n\t" \
    "v_mfma_f32_16x16x32_bf16 v[28:31], v[124:127], v[100:103], v[28:31]\n\t" \
    "ds_read_b64_tr_b16 v[94:95], %[b1] offset:43008\n\t" \
    "v_mfma_f32_16x16x32_bf16 v[32:35], v[112:115], v[104:107], v[32:35]\n\t" \
    "ds_read_b128 v[64:67], %[aa] offset:4096\n\t" \
    "v_mfma_f32_16x16x32_bf16 v[36:39], v[116:119], v[104:107], v[36:39]\n\t" \
    "ds_read_b128 v[68:71], %[aa] offset:12288\n\t" \
    "v_mfma_f32_16x16x32_bf16 v[40:43], v[120:123], v[104:107], v[40:43]\n\t" \
    "ds_read_b128 v[72:75], %[aa] offset:20480\n\t" \
    "v_mfma_f32_16x16x32_bf16 v[44:47], v[124:127], v[104:107], v[44:47]\n\t" \
    "ds_read_b128 v[76:79], %[aa] offset:28672\n\t" \
    "v_mfma_f32_16x16x32_bf16 v[48:51], v[112:115], v[108:111], v[48:51]\n\t" \
    "v_mfma_f32_16x16x32_bf16 v[52:55], v[116:119], v[108:111], v[52:55]\n\t" \
    "v_mfma_f32_16x16x32_bf16 v[56:59], v[120:123], v[108:111], v[56:59]\n\t" \
    "v_mfma_f32_16x16x32_bf16 v[60:63], v[124:127], v[108:111], v[60:63]\n\t" \
    "s_waitcnt lgkmcnt(0)\n\t" \
    "v_mfma_f32_16x16x32_bf16 v[0:3], v[80:83], v[64:67], v[0:3]\n\t" \
    "ds_read_b64_tr_b16 v[112:113], %[b0] offset:20480\n\t" \
    "v_mfma_f32_16x16x32_bf16 v[4:7], v[84:87], v[64:67], v[4:7]\n\t" \
    "ds_read_b64_tr_b16 v[114:115], %[b0] offset:22528\n\t" \
    "v_mfma_f32_16x16x32_bf16 v[8:11], v[88:91], v[64:67], v[8:11]\n\t" \
    "ds_read_b64_tr_b16 v[116:117], %[b1] offset:20480\n\t" \
    "v_mfma_f32_16x16x32_bf16 v[12:15], v[92:95], v[64:67], v[12:15]\n\t" \
    "ds_read_b64_tr_b16 v[118:119], %[b1] offset:22528\n\t" \
    "v_mfma_f32_16x16x32_bf16 v[16:19], v[80:83], v[68:71], v[16:19]\n\t" \
    "ds_read_b64_tr_b16 v[120:121], %[b0] offset:45056\n\t" \
    "v_mfma_f32_16x16x32_bf16 v[20:23], v[84:87], v[68:71], v[20:23]\n\t" \
    "ds_read_b64_tr_b16 v[122:123], %[b0] offset:47104\n\t" \
    "v_mfma_f32_16x16x32_bf16 v[24:27], v[88:91], v[68:71], v[24:27]\n\t" \
    "ds_read_b64_tr_b16 v[124:125], %[b1] offset:45056\n\t" \
    "v_mfma_f32_16x16x32_bf16 v[28:31], v[92:95], v[68:71], v[28:31]\n\t" \
    "ds_read_b64_tr_b16 v[126:127], %[b1] offset:47104\n\t" \
    "v_mfma_f32_16x16x32_bf16 v[32:35], v[80:83], v[72:75], v[32:35]\n\t" \
    "ds_read_b128 v[96:99], %[aa] offset:5120\n\t" \
    "v_mfma_f32_16x16x32_bf16 v[36:39], v[84:87], v[72:75], v[36:39]\n\t" \
    "ds_read_b128 v[100:103], %[aa] offset:13312\n\t" \
    "v_mfma_f32_16x16x32_bf16 v[40:43], v[88:91], v[72:75], v[40:43]\n\t" \
    "ds_read_b128 v[104:107], %[aa] offset:21504\n\t" \
    "v_mfma_f32_16x16x32_bf16 v[44:47], v[92:95], v[72:75], v[44:47]\n\t" \
    "ds_read_b128 v[108:111], %[aa] offset:29696\n\t" \
    "v_mfma_f32_16x16x32_bf16 v[48:51], v[80:83], v[76:79], v[48:51]\n\t" \
    "v_mfma_f32_16x16x32_bf16 v[52:55], v[84:87], v[76:79], v[52:55]\n\t" \
    "v_mfma_f32_16x16x32_bf16 v[56:59], v[88:91], v[76:79], v[56:59]\n\t" \
    "v_mfma_f32_16x16x32_bf16 v[60:63], v[92:95], v[76:79], v[60:63]\n\t" \
    "s_waitcnt lgkmcnt(0)\n\t" \
    "s_barrier\n\t" \
    "v_mfma_f32_16x16x32_bf16 v[0:3], v[112:115], v[96:99], v[0:3]\n\t" \
    "ds_read_b64_tr_b16 v[80:81], %[b0] offset:0\n\t" \
    "v_mfma_f32_16x16x32_bf16 v[4:7], v[116:119], v[96:99], v[4:7]\n\t" \
    "ds_read_b64_tr_b16 v[82:83], %[b0] offset:2048\n\t" \
    "v_mfma_f32_16x16x32_bf16 v[8:11], v[120:123], v[96:99], v[8:11]\n\t" \
    "ds_read_b64_tr_b16 v[84:85], %[b1] offset:0\n\t" \
    "v_mfma_f32_16x16x32_bf16 v[12:15], v[124:127], v[96:99], v[12:15]\n\t" \
    "ds_read_b64_tr_b16 v[86:87], %[b1] offset:2048\n\t" \
    "v_mfma_f32_16x16x32_bf16 v[16:19], v[112:115], v[100:103], v[16:19]\n\t" \
    "ds_read_b64_tr_b16 v[88:89], %[b0] offset:24576\n\t" \
    "v_mfma_f32_16x16x32_bf16 v[20:23], v[116:119], v[100:103], v[20:23]\n\t" \
    "ds_read_b64_tr_b16 v[90:91], %[b0] offset:26624\n\t" \
    "v_mfma_f32_16x16x32_bf16 v[24:27], v[120:123], v[100:103], v[24:27]\n\t" \
    "ds_read_b64_tr_b16 v[92:93], %[b1] offset:24576\n\t" \
    "v_mfma_f32_16x16x32_bf16 v[28:31], v[124:127], v[100:103], v[28:31]\n\t" \
    "ds_read_b64_tr_b16 v[94:95], %[b1] offset:26624\n\t" \
    "v_mfma_f32_16x16x32_bf16 v[32:35], v[112:115], v[104:107], v[32:35]\n\t" \
    "ds_read_b128 v[64:67], %[aa] offset:6144\n\t" \
    "v_mfma_f32_16x16x32_bf16 v[36:39], v[116:119], v[104:107], v[36:39]\n\t" \
    "ds_read_b128 v[68:71], %[aa] offset:14336\n\t" \
    "v_mfma_f32_16x16x32_bf16 v[40:43], v[120:123], v[104:107], v[40:43]\n\t" \
    "ds_read_b128 v[72:75], %[aa] offset:22528\n\t" \
    "v_mfma_f32_16x16x32_bf16 v[44:47], v[124:127], v[104:107], v[44:47]\n\t" \
    "ds_read_b128 v[76:79], %[aa] offset:30720\n\t" \
    "v_mfma_f32_16x16x32_bf16 v[48:51], v[112:115], v[108:111], v[48:51]\n\t" \
    "v_mfma_f32_16x16x32_bf16 v[52:55], v[116:119], v[108:111], v[52:55]\n\t" \
    "v_mfma_f32_16x16x32_bf16 v[56:59], v[120:123], v[108:111], v[56:59]\n\t" \
    "v_mfma_f32_16x16x32_bf16 v[60:63], v[124:127], v[108:111], v[60:63]\n\t" \
    "s_waitcnt lgkmcnt(0)\n\t" \
    "v_mfma_f32_16x16x32_bf16 v[0:3], v[80:83], v[64:67], v[0:3]\n\t" \
    "ds_read_b64_tr_b16 v[112:113], %[b0] offset:4096\n\t" \
    "v_mfma_f32_16x16x32_bf16 v[4:7], v[84:87], v[64:67], v[4:7]\n\t" \
    "ds_read_b64_tr_b16 v[114:115], %[b0] offset:6144\n\t" \
    "v_mfma_f32_16x16x32_bf16 v[8:11], v[88:91], v[64:67], v[8:11]\n\t" \
    "ds_read_b64_tr_b16 v[116:117], %[b1] offset:4096\n\t" \
    "v_mfma_f32_16x16x32_bf16 v[12:15], v[92:95], v[64:67], v[12:15]\n\t" \
    "ds_read_b64_tr_b16 v[118:119], %[b1] offset:6144\n\t" \
    "v_mfma_f32_16x16x32_bf16 v[16:19], v[80:83], v[68:71], v[16:19]\n\t" \
    "ds_read_b64_tr_b16 v[120:121], %[b0] offset:28672\n\t" \
    "v_mfma_f32_16x16x32_bf16 v[20:23], v[84:87], v[68:71], v[20:23]\n\t" \
    "ds_read_b64_tr_b16 v[122:123], %[b0] offset:30720\n\t" \
    "v_mfma_f32_16x16x32_bf16 v[24:27], v[88:91], v[68:71], v[24:27]\n\t" \
    "ds_read_b64_tr_b16 v[124:125], %[b1] offset:28672\n\t" \
    "v_mfma_f32_16x16x32_bf16 v[28:31], v[92:95], v[68:71], v[28:31]\n\t" \
    "ds_read_b64_tr_b16 v[126:127], %[b1] offset:30720\n\t" \
    "v_mfma_f32_16x16x32_bf16 v[32:35], v[80:83], v[72:75], v[32:35]\n\t" \
    "ds_read_b128 v[96:99], %[aa] offset:7168\n\t" \
    "v_mfma_f32_16x16x32_bf16 v[36:39], v[84:87], v[72:75], v[36:39]\n\t" \
    "ds_read_b128 v[100:103], %[aa] offset:15360\n\t" \
    "v_mfma_f32_16x16x32_bf16 v[40:43], v[88:91], v[72:75], v[40:43]\n\t" \
    "ds_read_b128 v[104:107], %[aa] offset:23552\n\t" \
    "v_mfma_f32_16x16x32_bf16 v[44:47], v[92:95], v[72:75], v[44:47]\n\t" \
    "ds_read_b128 v[108:111], %[aa] offset:31744\n\t" \
    "v_mfma_f32_16x16x32_bf16 v[48:51], v[80:83], v[76:79], v[48:51]\n\t" \
    "v_mfma_f32_16x16x32_bf16 v[52:55], v[84:87], v[76:79], v[52:55]\n\t" \
    "v_mfma_f32_16x16x32_bf16 v[56:59], v[88:91], v[76:79], v[56:59]\n\t" \
    "v_mfma_f32_16x16x32_bf16 v[60:63], v[92:95], v[76:79], v[60:63]\n\t" \
    "s_waitcnt lgkmcnt(0)\n\t" \
    "s_barrier\n\t" \
    "v_mfma_f32_16x16x32_bf16 v[0:3], v[112:115], v[96:99], v[0:3]\n\t" \
    "ds_read_b64_tr_b16 v[80:81], %[b0] offset:8192\n\t" \
    "v_mfma_f32_16x16x32_bf16 v[4:7], v[116:119], v[96:99], v[4:7]\n\t" \
    "ds_read_b64_tr_b16 v[82:83], %[b0] offset:10240\n\t" \
    "v_mfma_f32_16x16x32_bf16 v[8:11], v[120:123], v[96:99], v[8:11]\n\t" \
    "ds_read_b64_tr_b16 v[84:85], %[b1] offset:8192\n\t" \
    "v_mfma_f32_16x16x32_bf16 v[12:15], v[124:127], v[96:99], v[12:15]\n\t" \
    "ds_read_b64_tr_b16 v[86:87], %[b1] offset:10240\n\t" \
    "v_mfma_f32_16x16x32_bf16 v[16:19], v[112:115], v[100:103], v[16:19]\n\t" \
    "ds_read_b64_tr_b16 v[88:89], %[b0] offset:32768\n\t" \
    "v_mfma_f32_16x16x32_bf16 v[20:23], v[116:119], v[100:103], v[20:23]\n\t" \
    "ds_read_b64_tr_b16 v[90:91], %[b0] offset:34816\n\t" \
    "v_mfma_f32_16x16x32_bf16 v[24:27], v[120:123], v[100:103], v[24:27]\n\t" \
    "ds_read_b64_tr_b16 v[92:93], %[b1] offset:32768\n\t" \
    "v_mfma_f32_16x16x32_bf16 v[28:31], v[124:127], v[100:103], v[28:31]\n\t" \
    "ds_read_b64_tr_b16 v[94:95], %[b1] offset:34816\n\t" \
    "v_mfma_f32_16x16x32_bf16 v[32:35], v[112:115], v[104:107], v[32:35]\n\t" \
    "ds_read_b128 v[64:67], %[aa] offset:0\n\t" \
    "v_mfma_f32_16x16x32_bf16 v[36:39], v[116:119], v[104:107], v[36:39]\n\t" \
    "ds_read_b128 v[68:71], %[aa] offset:8192\n\t" \
    "v_mfma_f32_16x16x32_bf16 v[40:43], v[120:123], v[104:107], v[40:43]\n\t" \
    "ds_read_b128 v[72:75], %[aa] offset:16384\n\t" \
    "v_mfma_f32_16x16x32_bf16 v[44:47], v[124:127], v[104:107], v[44:47]\n\t" \
    "ds_read_b128 v[76:79], %[aa] offset:24576\n\t" \
    "v_mfma_f32_16x16x32_bf16 v[48:51], v[112:115], v[108:111], v[48:51]\n\t" \
    "v_mfma_f32_16x16x32_bf16 v[52:55], v[116:119], v[108:111], v[52:55]\n\t" \
    "v_mfma_f32_16x16x32_bf16 v[56:59], v[120:123], v[108:111], v[56:59]\n\t" \
    "v_mfma_f32_16x16x32_bf16 v[60:63], v[124:127], v[108:111], v[60:63]\n\t" \
    "s_waitcnt lgkmcnt(0)\n\t" \
    "v_mfma_f32_16x16x32_bf16 v[0:3], v[80:83], v[64:67], v[0:3]\n\t" \
    "ds_read_b64_tr_b16 v[112:113], %[b0] offset:12288\n\t" \
    "v_mfma_f32_16x16x32_bf16 v[4:7], v[84:87], v[64:67], v[4:7]\n\t" \
    "ds_read_b64_tr_b16 v[114:115], %[b0] offset:14336\n\t" \
    "v_mfma_f32_16x16x32_bf16 v[8:11], v[88:91], v[64:67], v[8:11]\n\t" \
    "ds_read_b64_tr_b16 v[116:117], %[b1] offset:12288\n\t" \
    "v_mfma_f32_16x16x32_bf16 v[12:15], v[92:95], v[64:67], v[12:15]\n\t" \
    "ds_read_b64_tr_b16 v[118:119], %[b1] offset:14336\n\t" \
    "v_mfma_f32_16x16x32_bf16 v[16:19], v[80:83], v[68:71], v[16:19]\n\t" \
    "ds_read_b64_tr_b16 v[120:121], %[b0] offset:36864\n\t" \
    "v_mfma_f32_16x16x32_bf16 v[20:23], v[84:87], v[68:71], v[20:23]\n\t" \
    "ds_read_b64_tr_b16 v[122:123], %[b0] offset:38912\n\t" \
    "v_mfma_f32_16x16x32_bf16 v[24:27], v[88:91], v[68:71], v[24:27]\n\t" \
    "ds_read_b64_tr_b16 v[124:125], %[b1] offset:36864\n\t" \
    "v_mfma_f32_16x16x32_bf16 v[28:31], v[92:95], v[68:71], v[28:31]\n\t" \
    "ds_read_b64_tr_b16 v[126:127], %[b1] offset:38912\n\t" \
    "v_mfma_f32_16x16x32_bf16 v[32:35], v[80:83], v[72:75], v[32:35]\n\t" \
    "ds_read_b128 v[96:99], %[aa] offset:1024\n\t" \
    "v_mfma_f32_16x16x32_bf16 v[36:39], v[84:87], v[72:75], v[36:39]\n\t" \
    "ds_read_b128 v[100:103], %[aa] offset:9216\n\t" \
    "v_mfma_f32_16x16x32_bf16 v[40:43], v[88:91], v[72:75], v[40:43]\n\t" \
    "ds_read_b128 v[104:107], %[aa] offset:17408\n\t" \
    "v_mfma_f32_16x16x32_bf16 v[44:47], v[92:95], v[72:75], v[44:47]\n\t" \
    "ds_read_b128 v[108:111], %[aa] offset:25600\n\t" \
    "v_mfma_f32_16x16x32_bf16 v[48:51], v[80:83], v[76:79], v[48:51]\n\t" \
    "v_mfma_f32_16x16x32_bf16 v[52:55], v[84:87], v[76:79], v[52:55]\n\t" \
    "v_mfma_f32_16x16x32_bf16 v[56:59], v[88:91], v[76:79], v[56:59]\n\t" \
    "v_mfma_f32_16x16x32_bf16 v[60:63], v[92:95], v[76:79], v[60:63]\n\t" \
    "s_waitcnt lgkmcnt(0)\n\t" \
    "s_barrier\n\t" \
    "v_mfma_f32_16x16x32_bf16 v[0:3], v[112:115], v[96:99], v[0:3]\n\t" \
    "ds_read_b64_tr_b16 v[80:81], %[b0] offset:16384\n\t" \
    "v_mfma_f32_16x16x32_bf16 v[4:7], v[116:119], v[96:99], v[4:7]\n\t" \
    "ds_read_b64_tr_b16 v[82:83], %[b0] offset:18432\n\t" \
    "v_mfma_f32_16x16x32_bf16 v[8:11], v[120:123], v[96:99], v[8:11]\n\t" \
    "ds_read_b64_tr_b16 v[84:85], %[b1] offset:16384\n\t" \
    "v_mfma_f32_16x16x32_bf16 v[12:15], v[124:127], v[96:99], v[12:15]\n\t" \
    "ds_read_b64_tr_b16 v[86:87], %[b1] offset:18432\n\t" \
    "v_mfma_f32_16x16x32_bf16 v[16:19], v[112:115], v[100:103], v[16:19]\n\t" \
    "ds_read_b64_tr_b16 v[88:89], %[b0] offset:40960\n\t" \
    "v_mfma_f32_16x16x32_bf16 v[20:23], v[116:119], v[100:103], v[20:23]\n\t" \
    "ds_read_b64_tr_b16 v[90:91], %[b0] offset:43008\n\t" \
    "v_mfma_f32_16x16x32_bf16 v[24:27], v[120:123], v[100:103], v[24:27]\n\t" \
    "ds_read_b64_tr_b16 v[92:93], %[b1] offset:40960\n\t" \
    "v_mfma_f32_16x16x32_bf16 v[28:31], v[124:127], v[100:103], v[28:31]\n\t" \
    "ds_read_b64_tr_b16 v[94:95], %[b1] offset:43008\n\t" \
    "v_mfma_f32_16x16x32_bf16 v[32:35], v[112:115], v[104:107], v[32:35]\n\t" \
    "ds_read_b128 v[64:67], %[aa] offset:2048\n\t" \
    "v_mfma_f32_16x16x32_bf16 v[36:39], v[116:119], v[104:107], v[36:39]\n\t" \
    "ds_read_b128 v[68:71], %[aa] offset:10240\n\t" \
    "v_mfma_f32_16x16x32_bf16 v[40:43], v[120:123], v[104:107], v[40:43]\n\t" \
    "ds_read_b128 v[72:75], %[aa] offset:18432\n\t" \
    "v_mfma_f32_16x16x32_bf16 v[44:47], v[124:127], v[104:107], v[44:47]\n\t" \
    "ds_read_b128 v[76:79], %[aa] offset:26624\n\t" \
    "v_mfma_f32_16x16x32_bf16 v[48:51], v[112:115], v[108:111], v[48:51]\n\t" \
    "v_mfma_f32_16x16x32_bf16 v[52:55], v[116:119], v[108:111], v[52:55]\n\t" \
    "v_mfma_f32_16x16x32_bf16 v[56:59], v[120:123], v[108:111], v[56:59]\n\t" \
    "v_mfma_f32_16x16x32_bf16 v[60:63], v[124:127], v[108:111], v[60:63]\n\t" \
    "s_waitcnt lgkmcnt(0)\n\t" \
    "v_mfma_f32_16x16x32_bf16 v[0:3], v[80:83], v[64:67], v[0:3]\n\t" \
    "ds_read_b64_tr_b16 v[112:113], %[b0] offset:20480\n\t" \
    "v_mfma_f32_16x16x32_bf16 v[4:7], v[84:87], v[64:67], v[4:7]\n\t" \
    "ds_read_b64_tr_b16 v[114:115], %[b0] offset:22528\n\t" \
    "v_mfma_f32_16x16x32_bf16 v[8:11], v[88:91], v[64:67], v[8:11]\n\t" \
    "ds_read_b64_tr_b16 v[116:117], %[b1] offset:20480\n\t" \
    "v_mfma_f32_16x16x32_bf16 v[12:15], v[92:95], v[64:67], v[12:15]\n\t" \
    "ds_read_b64_tr_b16 v[118:119], %[b1] offset:22528\n\t" \
    "v_mfma_f32_16x16x32_bf16 v[16:19], v[80:83], v[68:71], v[16:19]\n\t" \
    "ds_read_b64_tr_b16 v[120:121], %[b0] offset:45056\n\t" \
    "v_mfma_f32_16x16x32_bf16 v[20:23], v[84:87], v[68:71], v[20:23]\n\t" \
    "ds_read_b64_tr_b16 v[122:123], %[b0] offset:47104\n\t" \
    "v_mfma_f32_16x16x32_bf16 v[24:27], v[88:91], v[68:71], v[24:27]\n\t" \
    "ds_read_b64_tr_b16 v[124:125], %[b1] offset:45056\n\t" \
    "v_mfma_f32_16x16x32_bf16 v[28:31], v[92:95], v[68:71], v[28:31]\n\t" \
    "ds_read_b64_tr_b16 v[126:127], %[b1] offset:47104\n\t" \
    "v_mfma_f32_16x16x32_bf16 v[32:35], v[80:83], v[72:75], v[32:35]\n\t" \
    "ds_read_b128 v[96:99], %[aa] offset:3072\n\t" \
    "v_mfma_f32_16x16x32_bf16 v[36:39], v[84:87], v[72:75], v[36:39]\n\t" \
    "ds_read_b128 v[100:103], %[aa] offset:11264\n\t" \
    "v_mfma_f32_16x16x32_bf16 v[40:43], v[88:91], v[72:75], v[40:43]\n\t" \
    "ds_read_b128 v[104:107], %[aa] offset:19456\n\t" \
    "v_mfma_f32_16x16x32_bf16 v[44:47], v[92:95], v[72:75], v[44:47]\n\t" \
    "ds_read_b128 v[108:111], %[aa] offset:27648\n\t" \
    "v_mfma_f32_16x16x32_bf16 v[48:51], v[80:83], v[76:79], v[48:51]\n\t" \
    "v_mfma_f32_16x16x32_bf16 v[52:55], v[84:87], v[76:79], v[52:55]\n\t" \
    "v_mfma_f32_16x16x32_bf16 v[56:59], v[88:91], v[76:79], v[56:59]\n\t" \
    "v_mfma_f32_16x16x32_bf16 v[60:63], v[92:95], v[76:79], v[60:63]\n\t" \
    "s_waitcnt lgkmcnt(0)\n\t" \
    "s_barrier\n\t" \
    "v_mfma_f32_16x16x32_bf16 v[0:3], v[112:115], v[96:99], v[0:3]\n\t" \
    "ds_read_b64_tr_b16 v[80:81], %[b0] offset:0\n\t" \
    "v_mfma_f32_16x16x32_bf16 v[4:7], v[116:119], v[96:99], v[4:7]\n\t" \
    "ds_read_b64_tr_b16 v[82:83], %[b0] offset:2048\n\t" \
    "v_mfma_f32_16x16x32_bf16 v[8:11], v[120:123], v[96:99], v[8:11]\n\t" \
    "ds_read_b64_tr_b16 v[84:85], %[b1] offset:0\n\t" \
    "v_mfma_f32_16x16x32_bf16 v[12:15], v[124:127], v[96:99], v[12:15]\n\t" \
    "ds_read_b64_tr_b16 v[86:87], %[b1] offset:2048\n\t" \
    "v_mfma_f32_16x16x32_bf16 v[16:19], v[112:115], v[100:103], v[16:19]\n\t" \
    "ds_read_b64_tr_b16 v[88:89], %[b0] offset:24576\n\t" \
    "v_mfma_f32_16x16x32_bf16 v[20:23], v[116:119], v[100:103], v[20:23]\n\t" \
    "ds_read_b64_tr_b16 v[90:91], %[b0] offset:26624\n\t" \
    "v_mfma_f32_16x16x32_bf16 v[24:27], v[120:123], v[100:103], v[24:27]\n\t" \
    "ds_read_b64_tr_b16 v[92:93], %[b1] offset:24576\n\t" \
    "v_mfma_f32_16x16x32_bf16 v[28:31], v[124:127], v[100:103], v[28:31]\n\t" \
    "ds_read_b64_tr_b16 v[94:95], %[b1] offset:26624\n\t" \
    "v_mfma_f32_16x16x32_bf16 v[32:35], v[112:115], v[104:107], v[32:35]\n\t" \
    "ds_read_b128 v[64:67], %[aa] offset:4096\n\t" \
    "v_mfma_f32_16x16x32_bf16 v[36:39], v[116:119], v[104:107], v[36:39]\n\t" \
    "ds_read_b128 v[68:71], %[aa] offset:12288\n\t" \
    "v_mfma_f32_16x16x32_bf16 v[40:43], v[120:123], v[104:107], v[40:43]\n\t" \
    "ds_read_b128 v[72:75], %[aa] offset:20480\n\t" \
    "v_mfma_f32_16x16x32_bf16 v[44:47], v[124:127], v[104:107], v[44:47]\n\t" \
    "ds_read_b128 v[76:79], %[aa] offset:28672\n\t" \
    "v_mfma_f32_16x16x32_bf16 v[48:51], v[112:115], v[108:111], v[48:51]\n\t" \
    "ds_read_b128 v[128:131], %[rh] offset:0\n\t" \
    "ds_read_b128 v[132:135], %[rh] offset:256\n\t" \
    "v_mfma_f32_16x16x32_bf16 v[52:55], v[116:119], v[108:111], v[52:55]\n\t" \
    "ds_read_b128 v[136:139], %[rh] offset:16384\n\t" \
    "ds_read_b128 v[140:143], %[rh] offset:16640\n\t" \
    "v_mfma_f32_16x16x32_bf16 v[56:59], v[120:123], v[108:111], v[56:59]\n\t" \
    "ds_read_b128 v[144:147], %[rc] offset:0\n\t" \
    "ds_read_b128 v[148:151], %[rc] offset:64\n\t" \
    "v_mfma_f32_16x16x32_bf16 v[60:63], v[124:127], v[108:111], v[60:63]\n\t" \
    "ds_read_b128 v[152:155], %[rc] offset:32768\n\t" \
    "ds_read_b128 v[156:159], %[rc] offset:32832\n\t" \
    "s_waitcnt lgkmcnt(0)\n\t" \
    "v_mfma_f32_16x16x32_bf16 v[0:3], v[80:83], v[64:67], v[0:3]\n\t" \
    "ds_read_b64_tr_b16 v[112:113], %[b0] offset:4096\n\t" \
    "v_fmac_f32 v32, v136, v144\n\t" \
    "v_fmac_f32 v33, v136, v145\n\t" \
    "v_mfma_f32_16x16x32_bf16 v[4:7], v[84:87], v[64:67], v[4:7]\n\t" \
    "ds_read_b64_tr_b16 v[114:115], %[b0] offset:6144\n\t" \
    "v_fmac_f32 v34, v136, v146\n\t" \
    "v_fmac_f32 v35, v136, v147\n\t" \
    "v_mfma_f32_16x16x32_bf16 v[8:11], v[88:91], v[64:67], v[8:11]\n\t" \
    "ds_read_b64_tr_b16 v[116:117], %[b1] offset:4096\n\t" \
    "v_fmac_f32 v36, v136, v148\n\t" \
    "v_fmac_f32 v37, v136, v149\n\t" \
    "v_mfma_f32_16x16x32_bf16 v[12:15], v[92:95], v[64:67], v[12:15]\n\t" \
    "ds_read_b64_tr_b16 v[118:119], %[b1] offset:6144\n\t" \
    "v_fmac_f32 v38, v136, v150\n\t" \
    "v_fmac_f32 v39, v136, v151\n\t" \
    "v_mfma_f32_16x16x32_bf16 v[16:19], v[80:83], v[68:71], v[16:19]\n\t" \
    "ds_read_b64_tr_b16 v[120:121], %[b0] offset:28672\n\t" \
    "v_fmac_f32 v40, v136, v152\n\t" \
    "v_fmac_f32 v41, v136, v153\n\t" \
    "v_mfma_f32_16x16x32_bf16 v[20:23], v[84:87], v[68:71], v[20:23]\n\t" \
    "ds_read_b64_tr_b16 v[122:123], %[b0] offset:30720\n\t" \
    "v_fmac_f32 v42, v136, v154\n\t" \
    "v_fmac_f32 v43, v136, v155\n\t" \
    "v_mfma_f32_16x16x32_bf16 v[24:27], v[88:91], v[68:71], v[24:27]\n\t" \
    "ds_read_b64_tr_b16 v[124:125], %[b1] offset:28672\n\t" \
    "v_fmac_f32 v44, v136, v156\n\t" \
    "v_fmac_f32 v45, v136, v157\n\t" \
    "v_mfma_f32_16x16x32_bf16 v[28:31], v[92:95], v[68:71], v[28:31]\n\t" \
    "ds_read_b64_tr_b16 v[126:127], %[b1] offset:30720\n\t" \
    "v_fmac_f32 v46, v136, v158\n\t" \
    "v_fmac_f32 v47, v136, v159\n\t" \
    "v_mfma_f32_16x16x32_bf16 v[32:35], v[80:83], v[72:75], v[32:35]\n\t" \
    "ds_read_b128 v[96:99], %[aa] offset:5120\n\t" \
    "v_fmac_f32 v0, v128, v144\n\t" \
    "v_fmac_f32 v1, v128, v145\n\t" \
    "v_mfma_f32_16x16x32_bf16 v[36:39], v[84:87], v[72:75], v[36:39]\n\t" \
    "ds_read_b128 v[100:103], %[aa] offset:13312\n\t" \
    "v_fmac_f32 v2, v128, v146\n\t" \
    "v_fmac_f32 v3, v128, v147\n\t" \
    "v_mfma_f32_16x16x32_bf16 v[40:43], v[88:91], v[72:75], v[40:43]\n\t" \
    "ds_read_b128 v[104:107], %[aa] offset:21504\n\t" \
    "v_fmac_f32 v4, v128, v148\n\t" \
    "v_fmac_f32 v5, v128, v149\n\t" \
    "v_mfma_f32_16x16x32_bf16 v[44:47], v[92:95], v[72:75], v[44:47]\n\t" \
    "ds_read_b128 v[108:111], %[aa] offset:29696\n\t" \
    "v_fmac_f32 v6, v128, v150\n\t" \
    "v_fmac_f32 v7, v128, v151\n\t" \
    "v_mfma_f32_16x16x32_bf16 v[48:51], v[80:83], v[76:79], v[48:51]\n\t" \
    "v_fmac_f32 v8, v128, v152\n\t" \
    "v_fmac_f32 v9, v128, v153\n\t" \
    "v_mfma_f32_16x16x32_bf16 v[52:55], v[84:87], v[76:79], v[52:55]\n\t" \
    "v_fmac_f32 v10, v128, v154\n\t" \
    "v_fmac_f32 v11, v128, v155\n\t" \
    "v_mfma_f32_16x16x32_bf16 v[56:59], v[88:91], v[76:79], v[56:59]\n\t" \
    "v_fmac_f32 v12, v128, v156\n\t" \
    "v_fmac_f32 v13, v128, v157\n\t" \
    "v_mfma_f32_16x16x32_bf16 v[60:63], v[92:95], v[76:79], v[60:63]\n\t" \
    "v_fmac_f32 v14, v128, v158\n\t" \
    "v_fmac_f32 v15, v128, v159\n\t" \
    "s_waitcnt lgkmcnt(0)\n\t" \
    "s_barrier\n\t" \
    "s_cmp_eq_u32 %[wm], 0\n\t" \
    "s_cbranch_scc1 .Lk4p%=\n\t" \
    "s_setprio 2\n\t" \
    ".Lk4p%=:\n\t" \
    "v_mfma_f32_16x16x32_bf16 v[0:3], v[112:115], v[96:99], v[0:3]\n\t" \
    "ds_read_b64_tr_b16 v[80:81], %[b0] offset:8192\n\t" \
    "v_mfma_f32_16x16x32_bf16 v[4:7], v[116:119], v[96:99], v[4:7]\n\t" \
    "ds_read_b64_tr_b16 v[82:83], %[b0] offset:10240\n\t" \
    "v_mfma_f32_16x16x32_bf16 v[8:11], v[120:123], v[96:99], v[8:11]\n\t" \
    "ds_read_b64_tr_b16 v[84:85], %[b1] offset:8192\n\t" \
    "v_mfma_f32_16x16x32_bf16 v[12:15], v[124:127], v[96:99], v[12:15]\n\t" \
    "ds_read_b64_tr_b16 v[86:87], %[b1] offset:10240\n\t" \
    "v_mfma_f32_16x16x32_bf16 v[16:19], v[112:115], v[100:103], v[16:19]\n\t" \
    "ds_read_b64_tr_b16 v[88:89], %[b0] offset:32768\n\t" \
    "v_fmac_f32 v48, v140, v144\n\t" \
    "v_fmac_f32 v49, v140, v145\n\t" \
    "v_mfma_f32_16x16x32_bf16 v[20:23], v[116:119], v[100:103], v[20:23]\n\t" \
    "ds_read_b64_tr_b16 v[90:91], %[b0] offset:34816\n\t" \
    "v_fmac_f32 v50, v140, v146\n\t" \
    "v_fmac_f32 v51, v140, v147\n\t" \
    "v_mfma_f32_16x16x32_bf16 v[24:27], v[120:123], v[100:103], v[24:27]\n\t" \
    "ds_read_b64_tr_b16 v[92:93], %[b1] offset:32768\n\t" \
    "v_fmac_f32 v52, v140, v148\n\t" \
    "v_fmac_f32 v53, v140, v149\n\t" \
    "v_mfma_f32_16x16x32_bf16 v[28:31], v[124:127], v[100:103], v[28:31]\n\t" \
    "ds_read_b64_tr_b16 v[94:95], %[b1] offset:34816\n\t" \
    "v_fmac_f32 v54, v140, v150\n\t" \
    "v_fmac_f32 v55, v140, v151\n\t" \
    "v_mfma_f32_16x16x32_bf16 v[32:35], v[112:115], v[104:107], v[32:35]\n\t" \
    "ds_read_b128 v[64:67], %[aa] offset:6144\n\t" \
    "v_fmac_f32 v56, v140, v152\n\t" \
    "v_fmac_f32 v57, v140, v153\n\t" \
    "v_mfma_f32_16x16x32_bf16 v[36:39], v[116:119], v[104:107], v[36:39]\n\t" \
    "ds_read_b128 v[68:71], %[aa] offset:14336\n\t" \
    "v_fmac_f32 v58, v140, v154\n\t" \
    "v_fmac_f32 v59, v140, v155\n\t" \
    "v_mfma_f32_16x16x32_bf16 v[40:43], v[120:123], v[104:107], v[40:43]\n\t" \
    "ds_read_b128 v[72:75], %[aa] offset:22528\n\t" \
    "v_fmac_f32 v60, v140, v156\n\t" \
    "v_fmac_f32 v61, v140, v157\n\t" \
    "v_mfma_f32_16x16x32_bf16 v[44:47], v[124:127], v[104:107], v[44:47]\n\t" \
    "ds_read_b128 v[76:79], %[aa] offset:30720\n\t" \
    "v_fmac_f32 v62, v140, v158\n\t" \
    "v_fmac_f32 v63, v140, v159\n\t" \
    "v_mfma_f32_16x16x32_bf16 v[48:51], v[112:115], v[108:111], v[48:51]\n\t" \
    "v_fmac_f32 v16, v132, v144\n\t" \
    "v_fmac_f32 v17, v132, v145\n\t" \
    "v_mfma_f32_16x16x32_bf16 v[52:55], v[116:119], v[108:111], v[52:55]\n\t" \
    "v_fmac_f32 v18, v132, v146\n\t" \
    "v_fmac_f32 v19, v132, v147\n\t" \
    "v_mfma_f32_16x16x32_bf16 v[56:59], v[120:123], v[108:111], v[56:59]\n\t" \
    "v_fmac_f32 v20, v132, v148\n\t" \
    "v_fmac_f32 v21, v132, v149\n\t" \
    "v_mfma_f32_16x16x32_bf16 v[60:63], v[124:127], v[108:111], v[60:63]\n\t" \
    "v_fmac_f32 v22, v132, v150\n\t" \
    "v_fmac_f32 v23, v132, v151\n\t" \
    "s_waitcnt lgkmcnt(0)\n\t" \
    "v_mfma_f32_16x16x32_bf16 v[0:3], v[80:83], v[64:67], v[0:3]\n\t" \
    "ds_read_b64_tr_b16 v[112:113], %[b0] offset:12288\n\t" \
    "v_fmac_f32 v24, v132, v152\n\t" \
    "v_fmac_f32 v25, v132, v153\n\t" \
    "v_mfma_f32_16x16x32_bf16 v[4:7], v[84:87], v[64:67], v[4:7]\n\t" \
    "ds_read_b64_tr_b16 v[114:115], %[b0] offset:14336\n\t" \
    "v_fmac_f32 v26, v132, v154\n\t" \
    "v_fmac_f32 v27, v132, v155\n\t" \
    "v_mfma_f32_16x16x32_bf16 v[8:11], v[88:91], v[64:67], v[8:11]\n\t" \
    "ds_read_b64_tr_b16 v[116:117], %[b1] offset:12288\n\t" \
    "v_fmac_f32 v28, v132, v156\n\t" \
    "v_fmac_f32 v29, v132, v157\n\t" \
    "v_mfma_f32_16x16x32_bf16 v[12:15], v[92:95], v[64:67], v[12:15]\n\t" \
    "ds_read_b64_tr_b16 v[118:119], %[b1] offset:14336\n\t" \
    "v_fmac_f32 v30, v132, v158\n\t" \
    "v_fmac_f32 v31, v132, v159\n\t" \
    "v_mfma_f32_16x16x32_bf16 v[16:19], v[80:83], v[68:71], v[16:19]\n\t" \
    "ds_read_b64_tr_b16 v[120:121], %[b0] offset:36864\n\t" \
    "v_mfma_f32_16x16x32_bf16 v[20:23], v[84:87], v[68:71], v[20:23]\n\t" \
    "ds_read_b64_tr_b16 v[122:123], %[b0] offset:38912\n\t" \
    "v_mfma_f32_16x16x32_bf16 v[24:27], v[88:91], v[68:71], v[24:27]\n\t" \
    "ds_read_b64_tr_b16 v[124:125], %[b1] offset:36864\n\t" \
    "v_mfma_f32_16x16x32_bf16 v[28:31], v[92:95], v[68:71], v[28:31]\n\t" \
    "ds_read_b64_tr_b16 v[126:127], %[b1] offset:38912\n\t" \
    "v_mfma_f32_16x16x32_bf16 v[32:35], v[80:83], v[72:75], v[32:35]\n\t" \
    "ds_read_b128 v[96:99], %[aa] offset:7168\n\t" \
    "v_mfma_f32_16x16x32_bf16 v[36:39], v[84:87], v[72:75], v[36:39]\n\t" \
    "ds_read_b128 v[100:103], %[aa] offset:15360\n\t" \
    "v_mfma_f32_16x16x32_bf16 v[40:43], v[88:91], v[72:75], v[40:43]\n\t" \
    "ds_read_b128 v[104:107], %[aa] offset:23552\n\t" \
    "v_mfma_f32_16x16x32_bf16 v[44:47], v[92:95], v[72:75], v[44:47]\n\t" \
    "ds_read_b128 v[108:111], %[aa] offset:31744\n\t" \
    "v_mfma_f32_16x16x32_bf16 v[48:51], v[80:83], v[76:79], v[48:51]\n\t" \
    "v_mfma_f32_16x16x32_bf16 v[52:55], v[84:87], v[76:79], v[52:55]\n\t" \
    "v_mfma_f32_16x16x32_bf16 v[56:59], v[88:91], v[76:79], v[56:59]\n\t" \
    "v_mfma_f32_16x16x32_bf16 v[60:63], v[92:95], v[76:79], v[60:63]\n\t" \
    "s_waitcnt lgkmcnt(0)\n\t" \
    "v_mfma_f32_16x16x32_bf16 v[0:3], v[112:115], v[96:99], v[0:3]\n\t" \
    "v_mfma_f32_16x16x32_bf16 v[4:7], v[116:119], v[96:99], v[4:7]\n\t" \
    "v_mfma_f32_16x16x32_bf16 v[8:11], v[120:123], v[96:99], v[8:11]\n\t" \
    "v_mfma_f32_16x16x32_bf16 v[12:15], v[124:127], v[96:99], v[12:15]\n\t" \
    "v_mfma_f32_16x16x32_bf16 v[16:19], v[112:115], v[100:103], v[16:19]\n\t" \
    "v_mfma_f32_16x16x32_bf16 v[20:23], v[116:119], v[100:103], v[20:23]\n\t" \
    "v_mfma_f32_16x16x32_bf16 v[24:27], v[120:123], v[100:103], v[24:27]\n\t" \
    "v_mfma_f32_16x16x32_bf16 v[28:31], v[124:127], v[100:103], v[28:31]\n\t" \
    "v_mfma_f32_16x16x32_bf16 v[32:35], v[112:115], v[104:107], v[32:35]\n\t" \
    "v_mfma_f32_16x16x32_bf16 v[36:39], v[116:119], v[104:107], v[36:39]\n\t" \
    "v_mfma_f32_16x16x32_bf16 v[40:43], v[120:123], v[104:107], v[40:43]\n\t" \
    "v_mfma_f32_16x16x32_bf16 v[44:47], v[124:127], v[104:107], v[44:47]\n\t" \
    "v_mfma_f32_16x16x32_bf16 v[48:51], v[112:115], v[108:111], v[48:51]\n\t" \
    "v_mfma_f32_16x16x32_bf16 v[52:55], v[116:119], v[108:111], v[52:55]\n\t" \
    "v_mfma_f32_16x16x32_bf16 v[56:59], v[120:123], v[108:111], v[56:59]\n\t" \
    "v_mfma_f32_16x16x32_bf16 v[60:63], v[124:127], v[108:111], v[60:63]\n\t" \
    "s_nop 7\n\t" \
    "s_nop 3\n\t" \
    "s_setprio 0\n\t"
#define K4_CLOB "v64", "v65", "v66", "v67", "v68", "v69", "v70", "v71", "v72", "v73", "v74", "v75", "v76", "v77", "v78", "v79", "v80", "v81", "v82", "v83", "v84", "v85", "v86", "v87", "v88", "v89", "v90", "v91", "v92", "v93", "v94", "v95", "v96", "v97", "v98", "v99", "v100", "v101", "v102", "v103", "v104", "v105", "v106", "v107", "v108", "v109", "v110", "v111", "v112", "v113", "v114", "v115", "v116", "v117", "v118", "v119", "v120", "v121", "v122", "v123", "v124", "v125", "v126", "v127", "v144", "v145", "v146", "v147", "v148", "v149", "v150", "v151", "v152", "v153", "v154", "v155", "v156", "v157", "v158", "v159"
#define K1_KG ((rt ^ ct) & 3) * 2
#define K1_PROD \
    "s_cmp_gt_u32 %[w], 3\n\t" \
    "s_cbranch_scc1 .Lcons%=\n\t" \
    "s_add_u32 m0, %[la], 0\n\t" \
    "s_nop 0\n\t" \
    "global_load_lds_dwordx4 %[va0], %[sa] offset:0\n\t" \
    "s_add_u32 m0, %[la], 1024\n\t" \
    "s_nop 0\n\t" \
    "global_load_lds_dwordx4 %[va1], %[sa] offset:0\n\t" \
    "s_xor_b32 %[st], %[kg], 0\n\t" \
    "s_mul_i32 %[st], %[st], 0x30000\n\t" \
    "s_add_u32 m0, %[lb], 0\n\t" \
    "v_add_u32 %[vb], %[st], %[vb0]\n\t" \
    "global_load_lds_dwordx4 %[vb], %[sb] offset:0\n\t" \
    "s_add_u32 m0, %[lb], 3072\n\t" \
    "s_nop 0\n\t" \
    "global_load_lds_dwordx4 %[vb], %[sb] offset:1024\n\t" \
    "s_add_u32 m0, %[lb], 6144\n\t" \
    "s_nop 0\n\t" \
    "global_load_lds_dwordx4 %[vb], %[sb] offset:2048\n\t" \
    "s_add_u32 m0, %[lb], 12288\n\t" \
    "v_add_u32 %[vb], 0x18000, %[vb]\n\t" \
    "global_load_lds_dwordx4 %[vb], %[sb] offset:0\n\t" \
    "s_add_u32 m0, %[lb], 15360\n\t" \
    "s_nop 0\n\t" \
    "global_load_lds_dwordx4 %[vb], %[sb] offset:1024\n\t" \
    "s_add_u32 m0, %[lb], 18432\n\t" \
    "s_nop 0\n\t" \
    "global_load_lds_dwordx4 %[vb], %[sb] offset:2048\n\t" \
    "s_add_u32 m0, %[la], 8192\n\t" \
    "v_xor_b32 %[va0], 128, %[va0]\n\t" \
    "global_load_lds_dwordx4 %[va0], %[sa] offset:0\n\t" \
    "s_add_u32 m0, %[la], 9216\n\t" \
    "v_xor_b32 %[va1], 128, %[va1]\n\t" \
    "global_load_lds_dwordx4 %[va1], %[sa] offset:0\n\t" \
    "s_xor_b32 %[st], %[kg], 1\n\t" \
    "s_mul_i32 %[st], %[st], 0x30000\n\t" \
    "s_add_u32 m0, %[lb], 24576\n\t" \
    "v_add_u32 %[vb], %[st], %[vb0]\n\t" \
    "global_load_lds_dwordx4 %[vb], %[sb] offset:0\n\t" \
    "s_add_u32 m0, %[lb], 27648\n\t" \
    "s_nop 0\n\t" \
    "global_load_lds_dwordx4 %[vb], %[sb] offset:1024\n\t" \
    "s_add_u32 m0, %[lb], 30720\n\t" \
    "s_nop 0\n\t" \
    "global_load_lds_dwordx4 %[vb], %[sb] offset:2048\n\t" \
    "s_add_u32 m0, %[lb], 36864\n\t" \
    "v_add_u32 %[vb], 0x18000, %[vb]\n\t" \
    "global_load_lds_dwordx4 %[vb], %[sb] offset:0\n\t" \
    "s_add_u32 m0, %[lb], 39936\n\t" \
    "s_nop 0\n\t" \
    "global_load_lds_dwordx4 %[vb], %[sb] offset:1024\n\t" \
    "s_add_u32 m0, %[lb], 43008\n\t" \
    "s_nop 0\n\t" \
    "global_load_lds_dwordx4 %[vb], %[sb] offset:2048\n\t" \
    "s_add_u32 m0, %[la], 16384\n\t" \
    "v_xor_b32 %[va0], 384, %[va0]\n\t" \
    "global_load_lds_dwordx4 %[va0], %[sa] offset:0\n\t" \
    "s_waitcnt vmcnt(12)\n\t" \
    "s_barrier\n\t" \
    "s_add_u32 m0, %[la], 17408\n\t" \
    "v_xor_b32 %[va1], 384, %[va1]\n\t" \
    "global_load_lds_dwordx4 %[va1], %[sa] offset:0\n\t" \
    "s_xor_b32 %[st], %[kg], 2\n\t" \
    "s_mul_i32 %[st], %[st], 0x30000\n\t" \
    "s_add_u32 m0, %[lb], 49152\n\t" \
    "v_add_u32 %[vb], %[st], %[vb0]\n\t" \
    "global_load_lds_dwordx4 %[vb], %[sb] offset:0\n\t" \
    "s_add_u32 m0, %[lb], 52224\n\t" \
    "s_nop 0\n\t" \
    "global_load_lds_dwordx4 %[vb], %[sb] offset:1024\n\t" \
    "s_waitcnt vmcnt(12)\n\t" \
    "s_barrier\n\t" \
    "s_add_u32 m0, %[lb], 55296\n\t" \
    "s_nop 0\n\t" \
    "global_load_lds_dwordx4 %[vb], %[sb] offset:2048\n\t" \
    "s_add_u32 m0, %[lb], 61440\n\t" \
    "v_add_u32 %[vb], 0x18000, %[vb]\n\t" \
    "global_load_lds_dwordx4 %[vb], %[sb] offset:0\n\t" \
    "s_add_u32 m0, %[lb], 64512\n\t" \
    "s_nop 0\n\t" \
    "global_load_lds_dwordx4 %[vb], %[sb] offset:1024\n\t" \
    "s_add_u32 m0, %[lb], 67584\n\t" \
    "s_nop 0\n\t" \
    "global_load_lds_dwordx4 %[vb], %[sb] offset:2048\n\t" \
    "s_add_u32 m0, %[la], 24576\n\t" \
    "v_xor_b32 %[va0], 128, %[va0]\n\t" \
    "global_load_lds_dwordx4 %[va0], %[sa] offset:0\n\t" \
    "s_waitcnt vmcnt(12)\n\t" \
    "s_barrier\n\t" \
    "s_add_u32 m0, %[la], 25600\n\t" \
    "v_xor_b32 %[va1], 128, %[va1]\n\t" \
    "global_load_lds_dwordx4 %[va1], %[sa] offset:0\n\t" \
    "s_xor_b32 %[st], %[kg], 3\n\t" \
    "s_mul_i32 %[st], %[st], 0x30000\n\t" \
    "s_add_u32 m0, %[lb], 73728\n\t" \
    "v_add_u32 %[vb], %[st], %[vb0]\n\t" \
    "global_load_lds_dwordx4 %[vb], %[sb] offset:0\n\t" \
    "s_add_u32 m0, %[lb], 76800\n\t" \
    "s_nop 0\n\t" \
    "global_load_lds_dwordx4 %[vb], %[sb] offset:1024\n\t" \
    "s_waitcnt vmcnt(12)\n\t" \
    "s_barrier\n\t" \
    "s_add_u32 m0, %[lb], 79872\n\t" \
    "s_nop 0\n\t" \
    "global_load_lds_dwordx4 %[vb], %[sb] offset:2048\n\t" \
    "s_add_u32 m0, %[lb], 86016\n\t" \
    "v_add_u32 %[vb], 0x18000, %[vb]\n\t" \
    "global_load_lds_dwordx4 %[vb], %[sb] offset:0\n\t" \
    "s_add_u32 m0, %[lb], 89088\n\t" \
    "s_nop 0\n\t" \
    "global_load_lds_dwordx4 %[vb], %[sb] offset:1024\n\t" \
    "s_add_u32 m0, %[lb], 92160\n\t" \
    "s_nop 0\n\t" \
    "global_load_lds_dwordx4 %[vb], %[sb] offset:2048\n\t" \
    "s_add_u32 m0, %[la], 32768\n\t" \
    "v_xor_b32 %[va0], 896, %[va0]\n\t" \
    "global_load_lds_dwordx4 %[va0], %[sa] offset:0\n\t" \
    "s_add_u32 m0, %[la], 33792\n\t" \
    "v_xor_b32 %[va1], 896, %[va1]\n\t" \
    "global_load_lds_dwordx4 %[va1], %[sa] offset:0\n\t" \
    "s_add_u32 m0, %[la], 40960\n\t" \
    "v_xor_b32 %[va0], 128, %[va0]\n\t" \
    "global_load_lds_dwordx4 %[va0], %[sa] offset:0\n\t" \
    "s_add_u32 m0, %[la], 41984\n\t" \
    "v_xor_b32 %[va1], 128, %[va1]\n\t" \
    "global_load_lds_dwordx4 %[va1], %[sa] offset:0\n\t" \
    "s_waitcnt vmcnt(12)\n\t" \
    "s_barrier\n\t" \
    "s_add_u32 m0, %[la], 49152\n\t" \
    "v_xor_b32 %[va0], 384, %[va0]\n\t" \
    "global_load_lds_dwordx4 %[va0], %[sa] offset:0\n\t" \
    "s_add_u32 m0, %[la], 50176\n\t" \
    "v_xor_b32 %[va1], 384, %[va1]\n\t" \
    "global_load_lds_dwordx4 %[va1], %[sa] offset:0\n\t" \
    "s_add_u32 m0, %[la], 57344\n\t" \
    "v_xor_b32 %[va0], 128, %[va0]\n\t" \
    "global_load_lds_dwordx4 %[va0], %[sa] offset:0\n\t" \
    "s_add_u32 m0, %[la], 58368\n\t" \
    "v_xor_b32 %[va1], 128, %[va1]\n\t" \
    "global_load_lds_dwordx4 %[va1], %[sa] offset:0\n\t" \
    "s_xor_b32 %[st], %[kg], 4\n\t" \
    "s_mul_i32 %[st], %[st], 0x30000\n\t" \
    "s_add_u32 m0, %[lb], 0\n\t" \
    "v_add_u32 %[vb], %[st], %[vb0]\n\t" \
    "global_load_lds_dwordx4 %[vb], %[sb] offset:0\n\t" \
    "s_add_u32 m0, %[lb], 3072\n\t" \
    "s_nop 0\n\t" \
    "global_load_lds_dwordx4 %[vb], %[sb] offset:1024\n\t" \
    "s_add_u32 m0, %[lb], 6144\n\t" \
    "s_nop 0\n\t" \
    "global_load_lds_dwordx4 %[vb], %[sb] offset:2048\n\t" \
    "s_add_u32 m0, %[lb], 12288\n\t" \
    "v_add_u32 %[vb], 0x18000, %[vb]\n\t" \
    "global_load_lds_dwordx4 %[vb], %[sb] offset:0\n\t" \
    "s_waitcnt vmcnt(12)\n\t" \
    "s_barrier\n\t" \
    "s_add_u32 m0, %[lb], 15360\n\t" \
    "s_nop 0\n\t" \
    "global_load_lds_dwordx4 %[vb], %[sb] offset:1024\n\t" \
    "s_add_u32 m0, %[lb], 18432\n\t" \
    "s_nop 0\n\t" \
    "global_load_lds_dwordx4 %[vb], %[sb] offset:2048\n\t" \
    "s_xor_b32 %[st], %[kg], 5\n\t" \
    "s_mul_i32 %[st], %[st], 0x30000\n\t" \
    "s_add_u32 m0, %[lb], 24576\n\t" \
    "v_add_u32 %[vb], %[st], %[vb0]\n\t" \
    "global_load_lds_dwordx4 %[vb], %[sb] offset:0\n\t" \
    "s_add_u32 m0, %[lb], 27648\n\t" \
    "s_nop 0\n\t" \
    "global_load_lds_dwordx4 %[vb], %[sb] offset:1024\n\t" \
    "s_add_u32 m0, %[lb], 30720\n\t" \
    "s_nop 0\n\t" \
    "global_load_lds_dwordx4 %[vb], %[sb] offset:2048\n\t" \
    "s_add_u32 m0, %[lb], 36864\n\t" \
    "v_add_u32 %[vb], 0x18000, %[vb]\n\t" \
    "global_load_lds_dwordx4 %[vb], %[sb] offset:0\n\t" \
    "s_add_u32 m0, %[lb], 39936\n\t" \
    "s_nop 0\n\t" \
    "global_load_lds_dwordx4 %[vb], %[sb] offset:1024\n\t" \
    "s_add_u32 m0, %[lb], 43008\n\t" \
    "s_nop 0\n\t" \
    "global_load_lds_dwordx4 %[vb], %[sb] offset:2048\n\t" \
    "s_xor_b32 %[st], %[kg], 6\n\t" \
    "s_mul_i32 %[st], %[st], 0x30000\n\t" \
    "s_add_u32 m0, %[lb], 49152\n\t" \
    "v_add_u32 %[vb], %[st], %[vb0]\n\t" \
    "global_load_lds_dwordx4 %[vb], %[sb] offset:0\n\t" \
    "s_add_u32 m0, %[lb], 52224\n\t" \
    "s_nop 0\n\t" \
    "global_load_lds_dwordx4 %[vb], %[sb] offset:1024\n\t" \
    "s_add_u32 m0, %[lb], 55296\n\t" \
    "s_nop 0\n\t" \
    "global_load_lds_dwordx4 %[vb], %[sb] offset:2048\n\t" \
    "s_add_u32 m0, %[lb], 61440\n\t" \
    "v_add_u32 %[vb], 0x18000, %[vb]\n\t" \
    "global_load_lds_dwordx4 %[vb], %[sb] offset:0\n\t" \
    "s_add_u32 m0, %[lb], 64512\n\t" \
    "s_nop 0\n\t" \
    "global_load_lds_dwordx4 %[vb], %[sb] offset:1024\n\t" \
    "s_add_u32 m0, %[lb], 67584\n\t" \
    "s_nop 0\n\t" \
    "global_load_lds_dwordx4 %[vb], %[sb] offset:2048\n\t" \
    "v_mov_b32 v104, 0x3f803f80\n\t" \
    "v_mov_b32 v105, 0x3f803f80\n\t" \
    "v_mov_b32 v106, 0x3f803f80\n\t" \
    "v_mov_b32 v107, 0x3f803f80\n\t" \
    "ds_read_b128 v[64:67], %[p0] offset:0\n\t" \
    "ds_read_b128 v[68:71], %[p1] offset:0\n\t" \
    "ds_read_b128 v[72:75], %[p0] offset:8192\n\t" \
    "ds_read_b128 v[76:79], %[p1] offset:8192\n\t" \
    "ds_read_b128 v[80:83], %[p0] offset:16384\n\t" \
    "ds_read_b128 v[84:87], %[p1] offset:16384\n\t" \
    "ds_read_b128 v[88:91], %[p0] offset:24576\n\t" \
    "ds_read_b128 v[92:95], %[p1] offset:24576\n\t" \
    "s_waitcnt lgkmcnt(7)\n\t" \
    "v_mfma_f32_16x16x32_bf16 v[96:99], v[104:107], v[64:67], 0\n\t" \
    "v_mfma_f32_16x16x32_bf16 v[100:103], v[64:67], v[64:67], 0\n\t" \
    "s_waitcnt lgkmcnt(6)\n\t" \
    "v_mfma_f32_16x16x32_bf16 v[96:99], v[104:107], v[68:71], v[96:99]\n\t" \
    "v_mfma_f32_16x16x32_bf16 v[100:103], v[68:71], v[68:71], v[100:103]\n\t" \
    "s_waitcnt lgkmcnt(5)\n\t" \
    "v_mfma_f32_16x16x32_bf16 v[96:99], v[104:107], v[72:75], v[96:99]\n\t" \
    "v_mfma_f32_16x16x32_bf16 v[100:103], v[72:75], v[72:75], v[100:103]\n\t" \
    "s_waitcnt lgkmcnt(4)\n\t" \
    "v_mfma_f32_16x16x32_bf16 v[96:99], v[104:107], v[76:79], v[96:99]\n\t" \
    "v_mfma_f32_16x16x32_bf16 v[100:103], v[76:79], v[76:79], v[100:103]\n\t" \
    "s_waitcnt lgkmcnt(3)\n\t" \
    "v_mfma_f32_16x16x32_bf16 v[96:99], v[104:107], v[80:83], v[96:99]\n\t" \
    "v_mfma_f32_16x16x32_bf16 v[100:103], v[80:83], v[80:83], v[100:103]\n\t" \
    "s_waitcnt lgkmcnt(2)\n\t" \
    "v_mfma_f32_16x16x32_bf16 v[96:99], v[104:107], v[84:87], v[96:99]\n\t" \
    "v_mfma_f32_16x16x32_bf16 v[100:103], v[84:87], v[84:87], v[100:103]\n\t" \
    "s_waitcnt lgkmcnt(1)\n\t" \
    "v_mfma_f32_16x16x32_bf16 v[96:99], v[104:107], v[88:91], v[96:99]\n\t" \
    "v_mfma_f32_16x16x32_bf16 v[100:103], v[88:91], v[88:91], v[100:103]\n\t" \
    "s_waitcnt lgkmcnt(0)\n\t" \
    "v_mfma_f32_16x16x32_bf16 v[96:99], v[104:107], v[92:95], v[96:99]\n\t" \
    "v_mfma_f32_16x16x32_bf16 v[100:103], v[92:95], v[92:95], v[100:103]\n\t" \
    "s_waitcnt vmcnt(12)\n\t" \
    "s_barrier\n\t" \
    "s_xor_b32 %[st], %[kg], 7\n\t" \
    "s_mul_i32 %[st], %[st], 0x30000\n\t" \
    "s_add_u32 m0, %[lb], 73728\n\t" \
    "v_add_u32 %[vb], %[st], %[vb0]\n\t" \
    "global_load_lds_dwordx4 %[vb], %[sb] offset:0\n\t" \
    "s_add_u32 m0, %[lb], 76800\n\t" \
    "s_nop 0\n\t" \
    "global_load_lds_dwordx4 %[vb], %[sb] offset:1024\n\t" \
    "s_add_u32 m0, %[lb], 79872\n\t" \
    "s_nop 0\n\t" \
    "global_load_lds_dwordx4 %[vb], %[sb] offset:2048\n\t" \
    "s_add_u32 m0, %[lb], 86016\n\t" \
    "v_add_u32 %[vb], 0x18000, %[vb]\n\t" \
    "global_load_lds_dwordx4 %[vb], %[sb] offset:0\n\t" \
    "s_add_u32 m0, %[lb], 89088\n\t" \
    "s_nop 0\n\t" \
    "global_load_lds_dwordx4 %[vb], %[sb] offset:1024\n\t" \
    "s_add_u32 m0, %[lb], 92160\n\t" \
    "s_nop 0\n\t" \
    "global_load_lds_dwordx4 %[vb], %[sb] offset:2048\n\t" \
    "ds_read_b128 v[64:67], %[p0] offset:32768\n\t" \
    "ds_read_b128 v[68:71], %[p1] offset:32768\n\t" \
    "ds_read_b128 v[72:75], %[p0] offset:40960\n\t" \
    "ds_read_b128 v[76:79], %[p1] offset:40960\n\t" \
    "ds_read_b128 v[80:83], %[p0] offset:49152\n\t" \
    "ds_read_b128 v[84:87], %[p1] offset:49152\n\t" \
    "ds_read_b128 v[88:91], %[p0] offset:57344\n\t" \
    "ds_read_b128 v[92:95], %[p1] offset:57344\n\t" \
    "s_waitcnt lgkmcnt(7)\n\t" \
    "v_mfma_f32_16x16x32_bf16 v[96:99], v[104:107], v[64:67], v[96:99]\n\t" \
    "v_mfma_f32_16x16x32_bf16 v[100:103], v[64:67], v[64:67], v[100:103]\n\t" \
    "s_waitcnt lgkmcnt(6)\n\t" \
    "v_mfma_f32_16x16x32_bf16 v[96:99], v[104:107], v[68:71], v[96:99]\n\t" \
    "v_mfma_f32_16x16x32_bf16 v[100:103], v[68:71], v[68:71], v[100:103]\n\t" \
    "s_waitcnt lgkmcnt(5)\n\t" \
    "v_mfma_f32_16x16x32_bf16 v[96:99], v[104:107], v[72:75], v[96:99]\n\t" \
    "v_mfma_f32_16x16x32_bf16 v[100:103], v[72:75], v[72:75], v[100:103]\n\t" \
    "s_waitcnt lgkmcnt(4)\n\t" \
    "v_mfma_f32_16x16x32_bf16 v[96:99], v[104:107], v[76:79], v[96:99]\n\t" \
    "v_mfma_f32_16x16x32_bf16 v[100:103], v[76:79], v[76:79], v[100:103]\n\t" \
    "s_waitcnt lgkmcnt(3)\n\t" \
    "v_mfma_f32_16x16x32_bf16 v[96:99], v[104:107], v[80:83], v[96:99]\n\t" \
    "v_mfma_f32_16x16x32_bf16 v[100:103], v[80:83], v[80:83], v[100:103]\n\t" \
    "s_waitcnt lgkmcnt(2)\n\t" \
    "v_mfma_f32_16x16x32_bf16 v[96:99], v[104:107], v[84:87], v[96:99]\n\t" \
    "v_mfma_f32_16x16x32_bf16 v[100:103], v[84:87], v[84:87], v[100:103]\n\t" \
    "s_waitcnt lgkmcnt(1)\n\t" \
    "v_mfma_f32_16x16x32_bf16 v[96:99], v[104:107], v[88:91], v[96:99]\n\t" \
    "v_mfma_f32_16x16x32_bf16 v[100:103], v[88:91], v[88:91], v[100:103]\n\t" \
    "s_waitcnt lgkmcnt(0)\n\t" \
    "v_mfma_f32_16x16x32_bf16 v[96:99], v[104:107], v[92:95], v[96:99]\n\t" \
    "v_mfma_f32_16x16x32_bf16 v[100:103], v[92:95], v[92:95], v[100:103]\n\t" \
    "s_waitcnt vmcnt(12)\n\t" \
    "s_barrier\n\t" \
    "s_nop 7\n\t" \
    "s_nop 3\n\t" \
    "v_cndmask_b32 v100, v100, v101, %[m1]\n\t" \
    "v_cndmask_b32 v102, v102, v103, %[m1]\n\t" \
    "v_mul_f32 v96, 0x3b000000, v96\n\t" \
    "v_cndmask_b32 v100, v100, v102, %[m2]\n\t" \
    "v_mul_f32 v100, 0x3b000000, v100\n\t" \
    "v_fma_f32 v100, -v96, v96, v100\n\t" \
    "v_max_f32 v100, 0, v100\n\t" \
    "v_add_f32 v100, 0x3727c5ac, v100\n\t" \
    "v_rsq_f32 v100, v100\n\t" \
    "s_mov_b64 exec, %[m3]\n\t" \
    "s_nop 0\n\t" \
    "ds_write2st64_b32 %[ws], v96, v100 offset0:0 offset1:1\n\t" \
    "s_mov_b64 exec, -1\n\t" \
    "s_waitcnt lgkmcnt(0)\n\t" \
    "s_waitcnt vmcnt(6)\n\t" \
    "s_barrier\n\t" \
    "s_waitcnt vmcnt(3)\n\t" \
    "s_barrier\n\t" \
    "s_waitcnt vmcnt(0)\n\t" \
    "s_barrier\n\t" \
    "s_waitcnt vmcnt(0)\n\t" \
    "s_endpgm\n\t" \
    ".Lcons%=:\n\t"
#define K1P_CLOB "v64", "v65", "v66", "v67", "v68", "v69", "v70", "v71", "v72", "v73", "v74", "v75", "v76", "v77", "v78", "v79", "v80", "v81", "v82", "v83", "v84", "v85", "v86", "v87", "v88", "v89", "v90", "v91", "v92", "v93", "v94", "v95", "v96", "v97", "v98", "v99", "v100", "v101", "v102", "v103", "v104", "v105", "v106", "v107"
#define K1_CONS \
    "v_mov_b32 v68, 0x3f803f80\n\t" \
    "v_mov_b32 v69, 0x3f803f80\n\t" \
    "v_mov_b32 v70, 0x3f803f80\n\t" \
    "v_mov_b32 v71, 0x3f803f80\n\t" \
    "ds_read_b64_tr_b16 v[88:89], %[b0] offset:0\n\t" \
    "ds_read_b64_tr_b16 v[90:91], %[b0] offset:512\n\t" \
    "ds_read_b64_tr_b16 v[92:93], %[b0] offset:4096\n\t" \
    "ds_read_b64_tr_b16 v[94:95], %[b0] offset:4608\n\t" \
    "ds_read_b64_tr_b16 v[96:97], %[b0] offset:8192\n\t" \
    "ds_read_b64_tr_b16 v[98:99], %[b0] offset:8704\n\t" \
    "ds_read_b128 v[72:75], %[a0] offset:0\n\t" \
    "ds_read_b128 v[76:79], %[a0] offset:2048\n\t" \
    "ds_read_b128 v[80:83], %[a0] offset:4096\n\t" \
    "ds_read_b128 v[84:87], %[a0] offset:6144\n\t" \
    "s_waitcnt lgkmcnt(0)\n\t" \
    "s_barrier\n\t" \
    "v_mfma_f32_16x16x32_bf16 v[48:51], v[88:91], v[68:71], 0\n\t" \
    "ds_read_b64_tr_b16 v[116:117], %[b0] offset:12288\n\t" \
    "v_mfma_f32_16x16x32_bf16 v[52:55], v[92:95], v[68:71], 0\n\t" \
    "ds_read_b64_tr_b16 v[118:119], %[b0] offset:12800\n\t" \
    "v_mfma_f32_16x16x32_bf16 v[56:59], v[68:71], v[96:99], 0\n\t" \
    "ds_read_b64_tr_b16 v[120:121], %[b0] offset:16384\n\t" \
    "v_mfma_f32_16x16x32_bf16 v[0:3], v[88:91], v[72:75], 0\n\t" \
    "ds_read_b64_tr_b16 v[122:123], %[b0] offset:16896\n\t" \
    "v_mfma_f32_16x16x32_bf16 v[16:19], v[92:95], v[72:75], 0\n\t" \
    "ds_read_b64_tr_b16 v[124:125], %[b0] offset:20480\n\t" \
    "v_mfma_f32_16x16x32_bf16 v[32:35], v[72:75], v[96:99], 0\n\t" \
    "ds_read_b64_tr_b16 v[126:127], %[b0] offset:20992\n\t" \
    "v_mfma_f32_16x16x32_bf16 v[4:7], v[88:91], v[76:79], 0\n\t" \
    "ds_read_b128 v[100:103], %[a1] offset:0\n\t" \
    "v_mfma_f32_16x16x32_bf16 v[20:23], v[92:95], v[76:79], 0\n\t" \
    "ds_read_b128 v[104:107], %[a1] offset:2048\n\t" \
    "v_mfma_f32_16x16x32_bf16 v[36:39], v[76:79], v[96:99], 0\n\t" \
    "ds_read_b128 v[108:111], %[a1] offset:4096\n\t" \
    "v_mfma_f32_16x16x32_bf16 v[8:11], v[88:91], v[80:83], 0\n\t" \
    "ds_read_b128 v[112:115], %[a1] offset:6144\n\t" \
    "v_mfma_f32_16x16x32_bf16 v[24:27], v[92:95], v[80:83], 0\n\t" \
    "v_mfma_f32_16x16x32_bf16 v[40:43], v[80:83], v[96:99], 0\n\t" \
    "v_mfma_f32_16x16x32_bf16 v[12:15], v[88:91], v[84:87], 0\n\t" \
    "v_mfma_f32_16x16x32_bf16 v[28:31], v[92:95], v[84:87], 0\n\t" \
    "v_mfma_f32_16x16x32_bf16 v[44:47], v[84:87], v[96:99], 0\n\t" \
    "s_waitcnt lgkmcnt(0)\n\t" \
    "s_barrier\n\t" \
    "v_mfma_f32_16x16x32_bf16 v[48:51], v[116:119], v[68:71], v[48:51]\n\t" \
    "ds_read_b64_tr_b16 v[88:89], %[b0] offset:24576\n\t" \
    "v_mfma_f32_16x16x32_bf16 v[52:55], v[120:123], v[68:71], v[52:55]\n\t" \
    "ds_read_b64_tr_b16 v[90:91], %[b0] offset:25088\n\t" \
    "v_mfma_f32_16x16x32_bf16 v[56:59], v[68:71], v[124:127], v[56:59]\n\t" \
    "ds_read_b64_tr_b16 v[92:93], %[b0] offset:28672\n\t" \
    "v_mfma_f32_16x16x32_bf16 v[0:3], v[116:119], v[100:103], v[0:3]\n\t" \
    "ds_read_b64_tr_b16 v[94:95], %[b0] offset:29184\n\t" \
    "v_mfma_f32_16x16x32_bf16 v[16:19], v[120:123], v[100:103], v[16:19]\n\t" \
    "ds_read_b64_tr_b16 v[96:97], %[b0] offset:32768\n\t" \
    "v_mfma_f32_16x16x32_bf16 v[32:35], v[100:103], v[124:127], v[32:35]\n\t" \
    "ds_read_b64_tr_b16 v[98:99], %[b0] offset:33280\n\t" \
    "v_mfma_f32_16x16x32_bf16 v[4:7], v[116:119], v[104:107], v[4:7]\n\t" \
    "ds_read_b128 v[72:75], %[a0] offset:8192\n\t" \
    "v_mfma_f32_16x16x32_bf16 v[20:23], v[120:123], v[104:107], v[20:23]\n\t" \
    "ds_read_b128 v[76:79], %[a0] offset:10240\n\t" \
    "v_mfma_f32_16x16x32_bf16 v[36:39], v[104:107], v[124:127], v[36:39]\n\t" \
    "ds_read_b128 v[80:83], %[a0] offset:12288\n\t" \
    "v_mfma_f32_16x16x32_bf16 v[8:11], v[116:119], v[108:111], v[8:11]\n\t" \
    "ds_read_b128 v[84:87], %[a0] offset:14336\n\t" \
    "v_mfma_f32_16x16x32_bf16 v[24:27], v[120:123], v[108:111], v[24:27]\n\t" \
    "v_mfma_f32_16x16x32_bf16 v[40:43], v[108:111], v[124:127], v[40:43]\n\t" \
    "v_mfma_f32_16x16x32_bf16 v[12:15], v[116:119], v[112:115], v[12:15]\n\t" \
    "v_mfma_f32_16x16x32_bf16 v[28:31], v[120:123], v[112:115], v[28:31]\n\t" \
    "v_mfma_f32_16x16x32_bf16 v[44:47], v[112:115], v[124:127], v[44:47]\n\t" \
    "s_waitcnt lgkmcnt(0)\n\t" \
    "s_barrier\n\t" \
    "v_mfma_f32_16x16x32_bf16 v[48:51], v[88:91], v[68:71], v[48:51]\n\t" \
    "ds_read_b64_tr_b16 v[116:117], %[b0] offset:36864\n\t" \
    "v_mfma_f32_16x16x32_bf16 v[52:55], v[92:95], v[68:71], v[52:55]\n\t" \
    "ds_read_b64_tr_b16 v[118:119], %[b0] offset:37376\n\t" \
    "v_mfma_f32_16x16x32_bf16 v[56:59], v[68:71], v[96:99], v[56:59]\n\t" \
    "ds_read_b64_tr_b16 v[120:121], %[b0] offset:40960\n\t" \
    "v_mfma_f32_16x16x32_bf16 v[0:3], v[88:91], v[72:75], v[0:3]\n\t" \
    "ds_read_b64_tr_b16 v[122:123], %[b0] offset:41472\n\t" \
    "v_mfma_f32_16x16x32_bf16 v[16:19], v[92:95], v[72:75], v[16:19]\n\t" \
    "ds_read_b64_tr_b16 v[124:125], %[b0] offset:45056\n\t" \
    "v_mfma_f32_16x16x32_bf16 v[32:35], v[72:75], v[96:99], v[32:35]\n\t" \
    "ds_read_b64_tr_b16 v[126:127], %[b0] offset:45568\n\t" \
    "v_mfma_f32_16x16x32_bf16 v[4:7], v[88:91], v[76:79], v[4:7]\n\t" \
    "ds_read_b128 v[100:103], %[a1] offset:8192\n\t" \
    "v_mfma_f32_16x16x32_bf16 v[20:23], v[92:95], v[76:79], v[20:23]\n\t" \
    "ds_read_b128 v[104:107], %[a1] offset:10240\n\t" \
    "v_mfma_f32_16x16x32_bf16 v[36:39], v[76:79], v[96:99], v[36:39]\n\t" \
    "ds_read_b128 v[108:111], %[a1] offset:12288\n\t" \
    "v_mfma_f32_16x16x32_bf16 v[8:11], v[88:91], v[80:83], v[8:11]\n\t" \
    "ds_read_b128 v[112:115], %[a1] offset:14336\n\t" \
    "v_mfma_f32_16x16x32_bf16 v[24:27], v[92:95], v[80:83], v[24:27]\n\t" \
    "v_mfma_f32_16x16x32_bf16 v[40:43], v[80:83], v[96:99], v[40:43]\n\t" \
    "v_mfma_f32_16x16x32_bf16 v[12:15], v[88:91], v[84:87], v[12:15]\n\t" \
    "v_mfma_f32_16x16x32_bf16 v[28:31], v[92:95], v[84:87], v[28:31]\n\t" \
    "v_mfma_f32_16x16x32_bf16 v[44:47], v[84:87], v[96:99], v[44:47]\n\t" \
    "s_waitcnt lgkmcnt(0)\n\t" \
    "s_barrier\n\t" \
    "v_mfma_f32_16x16x32_bf16 v[48:51], v[116:119], v[68:71], v[48:51]\n\t" \
    "ds_read_b64_tr_b16 v[88:89], %[b1] offset:0\n\t" \
    "v_mfma_f32_16x16x32_bf16 v[52:55], v[120:123], v[68:71], v[52:55]\n\t" \
    "ds_read_b64_tr_b16 v[90:91], %[b1] offset:512\n\t" \
    "v_mfma_f32_16x16x32_bf16 v[56:59], v[68:71], v[124:127], v[56:59]\n\t" \
    "ds_read_b64_tr_b16 v[92:93], %[b1] offset:4096\n\t" \
    "v_mfma_f32_16x16x32_bf16 v[0:3], v[116:119], v[100:103], v[0:3]\n\t" \
    "ds_read_b64_tr_b16 v[94:95], %[b1] offset:4608\n\t" \
    "v_mfma_f32_16x16x32_bf16 v[16:19], v[120:123], v[100:103], v[16:19]\n\t" \
    "ds_read_b64_tr_b16 v[96:97], %[b1] offset:8192\n\t" \
    "v_mfma_f32_16x16x32_bf16 v[32:35], v[100:103], v[124:127], v[32:35]\n\t" \
    "ds_read_b64_tr_b16 v[98:99], %[b1] offset:8704\n\t" \
    "v_mfma_f32_16x16x32_bf16 v[4:7], v[116:119], v[104:107], v[4:7]\n\t" \
    "ds_read_b128 v[72:75], %[a0] offset:16384\n\t" \
    "v_mfma_f32_16x16x32_bf16 v[20:23], v[120:123], v[104:107], v[20:23]\n\t" \
    "ds_read_b128 v[76:79], %[a0] offset:18432\n\t" \
    "v_mfma_f32_16x16x32_bf16 v[36:39], v[104:107], v[124:127], v[36:39]\n\t" \
    "ds_read_b128 v[80:83], %[a0] offset:20480\n\t" \
    "v_mfma_f32_16x16x32_bf16 v[8:11], v[116:119], v[108:111], v[8:11]\n\t" \
    "ds_read_b128 v[84:87], %[a0] offset:22528\n\t" \
    "v_mfma_f32_16x16x32_bf16 v[24:27], v[120:123], v[108:111], v[24:27]\n\t" \
    "v_mfma_f32_16x16x32_bf16 v[40:43], v[108:111], v[124:127], v[40:43]\n\t" \
    "v_mfma_f32_16x16x32_bf16 v[12:15], v[116:119], v[112:115], v[12:15]\n\t" \
    "v_mfma_f32_16x16x32_bf16 v[28:31], v[120:123], v[112:115], v[28:31]\n\t" \
    "v_mfma_f32_16x16x32_bf16 v[44:47], v[112:115], v[124:127], v[44:47]\n\t" \
    "s_waitcnt lgkmcnt(0)\n\t" \
    "v_mfma_f32_16x16x32_bf16 v[48:51], v[88:91], v[68:71], v[48:51]\n\t" \
    "ds_read_b64_tr_b16 v[116:117], %[b1] offset:12288\n\t" \
    "v_mfma_f32_16x16x32_bf16 v[52:55], v[92:95], v[68:71], v[52:55]\n\t" \
    "ds_read_b64_tr_b16 v[118:119], %[b1] offset:12800\n\t" \
    "v_mfma_f32_16x16x32_bf16 v[56:59], v[68:71], v[96:99], v[56:59]\n\t" \
    "ds_read_b64_tr_b16 v[120:121], %[b1] offset:16384\n\t" \
    "v_mfma_f32_16x16x32_bf16 v[0:3], v[88:91], v[72:75], v[0:3]\n\t" \
    "ds_read_b64_tr_b16 v[122:123], %[b1] offset:16896\n\t" \
    "v_mfma_f32_16x16x32_bf16 v[16:19], v[92:95], v[72:75], v[16:19]\n\t" \
    "ds_read_b64_tr_b16 v[124:125], %[b1] offset:20480\n\t" \
    "v_mfma_f32_16x16x32_bf16 v[32:35], v[72:75], v[96:99], v[32:35]\n\t" \
    "ds_read_b64_tr_b16 v[126:127], %[b1] offset:20992\n\t" \
    "v_mfma_f32_16x16x32_bf16 v[4:7], v[88:91], v[76:79], v[4:7]\n\t" \
    "ds_read_b128 v[100:103], %[a1] offset:16384\n\t" \
    "v_mfma_f32_16x16x32_bf16 v[20:23], v[92:95], v[76:79], v[20:23]\n\t" \
    "ds_read_b128 v[104:107], %[a1] offset:18432\n\t" \
    "v_mfma_f32_16x16x32_bf16 v[36:39], v[76:79], v[96:99], v[36:39]\n\t" \
    "ds_read_b128 v[108:111], %[a1] offset:20480\n\t" \
    "v_mfma_f32_16x16x32_bf16 v[8:11], v[88:91], v[80:83], v[8:11]\n\t" \
    "ds_read_b128 v[112:115], %[a1] offset:22528\n\t" \
    "v_mfma_f32_16x16x32_bf16 v[24:27], v[92:95], v[80:83], v[24:27]\n\t" \
    "v_mfma_f32_16x16x32_bf16 v[40:43], v[80:83], v[96:99], v[40:43]\n\t" \
    "v_mfma_f32_16x16x32_bf16 v[12:15], v[88:91], v[84:87], v[12:15]\n\t" \
    "v_mfma_f32_16x16x32_bf16 v[28:31], v[92:95], v[84:87], v[28:31]\n\t" \
    "v_mfma_f32_16x16x32_bf16 v[44:47], v[84:87], v[96:99], v[44:47]\n\t" \
    "s_waitcnt lgkmcnt(0)\n\t" \
    "s_barrier\n\t" \
    "v_mfma_f32_16x16x32_bf16 v[48:51], v[116:119], v[68:71], v[48:51]\n\t" \
    "ds_read_b64_tr_b16 v[88:89], %[b1] offset:24576\n\t" \
    "v_mfma_f32_16x16x32_bf16 v[52:55], v[120:123], v[68:71], v[52:55]\n\t" \
    "ds_read_b64_tr_b16 v[90:91], %[b1] offset:25088\n\t" \
    "v_mfma_f32_16x16x32_bf16 v[56:59], v[68:71], v[124:127], v[56:59]\n\t" \
    "ds_read_b64_tr_b16 v[92:93], %[b1] offset:28672\n\t" \
    "v_mfma_f32_16x16x32_bf16 v[0:3], v[116:119], v[100:103], v[0:3]\n\t" \
    "ds_read_b64_tr_b16 v[94:95], %[b1] offset:29184\n\t" \
    "v_mfma_f32_16x16x32_bf16 v[16:19], v[120:123], v[100:103], v[16:19]\n\t" \
    "ds_read_b64_tr_b16 v[96:97], %[b1] offset:32768\n\t" \
    "v_mfma_f32_16x16x32_bf16 v[32:35], v[100:103], v[124:127], v[32:35]\n\t" \
    "ds_read_b64_tr_b16 v[98:99], %[b1] offset:33280\n\t" \
    "v_mfma_f32_16x16x32_bf16 v[4:7], v[116:119], v[104:107], v[4:7]\n\t" \
    "ds_read_b128 v[72:75], %[a0] offset:24576\n\t" \
    "v_mfma_f32_16x16x32_bf16 v[20:23], v[120:123], v[104:107], v[20:23]\n\t" \
    "ds_read_b128 v[76:79], %[a0] offset:26624\n\t" \
    "v_mfma_f32_16x16x32_bf16 v[36:39], v[104:107], v[124:127], v[36:39]\n\t" \
    "ds_read_b128 v[80:83], %[a0] offset:28672\n\t" \
    "v_mfma_f32_16x16x32_bf16 v[8:11], v[116:119], v[108:111], v[8:11]\n\t" \
    "ds_read_b128 v[84:87], %[a0] offset:30720\n\t" \
    "v_mfma_f32_16x16x32_bf16 v[24:27], v[120:123], v[108:111], v[24:27]\n\t" \
    "v_mfma_f32_16x16x32_bf16 v[40:43], v[108:111], v[124:127], v[40:43]\n\t" \
    "v_mfma_f32_16x16x32_bf16 v[12:15], v[116:119], v[112:115], v[12:15]\n\t" \
    "v_mfma_f32_16x16x32_bf16 v[28:31], v[120:123], v[112:115], v[28:31]\n\t" \
    "v_mfma_f32_16x16x32_bf16 v[44:47], v[112:115], v[124:127], v[44:47]\n\t" \
    "s_waitcnt lgkmcnt(0)\n\t" \
    "v_mfma_f32_16x16x32_bf16 v[48:51], v[88:91], v[68:71], v[48:51]\n\t" \
    "ds_read_b64_tr_b16 v[116:117], %[b1] offset:36864\n\t" \
    "v_mfma_f32_16x16x32_bf16 v[52:55], v[92:95], v[68:71], v[52:55]\n\t" \
    "ds_read_b64_tr_b16 v[118:119], %[b1] offset:37376\n\t" \
    "v_mfma_f32_16x16x32_bf16 v[56:59], v[68:71], v[96:99], v[56:59]\n\t" \
    "ds_read_b64_tr_b16 v[120:121], %[b1] offset:40960\n\t" \
    "v_mfma_f32_16x16x32_bf16 v[0:3], v[88:91], v[72:75], v[0:3]\n\t" \
    "ds_read_b64_tr_b16 v[122:123], %[b1] offset:41472\n\t" \
    "v_mfma_f32_16x16x32_bf16 v[16:19], v[92:95], v[72:75], v[16:19]\n\t" \
    "ds_read_b64_tr_b16 v[124:125], %[b1] offset:45056\n\t" \
    "v_mfma_f32_16x16x32_bf16 v[32:35], v[72:75], v[96:99], v[32:35]\n\t" \
    "ds_read_b64_tr_b16 v[126:127], %[b1] offset:45568\n\t" \
    "v_mfma_f32_16x16x32_bf16 v[4:7], v[88:91], v[76:79], v[4:7]\n\t" \
    "ds_read_b128 v[100:103], %[a1] offset:24576\n\t" \
    "v_mfma_f32_16x16x32_bf16 v[20:23], v[92:95], v[76:79], v[20:23]\n\t" \
    "ds_read_b128 v[104:107], %[a1] offset:26624\n\t" \
    "v_mfma_f32_16x16x32_bf16 v[36:39], v[76:79], v[96:99], v[36:39]\n\t" \
    "ds_read_b128 v[108:111], %[a1] offset:28672\n\t" \
    "v_mfma_f32_16x16x32_bf16 v[8:11], v[88:91], v[80:83], v[8:11]\n\t" \
    "ds_read_b128 v[112:115], %[a1] offset:30720\n\t" \
    "v_mfma_f32_16x16x32_bf16 v[24:27], v[92:95], v[80:83], v[24:27]\n\t" \
    "v_mfma_f32_16x16x32_bf16 v[40:43], v[80:83], v[96:99], v[40:43]\n\t" \
    "v_mfma_f32_16x16x32_bf16 v[12:15], v[88:91], v[84:87], v[12:15]\n\t" \
    "v_mfma_f32_16x16x32_bf16 v[28:31], v[92:95], v[84:87], v[28:31]\n\t" \
    "v_mfma_f32_16x16x32_bf16 v[44:47], v[84:87], v[96:99], v[44:47]\n\t" \
    "s_waitcnt lgkmcnt(0)\n\t" \
    "s_barrier\n\t" \
    "v_mfma_f32_16x16x32_bf16 v[48:51], v[116:119], v[68:71], v[48:51]\n\t" \
    "ds_read_b64_tr_b16 v[88:89], %[b0] offset:0\n\t" \
    "v_mfma_f32_16x16x32_bf16 v[52:55], v[120:123], v[68:71], v[52:55]\n\t" \
    "ds_read_b64_tr_b16 v[90:91], %[b0] offset:512\n\t" \
    "v_mfma_f32_16x16x32_bf16 v[56:59], v[68:71], v[124:127], v[56:59]\n\t" \
    "ds_read_b64_tr_b16 v[92:93], %[b0] offset:4096\n\t" \
    "v_mfma_f32_16x16x32_bf16 v[0:3], v[116:119], v[100:103], v[0:3]\n\t" \
    "ds_read_b64_tr_b16 v[94:95], %[b0] offset:4608\n\t" \
    "v_mfma_f32_16x16x32_bf16 v[16:19], v[120:123], v[100:103], v[16:19]\n\t" \
    "ds_read_b64_tr_b16 v[96:97], %[b0] offset:8192\n\t" \
    "v_mfma_f32_16x16x32_bf16 v[32:35], v[100:103], v[124:127], v[32:35]\n\t" \
    "ds_read_b64_tr_b16 v[98:99], %[b0] offset:8704\n\t" \
    "v_mfma_f32_16x16x32_bf16 v[4:7], v[116:119], v[104:107], v[4:7]\n\t" \
    "ds_read_b128 v[72:75], %[a0] offset:32768\n\t" \
    "v_mfma_f32_16x16x32_bf16 v[20:23], v[120:123], v[104:107], v[20:23]\n\t" \
    "ds_read_b128 v[76:79], %[a0] offset:34816\n\t" \
    "v_mfma_f32_16x16x32_bf16 v[36:39], v[104:107], v[124:127], v[36:39]\n\t" \
    "ds_read_b128 v[80:83], %[a0] offset:36864\n\t" \
    "v_mfma_f32_16x16x32_bf16 v[8:11], v[116:119], v[108:111], v[8:11]\n\t" \
    "ds_read_b128 v[84:87], %[a0] offset:38912\n\t" \
    "v_mfma_f32_16x16x32_bf16 v[24:27], v[120:123], v[108:111], v[24:27]\n\t" \
    "v_mfma_f32_16x16x32_bf16 v[40:43], v[108:111], v[124:127], v[40:43]\n\t" \
    "v_mfma_f32_16x16x32_bf16 v[12:15], v[116:119], v[112:115], v[12:15]\n\t" \
    "v_mfma_f32_16x16x32_bf16 v[28:31], v[120:123], v[112:115], v[28:31]\n\t" \
    "v_mfma_f32_16x16x32_bf16 v[44:47], v[112:115], v[124:127], v[44:47]\n\t" \
    "s_waitcnt lgkmcnt(0)\n\t" \
    "v_mfma_f32_16x16x32_bf16 v[48:51], v[88:91], v[68:71], v[48:51]\n\t" \
    "ds_read_b64_tr_b16 v[116:117], %[b0] offset:12288\n\t" \
    "v_mfma_f32_16x16x32_bf16 v[52:55], v[92:95], v[68:71], v[52:55]\n\t" \
    "ds_read_b64_tr_b16 v[118:119], %[b0] offset:12800\n\t" \
    "v_mfma_f32_16x16x32_bf16 v[56:59], v[68:71], v[96:99], v[56:59]\n\t" \
    "ds_read_b64_tr_b16 v[120:121], %[b0] offset:16384\n\t" \
    "v_mfma_f32_16x16x32_bf16 v[0:3], v[88:91], v[72:75], v[0:3]\n\t" \
    "ds_read_b64_tr_b16 v[122:123], %[b0] offset:16896\n\t" \
    "v_mfma_f32_16x16x32_bf16 v[16:19], v[92:95], v[72:75], v[16:19]\n\t" \
    "ds_read_b64_tr_b16 v[124:125], %[b0] offset:20480\n\t" \
    "v_mfma_f32_16x16x32_bf16 v[32:35], v[72:75], v[96:99], v[32:35]\n\t" \
    "ds_read_b64_tr_b16 v[126:127], %[b0] offset:20992\n\t" \
    "v_mfma_f32_16x16x32_bf16 v[4:7], v[88:91], v[76:79], v[4:7]\n\t" \
    "ds_read_b128 v[100:103], %[a1] offset:32768\n\t" \
    "v_mfma_f32_16x16x32_bf16 v[20:23], v[92:95], v[76:79], v[20:23]\n\t" \
    "ds_read_b128 v[104:107], %[a1] offset:34816\n\t" \
    "v_mfma_f32_16x16x32_bf16 v[36:39], v[76:79], v[96:99], v[36:39]\n\t" \
    "ds_read_b128 v[108:111], %[a1] offset:36864\n\t" \
    "v_mfma_f32_16x16x32_bf16 v[8:11], v[88:91], v[80:83], v[8:11]\n\t" \
    "ds_read_b128 v[112:115], %[a1] offset:38912\n\t" \
    "v_mfma_f32_16x16x32_bf16 v[24:27], v[92:95], v[80:83], v[24:27]\n\t" \
    "v_mfma_f32_16x16x32_bf16 v[40:43], v[80:83], v[96:99], v[40:43]\n\t" \
    "v_mfma_f32_16x16x32_bf16 v[12:15], v[88:91], v[84:87], v[12:15]\n\t" \
    "v_mfma_f32_16x16x32_bf16 v[28:31], v[92:95], v[84:87], v[28:31]\n\t" \
    "v_mfma_f32_16x16x32_bf16 v[44:47], v[84:87], v[96:99], v[44:47]\n\t" \
    "s_waitcnt lgkmcnt(0)\n\t" \
    "s_barrier\n\t" \
    "v_mfma_f32_16x16x32_bf16 v[48:51], v[116:119], v[68:71], v[48:51]\n\t" \
    "ds_read_b64_tr_b16 v[88:89], %[b0] offset:24576\n\t" \
    "v_mfma_f32_16x16x32_bf16 v[52:55], v[120:123], v[68:71], v[52:55]\n\t" \
    "ds_read_b64_tr_b16 v[90:91], %[b0] offset:25088\n\t" \
    "v_mfma_f32_16x16x32_bf16 v[56:59], v[68:71], v[124:127], v[56:59]\n\t" \
    "ds_read_b64_tr_b16 v[92:93], %[b0] offset:28672\n\t" \
    "v_mfma_f32_16x16x32_bf16 v[0:3], v[116:119], v[100:103], v[0:3]\n\t" \
    "ds_read_b64_tr_b16 v[94:95], %[b0] offset:29184\n\t" \
    "v_mfma_f32_16x16x32_bf16 v[16:19], v[120:123], v[100:103], v[16:19]\n\t" \
    "ds_read_b64_tr_b16 v[96:97], %[b0] offset:32768\n\t" \
    "v_mfma_f32_16x16x32_bf16 v[32:35], v[100:103], v[124:127], v[32:35]\n\t" \
    "ds_read_b64_tr_b16 v[98:99], %[b0] offset:33280\n\t" \
    "v_mfma_f32_16x16x32_bf16 v[4:7], v[116:119], v[104:107], v[4:7]\n\t" \
    "ds_read_b128 v[72:75], %[a0] offset:40960\n\t" \
    "v_mfma_f32_16x16x32_bf16 v[20:23], v[120:123], v[104:107], v[20:23]\n\t" \
    "ds_read_b128 v[76:79], %[a0] offset:43008\n\t" \
    "v_mfma_f32_16x16x32_bf16 v[36:39], v[104:107], v[124:127], v[36:39]\n\t" \
    "ds_read_b128 v[80:83], %[a0] offset:45056\n\t" \
    "v_mfma_f32_16x16x32_bf16 v[8:11], v[116:119], v[108:111], v[8:11]\n\t" \
    "ds_read_b128 v[84:87], %[a0] offset:47104\n\t" \
    "v_mfma_f32_16x16x32_bf16 v[24:27], v[120:123], v[108:111], v[24:27]\n\t" \
    "v_mfma_f32_16x16x32_bf16 v[40:43], v[108:111], v[124:127], v[40:43]\n\t" \
    "v_mfma_f32_16x16x32_bf16 v[12:15], v[116:119], v[112:115], v[12:15]\n\t" \
    "v_mfma_f32_16x16x32_bf16 v[28:31], v[120:123], v[112:115], v[28:31]\n\t" \
    "v_mfma_f32_16x16x32_bf16 v[44:47], v[112:115], v[124:127], v[44:47]\n\t" \
    "s_waitcnt lgkmcnt(0)\n\t" \
    "v_mfma_f32_16x16x32_bf16 v[48:51], v[88:91], v[68:71], v[48:51]\n\t" \
    "ds_read_b64_tr_b16 v[116:117], %[b0] offset:36864\n\t" \
    "v_mfma_f32_16x16x32_bf16 v[52:55], v[92:95], v[68:71], v[52:55]\n\t" \
    "ds_read_b64_tr_b16 v[118:119], %[b0] offset:37376\n\t" \
    "v_mfma_f32_16x16x32_bf16 v[56:59], v[68:71], v[96:99], v[56:59]\n\t" \
    "ds_read_b64_tr_b16 v[120:121], %[b0] offset:40960\n\t" \
    "v_mfma_f32_16x16x32_bf16 v[0:3], v[88:91], v[72:75], v[0:3]\n\t" \
    "ds_read_b64_tr_b16 v[122:123], %[b0] offset:41472\n\t" \
    "v_mfma_f32_16x16x32_bf16 v[16:19], v[92:95], v[72:75], v[16:19]\n\t" \
    "ds_read_b64_tr_b16 v[124:125], %[b0] offset:45056\n\t" \
    "v_mfma_f32_16x16x32_bf16 v[32:35], v[72:75], v[96:99], v[32:35]\n\t" \
    "ds_read_b64_tr_b16 v[126:127], %[b0] offset:45568\n\t" \
    "v_mfma_f32_16x16x32_bf16 v[4:7], v[88:91], v[76:79], v[4:7]\n\t" \
    "ds_read_b128 v[100:103], %[a1] offset:40960\n\t" \
    "v_mfma_f32_16x16x32_bf16 v[20:23], v[92:95], v[76:79], v[20:23]\n\t" \
    "ds_read_b128 v[104:107], %[a1] offset:43008\n\t" \
    "v_mfma_f32_16x16x32_bf16 v[36:39], v[76:79], v[96:99], v[36:39]\n\t" \
    "ds_read_b128 v[108:111], %[a1] offset:45056\n\t" \
    "v_mfma_f32_16x16x32_bf16 v[8:11], v[88:91], v[80:83], v[8:11]\n\t" \
    "ds_read_b128 v[112:115], %[a1] offset:47104\n\t" \
    "v_mfma_f32_16x16x32_bf16 v[24:27], v[92:95], v[80:83], v[24:27]\n\t" \
    "v_mfma_f32_16x16x32_bf16 v[40:43], v[80:83], v[96:99], v[40:43]\n\t" \
    "v_mfma_f32_16x16x32_bf16 v[12:15], v[88:91], v[84:87], v[12:15]\n\t" \
    "v_mfma_f32_16x16x32_bf16 v[28:31], v[92:95], v[84:87], v[28:31]\n\t" \
    "v_mfma_f32_16x16x32_bf16 v[44:47], v[84:87], v[96:99], v[44:47]\n\t" \
    "s_waitcnt lgkmcnt(0)\n\t" \
    "s_barrier\n\t" \
    "v_mfma_f32_16x16x32_bf16 v[48:51], v[116:119], v[68:71], v[48:51]\n\t" \
    "ds_read_b64_tr_b16 v[88:89], %[b1] offset:0\n\t" \
    "v_mfma_f32_16x16x32_bf16 v[52:55], v[120:123], v[68:71], v[52:55]\n\t" \
    "ds_read_b64_tr_b16 v[90:91], %[b1] offset:512\n\t" \
    "v_mfma_f32_16x16x32_bf16 v[56:59], v[68:71], v[124:127], v[56:59]\n\t" \
    "ds_read_b64_tr_b16 v[92:93], %[b1] offset:4096\n\t" \
    "v_mfma_f32_16x16x32_bf16 v[0:3], v[116:119], v[100:103], v[0:3]\n\t" \
    "ds_read_b64_tr_b16 v[94:95], %[b1] offset:4608\n\t" \
    "v_mfma_f32_16x16x32_bf16 v[16:19], v[120:123], v[100:103], v[16:19]\n\t" \
    "ds_read_b64_tr_b16 v[96:97], %[b1] offset:8192\n\t" \
    "v_mfma_f32_16x16x32_bf16 v[32:35], v[100:103], v[124:127], v[32:35]\n\t" \
    "ds_read_b64_tr_b16 v[98:99], %[b1] offset:8704\n\t" \
    "v_mfma_f32_16x16x32_bf16 v[4:7], v[116:119], v[104:107], v[4:7]\n\t" \
    "ds_read_b128 v[72:75], %[a0] offset:49152\n\t" \
    "v_mfma_f32_16x16x32_bf16 v[20:23], v[120:123], v[104:107], v[20:23]\n\t" \
    "ds_read_b128 v[76:79], %[a0] offset:51200\n\t" \
    "v_mfma_f32_16x16x32_bf16 v[36:39], v[104:107], v[124:127], v[36:39]\n\t" \
    "ds_read_b128 v[80:83], %[a0] offset:53248\n\t" \
    "v_mfma_f32_16x16x32_bf16 v[8:11], v[116:119], v[108:111], v[8:11]\n\t" \
    "ds_read_b128 v[84:87], %[a0] offset:55296\n\t" \
    "v_mfma_f32_16x16x32_bf16 v[24:27], v[120:123], v[108:111], v[24:27]\n\t" \
    "ds_read_b128 v[152:155], %[rv] offset:4096\n\t" \
    "v_mfma_f32_16x16x32_bf16 v[40:43], v[108:111], v[124:127], v[40:43]\n\t" \
    "ds_read_b128 v[156:159], %[rv] offset:4352\n\t" \
    "v_mfma_f32_16x16x32_bf16 v[12:15], v[116:119], v[112:115], v[12:15]\n\t" \
    "ds_read_b128 v[160:163], %[rv] offset:6144\n\t" \
    "v_mfma_f32_16x16x32_bf16 v[28:31], v[120:123], v[112:115], v[28:31]\n\t" \
    "ds_read_b128 v[164:167], %[rv] offset:6400\n\t" \
    "v_mfma_f32_16x16x32_bf16 v[44:47], v[112:115], v[124:127], v[44:47]\n\t" \
    "s_waitcnt lgkmcnt(0)\n\t" \
    "v_mfma_f32_16x16x32_bf16 v[48:51], v[88:91], v[68:71], v[48:51]\n\t" \
    "ds_read_b64_tr_b16 v[116:117], %[b1] offset:12288\n\t" \
    "v_mfma_f32_16x16x32_bf16 v[52:55], v[92:95], v[68:71], v[52:55]\n\t" \
    "ds_read_b64_tr_b16 v[118:119], %[b1] offset:12800\n\t" \
    "v_mfma_f32_16x16x32_bf16 v[56:59], v[68:71], v[96:99], v[56:59]\n\t" \
    "ds_read_b64_tr_b16 v[120:121], %[b1] offset:16384\n\t" \
    "v_mfma_f32_16x16x32_bf16 v[0:3], v[88:91], v[72:75], v[0:3]\n\t" \
    "ds_read_b64_tr_b16 v[122:123], %[b1] offset:16896\n\t" \
    "v_mfma_f32_16x16x32_bf16 v[16:19], v[92:95], v[72:75], v[16:19]\n\t" \
    "ds_read_b64_tr_b16 v[124:125], %[b1] offset:20480\n\t" \
    "v_mfma_f32_16x16x32_bf16 v[32:35], v[72:75], v[96:99], v[32:35]\n\t" \
    "ds_read_b64_tr_b16 v[126:127], %[b1] offset:20992\n\t" \
    "v_mfma_f32_16x16x32_bf16 v[4:7], v[88:91], v[76:79], v[4:7]\n\t" \
    "ds_read_b128 v[100:103], %[a1] offset:49152\n\t" \
    "v_mfma_f32_16x16x32_bf16 v[20:23], v[92:95], v[76:79], v[20:23]\n\t" \
    "ds_read_b128 v[104:107], %[a1] offset:51200\n\t" \
    "v_mfma_f32_16x16x32_bf16 v[36:39], v[76:79], v[96:99], v[36:39]\n\t" \
    "ds_read_b128 v[108:111], %[a1] offset:53248\n\t" \
    "v_mfma_f32_16x16x32_bf16 v[8:11], v[88:91], v[80:83], v[8:11]\n\t" \
    "ds_read_b128 v[112:115], %[a1] offset:55296\n\t" \
    "v_mfma_f32_16x16x32_bf16 v[24:27], v[92:95], v[80:83], v[24:27]\n\t" \
    "ds_read2st64_b32 v[128:129], %[rq] offset0:0 offset1:1\n\t" \
    "v_mfma_f32_16x16x32_bf16 v[40:43], v[80:83], v[96:99], v[40:43]\n\t" \
    "ds_read2st64_b32 v[130:131], %[rq] offset0:8 offset1:9\n\t" \
    "v_mfma_f32_16x16x32_bf16 v[12:15], v[88:91], v[84:87], v[12:15]\n\t" \
    "ds_read2st64_b32 v[132:133], %[rq] offset0:16 offset1:17\n\t" \
    "v_mfma_f32_16x16x32_bf16 v[28:31], v[92:95], v[84:87], v[28:31]\n\t" \
    "ds_read2st64_b32 v[134:135], %[rq] offset0:24 offset1:25\n\t" \
    "v_mfma_f32_16x16x32_bf16 v[44:47], v[84:87], v[96:99], v[44:47]\n\t" \
    "s_waitcnt lgkmcnt(0)\n\t" \
    "s_barrier\n\t" \
    "v_mfma_f32_16x16x32_bf16 v[48:51], v[116:119], v[68:71], v[48:51]\n\t" \
    "ds_read_b64_tr_b16 v[88:89], %[b1] offset:24576\n\t" \
    "v_mfma_f32_16x16x32_bf16 v[52:55], v[120:123], v[68:71], v[52:55]\n\t" \
    "ds_read_b64_tr_b16 v[90:91], %[b1] offset:25088\n\t" \
    "v_mfma_f32_16x16x32_bf16 v[56:59], v[68:71], v[124:127], v[56:59]\n\t" \
    "ds_read_b64_tr_b16 v[92:93], %[b1] offset:28672\n\t" \
    "v_mfma_f32_16x16x32_bf16 v[0:3], v[116:119], v[100:103], v[0:3]\n\t" \
    "ds_read_b64_tr_b16 v[94:95], %[b1] offset:29184\n\t" \
    "v_mfma_f32_16x16x32_bf16 v[16:19], v[120:123], v[100:103], v[16:19]\n\t" \
    "ds_read_b64_tr_b16 v[96:97], %[b1] offset:32768\n\t" \
    "v_mfma_f32_16x16x32_bf16 v[32:35], v[100:103], v[124:127], v[32:35]\n\t" \
    "ds_read_b64_tr_b16 v[98:99], %[b1] offset:33280\n\t" \
    "v_mfma_f32_16x16x32_bf16 v[4:7], v[116:119], v[104:107], v[4:7]\n\t" \
    "ds_read_b128 v[72:75], %[a0] offset:57344\n\t" \
    "v_mfma_f32_16x16x32_bf16 v[20:23], v[120:123], v[104:107], v[20:23]\n\t" \
    "ds_read_b128 v[76:79], %[a0] offset:59392\n\t" \
    "v_mfma_f32_16x16x32_bf16 v[36:39], v[104:107], v[124:127], v[36:39]\n\t" \
    "ds_read_b128 v[80:83], %[a0] offset:61440\n\t" \
    "v_mfma_f32_16x16x32_bf16 v[8:11], v[116:119], v[108:111], v[8:11]\n\t" \
    "ds_read_b128 v[84:87], %[a0] offset:63488\n\t" \
    "v_mfma_f32_16x16x32_bf16 v[24:27], v[120:123], v[108:111], v[24:27]\n\t" \
    "ds_read_b128 v[136:139], %[rv] offset:0\n\t" \
    "v_mfma_f32_16x16x32_bf16 v[40:43], v[108:111], v[124:127], v[40:43]\n\t" \
    "ds_read_b128 v[140:143], %[rv] offset:256\n\t" \
    "v_mfma_f32_16x16x32_bf16 v[12:15], v[116:119], v[112:115], v[12:15]\n\t" \
    "ds_read_b128 v[144:147], %[rv] offset:2048\n\t" \
    "v_mfma_f32_16x16x32_bf16 v[28:31], v[120:123], v[112:115], v[28:31]\n\t" \
    "ds_read_b128 v[148:151], %[rv] offset:2304\n\t" \
    "v_mfma_f32_16x16x32_bf16 v[44:47], v[112:115], v[124:127], v[44:47]\n\t" \
    "s_waitcnt lgkmcnt(0)\n\t" \
    "v_mfma_f32_16x16x32_bf16 v[48:51], v[88:91], v[68:71], v[48:51]\n\t" \
    "v_mfma_f32_16x16x32_bf16 v[52:55], v[92:95], v[68:71], v[52:55]\n\t" \
    "v_mfma_f32_16x16x32_bf16 v[56:59], v[68:71], v[96:99], v[56:59]\n\t" \
    "v_mfma_f32_16x16x32_bf16 v[0:3], v[88:91], v[72:75], v[0:3]\n\t" \
    "v_mfma_f32_16x16x32_bf16 v[16:19], v[92:95], v[72:75], v[16:19]\n\t" \
    "v_mfma_f32_16x16x32_bf16 v[32:35], v[72:75], v[96:99], v[32:35]\n\t" \
    "v_mfma_f32_16x16x32_bf16 v[4:7], v[88:91], v[76:79], v[4:7]\n\t" \
    "v_mfma_f32_16x16x32_bf16 v[20:23], v[92:95], v[76:79], v[20:23]\n\t" \
    "v_mfma_f32_16x16x32_bf16 v[36:39], v[76:79], v[96:99], v[36:39]\n\t" \
    "v_mfma_f32_16x16x32_bf16 v[8:11], v[88:91], v[80:83], v[8:11]\n\t" \
    "v_mfma_f32_16x16x32_bf16 v[24:27], v[92:95], v[80:83], v[24:27]\n\t" \
    "v_mfma_f32_16x16x32_bf16 v[40:43], v[80:83], v[96:99], v[40:43]\n\t" \
    "v_mfma_f32_16x16x32_bf16 v[12:15], v[88:91], v[84:87], v[12:15]\n\t" \
    "v_mfma_f32_16x16x32_bf16 v[28:31], v[92:95], v[84:87], v[28:31]\n\t" \
    "v_mfma_f32_16x16x32_bf16 v[44:47], v[84:87], v[96:99], v[44:47]\n\t" \
    "s_barrier\n\t" \
    "ds_read_b64_tr_b16 v[116:117], %[b1] offset:36864\n\t" \
    "ds_read_b64_tr_b16 v[118:119], %[b1] offset:37376\n\t" \
    "ds_read_b64_tr_b16 v[120:121], %[b1] offset:40960\n\t" \
    "ds_read_b64_tr_b16 v[122:123], %[b1] offset:41472\n\t" \
    "ds_read_b64_tr_b16 v[124:125], %[b1] offset:45056\n\t" \
    "ds_read_b64_tr_b16 v[126:127], %[b1] offset:45568\n\t" \
    "ds_read_b128 v[100:103], %[a1] offset:57344\n\t" \
    "ds_read_b128 v[104:107], %[a1] offset:59392\n\t" \
    "ds_read_b128 v[108:111], %[a1] offset:61440\n\t" \
    "ds_read_b128 v[112:115], %[a1] offset:63488\n\t" \
    "s_waitcnt lgkmcnt(0)\n\t" \
    "v_mfma_f32_16x16x32_bf16 v[48:51], v[116:119], v[68:71], v[48:51]\n\t" \
    "v_mfma_f32_16x16x32_bf16 v[52:55], v[120:123], v[68:71], v[52:55]\n\t" \
    "v_mfma_f32_16x16x32_bf16 v[56:59], v[68:71], v[124:127], v[56:59]\n\t" \
    "v_mfma_f32_16x16x32_bf16 v[0:3], v[116:119], v[100:103], v[0:3]\n\t" \
    "v_mfma_f32_16x16x32_bf16 v[16:19], v[120:123], v[100:103], v[16:19]\n\t" \
    "v_mfma_f32_16x16x32_bf16 v[32:35], v[100:103], v[124:127], v[32:35]\n\t" \
    "v_mfma_f32_16x16x32_bf16 v[4:7], v[116:119], v[104:107], v[4:7]\n\t" \
    "v_mfma_f32_16x16x32_bf16 v[20:23], v[120:123], v[104:107], v[20:23]\n\t" \
    "v_mfma_f32_16x16x32_bf16 v[36:39], v[104:107], v[124:127], v[36:39]\n\t" \
    "v_mfma_f32_16x16x32_bf16 v[8:11], v[116:119], v[108:111], v[8:11]\n\t" \
    "v_mfma_f32_16x16x32_bf16 v[24:27], v[120:123], v[108:111], v[24:27]\n\t" \
    "v_mfma_f32_16x16x32_bf16 v[40:43], v[108:111], v[124:127], v[40:43]\n\t" \
    "v_mfma_f32_16x16x32_bf16 v[12:15], v[116:119], v[112:115], v[12:15]\n\t" \
    "v_mfma_f32_16x16x32_bf16 v[28:31], v[120:123], v[112:115], v[28:31]\n\t" \
    "v_mfma_f32_16x16x32_bf16 v[44:47], v[112:115], v[124:127], v[44:47]\n\t" \
    "s_nop 7\n\t" \
    "s_nop 3\n\t"
#define K1_CLOB "v60", "v61", "v62", "v63", "v64", "v65", "v66", "v67", "v68", "v69", "v70", "v71", "v72", "v73", "v74", "v75", "v76", "v77", "v78", "v79", "v80", "v81", "v82", "v83", "v84", "v85", "v86", "v87", "v88", "v89", "v90", "v91", "v92", "v93", "v94", "v95", "v96", "v97", "v98", "v99", "v100", "v101", "v102", "v103", "v104", "v105", "v106", "v107", "v108", "v109", "v110", "v111", "v112", "v113", "v114", "v115", "v116", "v117", "v118", "v119", "v120", "v121", "v122", "v123", "v124", "v125", "v126", "v127"
__global__ __launch_bounds__(512) void k1_qkv(const char* __restrict__ x, const char* __restrict__ wqkv, char* __restrict__ ws) {
    extern __shared__ __attribute__((aligned(16))) char smem[];
    __builtin_amdgcn_sched_barrier(0);
    unsigned cp = ((blockIdx.x >> 3) * 16 + ((threadIdx.x >> 4) & 15)) * 128;
    asm volatile("code_beg:\n\t"
                 "v_cmp_lt_u32 vcc, 0xff, %1\n\ts_cbranch_vccz .Lnp%=\n\t" CODE_PF("%0", "%0") ".Lnp%=:"
                 : "+v"(cp) : "v"(threadIdx.x) : "memory", "vcc", "scc");
    __builtin_amdgcn_sched_barrier(0);
    TSDECL; TSTAMP(0);
    const unsigned lds0 = (unsigned)(uintptr_t)(lds_ptr)smem;
    const int lane = threadIdx.x & 63, w = __builtin_amdgcn_readfirstlane(threadIdx.x >> 6);
    const int b = blockIdx.x, xcd = b & 7, bi = b >> 3;
    const int rt = (xcd >> 1) * 8 + (bi >> 2), ct = (xcd & 1) * 4 + (bi & 3);
    const int g = lane >> 4, r16 = lane & 15;
    const unsigned a0 = lds0 + r16 * 128 + ((g ^ ((r16 >> 1) & 7)) << 4), rq = lds0 + r16 * 4;
    {
        const int kg = K1_KG;
        unsigned va0 = (unsigned)((rt * 64 + w * 16 + (lane >> 3)) * 1024 + kg * 128 + (((lane & 7) ^ g) << 4)), va1 = (va0 ^ 64) + 8192, vb, st;
        const unsigned vb0 = (unsigned)((w * 8 + (lane >> 3)) * 3072 + ct * 128 + (((lane & 7) ^ ((2 * (w & 1) + (g & 1)) << 1)) << 4));
        asm volatile(K1_PROD : [vb] "=&v"(vb), [va0] "+v"(va0), [va1] "+v"(va1), [st] "=&s"(st)
                     : [vb0] "v"(vb0), [kg] "s"(kg), [sa] "s"(x), [sb] "s"(wqkv),
                       [la] "s"(lds0 + w * 2048), [lb] "s"(lds0 + 65536 + w * 1024), [w] "s"(w),
                       [p0] "v"(a0 + w * 2048), [p1] "v"((a0 ^ 64) + w * 2048), [ws] "v"(rq + w * 2048),
                       [m1] "s"(0xAAAAAAAAAAAAAAAAull), [m2] "s"(0xCCCCCCCCCCCCCCCCull), [m3] "s"(0xF0000F0000F0000Full)
                     : "memory", "scc", K1P_CLOB);
    }
    const int c = w - 4, head = ct * 4 + c;
    const unsigned b0 = lds0 + 65536 + g * 1024 + (r16 >> 2) * 128 + ((c ^ (2 * (g & 1) + (r16 >> 3))) << 5) + (lane & 3) * 8;
    unsigned oq = head * 65536 + rt * 2048 + lane * 16, ov = WS_KV + 65536 + head * 131072 + rt * 2048 + lane * 16;
    unsigned ok = WS_KV + head * 131072 + rt * 2048 + (g >> 1) * 512 + ((r16 & 7) + (r16 >> 3) * 16 + (g & 1) * 8) * 16;
    if (threadIdx.x >= 508) *(unsigned*)(ws + WS_ST + FLAG2 + (lane & 3) * 32768 + b * 128) = lane == 62 && xcd == 7;
    f32x16 aq, ak, av, sa, sb;
    f32x8 sq;
    f32x4 cq, ck, cv;
    asm volatile(BAR0 K1_CONS "s_waitcnt vmcnt(0)\n\t"
                 : "=&{v[0:15]}"(aq), "=&{v[16:31]}"(ak), "=&{v[32:47]}"(av), "=&{v[48:51]}"(cq), "=&{v[52:55]}"(ck), "=&{v[56:59]}"(cv),
                   "=&{v[128:135]}"(sq), "=&{v[136:151]}"(sa), "=&{v[152:167]}"(sb), "+v"(oq), "+v"(ok), "+v"(ov), "+s"(ws), "+v"(cp) TSOUT
                 : [a0] "v"(a0), [a1] "v"(a0 ^ 64), [b0] "v"(b0), [b1] "v"(b0 + 49152), [rq] "v"(rq), [rv] "v"(lds0 + g * 16)
                 : "memory", "vcc", K1_CLOB);
    TSTAMP(2);
#pragma unroll
    for (int p = 0; p < 2; ++p) {
        unsigned t[4];
#pragma unroll
        for (int j = 0; j < 4; ++j) {
            const int i = 8 * p + 2 * j;
            const float m = sq[4 * p + (j & 2)], r = sq[4 * p + (j & 2) + 1];
            t[j] = cvtpk((ak[i] - m * ck[i & 3]) * r, (ak[i + 1] - m * ck[(i & 3) + 1]) * r);
        }
        ST16(ws, ok, p * 1024, swap16(t[0], t[1], t[2], t[3]));
        const f32x16 sv = p ? sb : sa;
#pragma unroll
        for (int j = 0; j < 4; ++j) {
            const int i = 8 * p + 2 * j, e = (j & 2) * 4 + (j & 1) * 2;
            t[j] = cvtpk((av[i] - sv[e] * cv[0]) * sv[e + 4], (av[i + 1] - sv[e + 1] * cv[0]) * sv[e + 5]);
        }
        ST16(ws, ov, p * 1024, ((u32x4){t[0], t[1], t[2], t[3]}));
#pragma unroll
        for (int j = 0; j < 4; ++j) {
            const int i = 8 * p + 2 * j;
            const float m = sq[4 * p + (j & 2)], r = sq[4 * p + (j & 2) + 1] * QSCALE;
            t[j] = cvtpk((aq[i] - m * cq[i & 3]) * r, (aq[i + 1] - m * cq[(i & 3) + 1]) * r);
        }
        ST16(ws, oq, p * 1024, swap16(t[0], t[1], t[2], t[3]));
        __builtin_amdgcn_sched_barrier(0);
    }
    TSFLUSH(0, 256);
}

#define ATT_ASM \
    "s_mov_b32 %[sv], m0\n\t" \
    "v_pk_mov_b32 v[120:121], %[cs], %[cs]\n\t" \
    "v_pk_mov_b32 v[122:123], %[cs], %[cs]\n\t" \
    "v_pk_mov_b32 v[124:125], %[cs], %[cs]\n\t" \
    "v_pk_mov_b32 v[126:127], %[cs], %[cs]\n\t" \
    "v_pk_mov_b32 v[128:129], %[cs], %[cs]\n\t" \
    "v_pk_mov_b32 v[130:131], %[cs], %[cs]\n\t" \
    "v_pk_mov_b32 v[132:133], %[cs], %[cs]\n\t" \
    "v_pk_mov_b32 v[134:135], %[cs], %[cs]\n\t" \
    "s_cmp_lg_u32 %[pass], 0\n\t" \
    "s_cbranch_scc1 .Latt_m%=\n\t" \
    "global_load_dwordx4 v[104:107], %[qo0], %[qb]\n\t" \
    "global_load_dwordx4 v[108:111], %[qo1], %[qb]\n\t" \
    "s_add_u32 m0, %[lds0], %[g0]\n\t" \
    "v_add_u32 v137, %[g0], %[l16]\n\t" \
    "s_cmp_eq_u32 %[lead], 0\n\t" \
    "s_cbranch_scc1 .Latt_f%=\n\t" \
    "s_setprio 1\n\t" \
    "global_load_lds_dwordx4 v137, %[kv] offset:0\n\t" \
    "global_load_lds_dwordx4 v137, %[kv] offset:1024\n\t" \
    "global_load_lds_dwordx4 v137, %[kv] offset:2048\n\t" \
    "global_load_lds_dwordx4 v137, %[kv] offset:3072\n\t" \
    "s_waitcnt vmcnt(0)\n\t" \
    "s_barrier\n\t" \
    "s_branch .Latt_x%=\n\t" \
    ".Latt_f%=:\n\t" \
    "global_load_lds_dwordx4 v137, %[kv] offset:-4096\n\t" \
    "global_load_lds_dwordx4 v137, %[kv] offset:-3072\n\t" \
    "global_load_lds_dwordx4 v137, %[kv] offset:-2048\n\t" \
    "global_load_lds_dwordx4 v137, %[kv] offset:-1024\n\t" \
    "global_load_lds_dwordx4 v137, %[kv] offset:0\n\t" \
    "global_load_lds_dwordx4 v137, %[kv] offset:1024\n\t" \
    "global_load_lds_dwordx4 v137, %[kv] offset:2048\n\t" \
    "global_load_lds_dwordx4 v137, %[kv] offset:3072\n\t" \
    "s_add_u32 m0, m0, 8192\n\t" \
    "v_add_u32 v137, 8192, v137\n\t" \
    "global_load_lds_dwordx4 v137, %[kv] offset:-4096\n\t" \
    "global_load_lds_dwordx4 v137, %[kv] offset:-3072\n\t" \
    "global_load_lds_dwordx4 v137, %[kv] offset:-2048\n\t" \
    "global_load_lds_dwordx4 v137, %[kv] offset:-1024\n\t" \
    "global_load_lds_dwordx4 v137, %[kv] offset:0\n\t" \
    "global_load_lds_dwordx4 v137, %[kv] offset:1024\n\t" \
    "global_load_lds_dwordx4 v137, %[kv] offset:2048\n\t" \
    "global_load_lds_dwordx4 v137, %[kv] offset:3072\n\t" \
    "s_barrier\n\t" \
    "s_add_u32 m0, m0, 8192\n\t" \
    "v_add_u32 v137, 8192, v137\n\t" \
    "global_load_lds_dwordx4 v137, %[kv] offset:-4096\n\t" \
    "global_load_lds_dwordx4 v137, %[kv] offset:-3072\n\t" \
    "global_load_lds_dwordx4 v137, %[kv] offset:-2048\n\t" \
    "global_load_lds_dwordx4 v137, %[kv] offset:-1024\n\t" \
    "global_load_lds_dwordx4 v137, %[kv] offset:0\n\t" \
    "global_load_lds_dwordx4 v137, %[kv] offset:1024\n\t" \
    "global_load_lds_dwordx4 v137, %[kv] offset:2048\n\t" \
    "global_load_lds_dwordx4 v137, %[kv] offset:3072\n\t" \
    "s_add_u32 m0, %[lds0], %[g1]\n\t" \
    "v_add_u32 v137, %[g1], %[l16]\n\t" \
    "global_load_lds_dwordx4 v137, %[kv] offset:0\n\t" \
    "global_load_lds_dwordx4 v137, %[kv] offset:1024\n\t" \
    "global_load_lds_dwordx4 v137, %[kv] offset:2048\n\t" \
    "global_load_lds_dwordx4 v137, %[kv] offset:3072\n\t" \
    "s_waitcnt vmcnt(28)\n\t" \
    ".Latt_x%=:\n\t" \
    "ds_read_b128 v[116:119], %[kr]\n\t" \
    "s_waitcnt lgkmcnt(0)\n\t" \
    "v_mfma_f32_32x32x16_bf16 v[64:79], v[116:119], v[104:107], 0\n\t" \
    "v_mfma_f32_32x32x16_bf16 v[80:95], v[116:119], v[108:111], 0\n\t" \
    "s_nop 10\n\t" \
    "v_max3_f32 v64, v64, v65, v66\n\t" \
    "v_max3_f32 v64, v64, v67, v68\n\t" \
    "v_max3_f32 v64, v64, v69, v70\n\t" \
    "v_max3_f32 v64, v64, v71, v72\n\t" \
    "v_max3_f32 v64, v64, v73, v74\n\t" \
    "v_max3_f32 v80, v80, v81, v82\n\t" \
    "v_max3_f32 v64, v64, v75, v76\n\t" \
    "v_max3_f32 v80, v80, v83, v84\n\t" \
    "v_max3_f32 v64, v64, v77, v78\n\t" \
    "v_max3_f32 v80, v80, v85, v86\n\t" \
    "v_max_f32 v64, v64, v79\n\t" \
    "v_max3_f32 v80, v80, v87, v88\n\t" \
    "v_max3_f32 v80, v80, v89, v90\n\t" \
    "v_mov_b32 v65, v64\n\t" \
    "v_max3_f32 v80, v80, v91, v92\n\t" \
    "v_max3_f32 v80, v80, v93, v94\n\t" \
    "v_permlane32_swap_b32 v64, v65\n\t" \
    "v_max_f32 v80, v80, v95\n\t" \
    "v_mov_b32 v81, v80\n\t" \
    "v_min_f32 v138, -v64, -v65\n\t" \
    "s_nop 0\n\t" \
    "v_permlane32_swap_b32 v80, v81\n\t" \
    "s_nop 0\n\t" \
    "v_min_f32 v139, -v80, -v81\n\t" \
    ".Latt_m%=:\n\t" \
    "v_pk_mov_b32 v[32:33], v[138:139], v[138:139] op_sel:[0,0]\n\t" \
    "v_pk_mov_b32 v[34:35], v[138:139], v[138:139] op_sel:[0,0]\n\t" \
    "v_pk_mov_b32 v[36:37], v[138:139], v[138:139] op_sel:[0,0]\n\t" \
    "v_pk_mov_b32 v[38:39], v[138:139], v[138:139] op_sel:[0,0]\n\t" \
    "v_pk_mov_b32 v[40:41], v[138:139], v[138:139] op_sel:[0,0]\n\t" \
    "v_pk_mov_b32 v[42:43], v[138:139], v[138:139] op_sel:[0,0]\n\t" \
    "v_pk_mov_b32 v[44:45], v[138:139], v[138:139] op_sel:[0,0]\n\t" \
    "v_pk_mov_b32 v[46:47], v[138:139], v[138:139] op_sel:[0,0]\n\t" \
    "v_pk_mov_b32 v[48:49], v[138:139], v[138:139] op_sel:[1,1]\n\t" \
    "v_pk_mov_b32 v[50:51], v[138:139], v[138:139] op_sel:[1,1]\n\t" \
    "v_pk_mov_b32 v[52:53], v[138:139], v[138:139] op_sel:[1,1]\n\t" \
    "v_pk_mov_b32 v[54:55], v[138:139], v[138:139] op_sel:[1,1]\n\t" \
    "v_pk_mov_b32 v[56:57], v[138:139], v[138:139] op_sel:[1,1]\n\t" \
    "v_pk_mov_b32 v[58:59], v[138:139], v[138:139] op_sel:[1,1]\n\t" \
    "v_pk_mov_b32 v[60:61], v[138:139], v[138:139] op_sel:[1,1]\n\t" \
    "v_pk_mov_b32 v[62:63], v[138:139], v[138:139] op_sel:[1,1]\n\t" \
    "s_cmp_lg_u32 %[lead], 0\n\t" \
    "s_cbranch_scc1 .Latt_a%=\n\t" \
    "s_waitcnt vmcnt(0)\n\t" \
    "s_barrier\n\t" \
    ".Latt_a%=:\n\t" \
    "ds_read_b128 v[112:115], %[ka]\n\t" \
    "s_mov_b64 exec, %[vm]\n\t" \
    "ds_read_b128 v[120:123], %[va]\n\t" \
    "ds_read_b128 v[124:127], %[va] offset:512\n\t" \
    "s_mov_b64 exec, -1\n\t" \
    "s_waitcnt lgkmcnt(2)\n\t" \
    "v_mfma_f32_32x32x16_bf16 v[64:79], v[112:115], v[104:107], v[32:47]\n\t" \
    "s_waitcnt lgkmcnt(0)\n\t" \
    "s_nop 9\n\t" \
    "v_mfma_f32_32x32x16_bf16 v[80:95], v[112:115], v[108:111], v[48:63]\n\t" \
    "ds_read_b128 v[116:119], %[ka] offset:1024\n\t" \
    "v_exp_f32 v64, v64\n\t" \
    "v_exp_f32 v65, v65\n\t" \
    "v_exp_f32 v66, v66\n\t" \
    "v_exp_f32 v67, v67\n\t" \
    "v_exp_f32 v68, v68\n\t" \
    "v_cvt_pk_bf16_f32 v96, v64, v65\n\t" \
    "v_cvt_pk_bf16_f32 v97, v66, v67\n\t" \
    "s_mov_b64 exec, %[vm]\n\t" \
    "ds_read_b128 v[128:131], %[va] offset:1024\n\t" \
    "ds_read_b128 v[132:135], %[va] offset:1536\n\t" \
    "s_mov_b64 exec, -1\n\t" \
    "v_exp_f32 v69, v69\n\t" \
    "v_exp_f32 v70, v70\n\t" \
    "v_exp_f32 v71, v71\n\t" \
    "v_exp_f32 v72, v72\n\t" \
    "v_cvt_pk_bf16_f32 v98, v68, v69\n\t" \
    "v_cvt_pk_bf16_f32 v99, v70, v71\n\t" \
    "v_exp_f32 v73, v73\n\t" \
    "v_exp_f32 v74, v74\n\t" \
    "v_mfma_f32_32x32x16_bf16 v[0:15], v[120:123], v[96:99], 0\n\t" \
    "v_exp_f32 v75, v75\n\t" \
    "v_exp_f32 v76, v76\n\t" \
    "v_exp_f32 v77, v77\n\t" \
    "v_exp_f32 v78, v78\n\t" \
    "v_exp_f32 v79, v79\n\t" \
    "v_cvt_pk_bf16_f32 v100, v72, v73\n\t" \
    "v_cvt_pk_bf16_f32 v101, v74, v75\n\t" \
    "v_cvt_pk_bf16_f32 v102, v76, v77\n\t" \
    "v_cvt_pk_bf16_f32 v103, v78, v79\n\t" \
    "s_waitcnt lgkmcnt(0)\n\t" \
    "v_mfma_f32_32x32x16_bf16 v[64:79], v[116:119], v[104:107], v[32:47]\n\t" \
    "v_exp_f32 v80, v80\n\t" \
    "v_exp_f32 v81, v81\n\t" \
    "v_exp_f32 v82, v82\n\t" \
    "v_exp_f32 v83, v83\n\t" \
    "v_exp_f32 v84, v84\n\t" \
    "v_cvt_pk_bf16_f32 v96, v80, v81\n\t" \
    "v_cvt_pk_bf16_f32 v97, v82, v83\n\t" \
    "v_mfma_f32_32x32x16_bf16 v[0:15], v[124:127], v[100:103], v[0:15]\n\t" \
    "v_exp_f32 v85, v85\n\t" \
    "v_exp_f32 v86, v86\n\t" \
    "v_exp_f32 v87, v87\n\t" \
    "v_exp_f32 v88, v88\n\t" \
    "v_cvt_pk_bf16_f32 v98, v84, v85\n\t" \
    "v_cvt_pk_bf16_f32 v99, v86, v87\n\t" \
    "v_exp_f32 v89, v89\n\t" \
    "v_exp_f32 v90, v90\n\t" \
    "v_mfma_f32_32x32x16_bf16 v[16:31], v[120:123], v[96:99], 0\n\t" \
    "v_exp_f32 v91, v91\n\t" \
    "v_exp_f32 v92, v92\n\t" \
    "v_exp_f32 v93, v93\n\t" \
    "v_exp_f32 v94, v94\n\t" \
    "v_exp_f32 v95, v95\n\t" \
    "v_cvt_pk_bf16_f32 v100, v88, v89\n\t" \
    "v_cvt_pk_bf16_f32 v101, v90, v91\n\t" \
    "v_cvt_pk_bf16_f32 v102, v92, v93\n\t" \
    "v_cvt_pk_bf16_f32 v103, v94, v95\n\t" \
    "v_mfma_f32_32x32x16_bf16 v[80:95], v[116:119], v[108:111], v[48:63]\n\t" \
    "ds_read_b128 v[112:115], %[ka] offset:2048\n\t" \
    "v_exp_f32 v64, v64\n\t" \
    "v_exp_f32 v65, v65\n\t" \
    "v_exp_f32 v66, v66\n\t" \
    "v_exp_f32 v67, v67\n\t" \
    "v_exp_f32 v68, v68\n\t" \
    "v_cvt_pk_bf16_f32 v96, v64, v65\n\t" \
    "v_cvt_pk_bf16_f32 v97, v66, v67\n\t" \
    "v_mfma_f32_32x32x16_bf16 v[16:31], v[124:127], v[100:103], v[16:31]\n\t" \
    "s_mov_b64 exec, %[vm]\n\t" \
    "ds_read_b128 v[120:123], %[va] offset:2048\n\t" \
    "ds_read_b128 v[124:127], %[va] offset:2560\n\t" \
    "s_mov_b64 exec, -1\n\t" \
    "v_exp_f32 v69, v69\n\t" \
    "v_exp_f32 v70, v70\n\t" \
    "v_exp_f32 v71, v71\n\t" \
    "v_exp_f32 v72, v72\n\t" \
    "v_cvt_pk_bf16_f32 v98, v68, v69\n\t" \
    "v_cvt_pk_bf16_f32 v99, v70, v71\n\t" \
    "v_exp_f32 v73, v73\n\t" \
    "v_exp_f32 v74, v74\n\t" \
    "v_mfma_f32_32x32x16_bf16 v[0:15], v[128:131], v[96:99], v[0:15]\n\t" \
    "v_exp_f32 v75, v75\n\t" \
    "v_exp_f32 v76, v76\n\t" \
    "v_exp_f32 v77, v77\n\t" \
    "v_exp_f32 v78, v78\n\t" \
    "v_exp_f32 v79, v79\n\t" \
    "v_cvt_pk_bf16_f32 v100, v72, v73\n\t" \
    "v_cvt_pk_bf16_f32 v101, v74, v75\n\t" \
    "v_cvt_pk_bf16_f32 v102, v76, v77\n\t" \
    "v_cvt_pk_bf16_f32 v103, v78, v79\n\t" \
    "s_waitcnt lgkmcnt(0)\n\t" \
    "v_mfma_f32_32x32x16_bf16 v[64:79], v[112:115], v[104:107], v[32:47]\n\t" \
    "v_exp_f32 v80, v80\n\t" \
    "v_exp_f32 v81, v81\n\t" \
    "v_exp_f32 v82, v82\n\t" \
    "v_exp_f32 v83, v83\n\t" \
    "v_exp_f32 v84, v84\n\t" \
    "v_cvt_pk_bf16_f32 v96, v80, v81\n\t" \
    "v_cvt_pk_bf16_f32 v97, v82, v83\n\t" \
    "v_mfma_f32_32x32x16_bf16 v[0:15], v[132:135], v[100:103], v[0:15]\n\t" \
    "v_exp_f32 v85, v85\n\t" \
    "v_exp_f32 v86, v86\n\t" \
    "v_exp_f32 v87, v87\n\t" \
    "v_exp_f32 v88, v88\n\t" \
    "v_cvt_pk_bf16_f32 v98, v84, v85\n\t" \
    "v_cvt_pk_bf16_f32 v99, v86, v87\n\t" \
    "v_exp_f32 v89, v89\n\t" \
    "v_exp_f32 v90, v90\n\t" \
    "v_mfma_f32_32x32x16_bf16 v[16:31], v[128:131], v[96:99], v[16:31]\n\t" \
    "v_exp_f32 v91, v91\n\t" \
    "v_exp_f32 v92, v92\n\t" \
    "v_exp_f32 v93, v93\n\t" \
    "v_exp_f32 v94, v94\n\t" \
    "v_exp_f32 v95, v95\n\t" \
    "v_cvt_pk_bf16_f32 v100, v88, v89\n\t" \
    "v_cvt_pk_bf16_f32 v101, v90, v91\n\t" \
    "v_cvt_pk_bf16_f32 v102, v92, v93\n\t" \
    "v_cvt_pk_bf16_f32 v103, v94, v95\n\t" \
    "v_mfma_f32_32x32x16_bf16 v[80:95], v[112:115], v[108:111], v[48:63]\n\t" \
    "ds_read_b128 v[116:119], %[ka] offset:3072\n\t" \
    "v_exp_f32 v64, v64\n\t" \
    "v_exp_f32 v65, v65\n\t" \
    "v_exp_f32 v66, v66\n\t" \
    "v_exp_f32 v67, v67\n\t" \
    "v_exp_f32 v68, v68\n\t" \
    "v_cvt_pk_bf16_f32 v96, v64, v65\n\t" \
    "v_cvt_pk_bf16_f32 v97, v66, v67\n\t" \
    "v_mfma_f32_32x32x16_bf16 v[16:31], v[132:135], v[100:103], v[16:31]\n\t" \
    "s_mov_b64 exec, %[vm]\n\t" \
    "ds_read_b128 v[128:131], %[va] offset:3072\n\t" \
    "ds_read_b128 v[132:135], %[va] offset:3584\n\t" \
    "s_mov_b64 exec, -1\n\t" \
    "v_exp_f32 v69, v69\n\t" \
    "v_exp_f32 v70, v70\n\t" \
    "v_exp_f32 v71, v71\n\t" \
    "v_exp_f32 v72, v72\n\t" \
    "v_cvt_pk_bf16_f32 v98, v68, v69\n\t" \
    "v_cvt_pk_bf16_f32 v99, v70, v71\n\t" \
    "v_exp_f32 v73, v73\n\t" \
    "v_exp_f32 v74, v74\n\t" \
    "v_mfma_f32_32x32x16_bf16 v[0:15], v[120:123], v[96:99], v[0:15]\n\t" \
    "v_exp_f32 v75, v75\n\t" \
    "v_exp_f32 v76, v76\n\t" \
    "v_exp_f32 v77, v77\n\t" \
    "v_exp_f32 v78, v78\n\t" \
    "v_exp_f32 v79, v79\n\t" \
    "v_cvt_pk_bf16_f32 v100, v72, v73\n\t" \
    "v_cvt_pk_bf16_f32 v101, v74, v75\n\t" \
    "v_cvt_pk_bf16_f32 v102, v76, v77\n\t" \
    "v_cvt_pk_bf16_f32 v103, v78, v79\n\t" \
    "s_waitcnt lgkmcnt(0)\n\t" \
    "v_mfma_f32_32x32x16_bf16 v[64:79], v[116:119], v[104:107], v[32:47]\n\t" \
    "v_exp_f32 v80, v80\n\t" \
    "v_exp_f32 v81, v81\n\t" \
    "v_exp_f32 v82, v82\n\t" \
    "v_exp_f32 v83, v83\n\t" \
    "v_exp_f32 v84, v84\n\t" \
    "v_cvt_pk_bf16_f32 v96, v80, v81\n\t" \
    "v_cvt_pk_bf16_f32 v97, v82, v83\n\t" \
    "v_mfma_f32_32x32x16_bf16 v[0:15], v[124:127], v[100:103], v[0:15]\n\t" \
    "v_exp_f32 v85, v85\n\t" \
    "v_exp_f32 v86, v86\n\t" \
    "v_exp_f32 v87, v87\n\t" \
    "v_exp_f32 v88, v88\n\t" \
    "v_cvt_pk_bf16_f32 v98, v84, v85\n\t" \
    "v_cvt_pk_bf16_f32 v99, v86, v87\n\t" \
    "v_exp_f32 v89, v89\n\t" \
    "v_exp_f32 v90, v90\n\t" \
    "v_mfma_f32_32x32x16_bf16 v[16:31], v[120:123], v[96:99], v[16:31]\n\t" \
    "v_exp_f32 v91, v91\n\t" \
    "v_exp_f32 v92, v92\n\t" \
    "v_exp_f32 v93, v93\n\t" \
    "v_exp_f32 v94, v94\n\t" \
    "v_exp_f32 v95, v95\n\t" \
    "v_cvt_pk_bf16_f32 v100, v88, v89\n\t" \
    "v_cvt_pk_bf16_f32 v101, v90, v91\n\t" \
    "v_cvt_pk_bf16_f32 v102, v92, v93\n\t" \
    "v_cvt_pk_bf16_f32 v103, v94, v95\n\t" \
    "v_mfma_f32_32x32x16_bf16 v[80:95], v[116:119], v[108:111], v[48:63]\n\t" \
    "ds_read_b128 v[112:115], %[ka] offset:4096\n\t" \
    "v_exp_f32 v64, v64\n\t" \
    "v_exp_f32 v65, v65\n\t" \
    "v_exp_f32 v66, v66\n\t" \
    "v_exp_f32 v67, v67\n\t" \
    "v_exp_f32 v68, v68\n\t" \
    "v_cvt_pk_bf16_f32 v96, v64, v65\n\t" \
    "v_cvt_pk_bf16_f32 v97, v66, v67\n\t" \
    "v_mfma_f32_32x32x16_bf16 v[16:31], v[124:127], v[100:103], v[16:31]\n\t" \
    "s_mov_b64 exec, %[vm]\n\t" \
    "ds_read_b128 v[120:123], %[va] offset:4096\n\t" \
    "ds_read_b128 v[124:127], %[va] offset:4608\n\t" \
    "s_mov_b64 exec, -1\n\t" \
    "v_exp_f32 v69, v69\n\t" \
    "v_exp_f32 v70, v70\n\t" \
    "v_exp_f32 v71, v71\n\t" \
    "v_exp_f32 v72, v72\n\t" \
    "v_cvt_pk_bf16_f32 v98, v68, v69\n\t" \
    "v_cvt_pk_bf16_f32 v99, v70, v71\n\t" \
    "v_exp_f32 v73, v73\n\t" \
    "v_exp_f32 v74, v74\n\t" \
    "v_mfma_f32_32x32x16_bf16 v[0:15], v[128:131], v[96:99], v[0:15]\n\t" \
    "v_exp_f32 v75, v75\n\t" \
    "v_exp_f32 v76, v76\n\t" \
    "v_exp_f32 v77, v77\n\t" \
    "v_exp_f32 v78, v78\n\t" \
    "v_exp_f32 v79, v79\n\t" \
    "v_cvt_pk_bf16_f32 v100, v72, v73\n\t" \
    "v_cvt_pk_bf16_f32 v101, v74, v75\n\t" \
    "v_cvt_pk_bf16_f32 v102, v76, v77\n\t" \
    "v_cvt_pk_bf16_f32 v103, v78, v79\n\t" \
    "s_waitcnt lgkmcnt(0)\n\t" \
    "v_mfma_f32_32x32x16_bf16 v[64:79], v[112:115], v[104:107], v[32:47]\n\t" \
    "v_exp_f32 v80, v80\n\t" \
    "v_exp_f32 v81, v81\n\t" \
    "v_exp_f32 v82, v82\n\t" \
    "v_exp_f32 v83, v83\n\t" \
    "v_exp_f32 v84, v84\n\t" \
    "v_cvt_pk_bf16_f32 v96, v80, v81\n\t" \
    "v_cvt_pk_bf16_f32 v97, v82, v83\n\t" \
    "v_mfma_f32_32x32x16_bf16 v[0:15], v[132:135], v[100:103], v[0:15]\n\t" \
    "v_exp_f32 v85, v85\n\t" \
    "v_exp_f32 v86, v86\n\t" \
    "v_exp_f32 v87, v87\n\t" \
    "v_exp_f32 v88, v88\n\t" \
    "v_cvt_pk_bf16_f32 v98, v84, v85\n\t" \
    "v_cvt_pk_bf16_f32 v99, v86, v87\n\t" \
    "v_exp_f32 v89, v89\n\t" \
    "v_exp_f32 v90, v90\n\t" \
    "v_mfma_f32_32x32x16_bf16 v[16:31], v[128:131], v[96:99], v[16:31]\n\t" \
    "v_exp_f32 v91, v91\n\t" \
    "v_exp_f32 v92, v92\n\t" \
    "v_exp_f32 v93, v93\n\t" \
    "v_exp_f32 v94, v94\n\t" \
    "v_exp_f32 v95, v95\n\t" \
    "v_cvt_pk_bf16_f32 v100, v88, v89\n\t" \
    "v_cvt_pk_bf16_f32 v101, v90, v91\n\t" \
    "v_cvt_pk_bf16_f32 v102, v92, v93\n\t" \
    "v_cvt_pk_bf16_f32 v103, v94, v95\n\t" \
    "v_mfma_f32_32x32x16_bf16 v[80:95], v[112:115], v[108:111], v[48:63]\n\t" \
    "ds_read_b128 v[116:119], %[ka] offset:5120\n\t" \
    "v_exp_f32 v64, v64\n\t" \
    "v_exp_f32 v65, v65\n\t" \
    "v_exp_f32 v66, v66\n\t" \
    "v_exp_f32 v67, v67\n\t" \
    "v_exp_f32 v68, v68\n\t" \
    "v_cvt_pk_bf16_f32 v96, v64, v65\n\t" \
    "v_cvt_pk_bf16_f32 v97, v66, v67\n\t" \
    "v_mfma_f32_32x32x16_bf16 v[16:31], v[132:135], v[100:103], v[16:31]\n\t" \
    "s_mov_b64 exec, %[vm]\n\t" \
    "ds_read_b128 v[128:131], %[va] offset:5120\n\t" \
    "ds_read_b128 v[132:135], %[va] offset:5632\n\t" \
    "s_mov_b64 exec, -1\n\t" \
    "v_exp_f32 v69, v69\n\t" \
    "v_exp_f32 v70, v70\n\t" \
    "v_exp_f32 v71, v71\n\t" \
    "v_exp_f32 v72, v72\n\t" \
    "v_cvt_pk_bf16_f32 v98, v68, v69\n\t" \
    "v_cvt_pk_bf16_f32 v99, v70, v71\n\t" \
    "v_exp_f32 v73, v73\n\t" \
    "v_exp_f32 v74, v74\n\t" \
    "v_mfma_f32_32x32x16_bf16 v[0:15], v[120:123], v[96:99], v[0:15]\n\t" \
    "v_exp_f32 v75, v75\n\t" \
    "v_exp_f32 v76, v76\n\t" \
    "v_exp_f32 v77, v77\n\t" \
    "v_exp_f32 v78, v78\n\t" \
    "v_exp_f32 v79, v79\n\t" \
    "v_cvt_pk_bf16_f32 v100, v72, v73\n\t" \
    "v_cvt_pk_bf16_f32 v101, v74, v75\n\t" \
    "v_cvt_pk_bf16_f32 v102, v76, v77\n\t" \
    "v_cvt_pk_bf16_f32 v103, v78, v79\n\t" \
    "s_waitcnt lgkmcnt(0)\n\t" \
    "v_mfma_f32_32x32x16_bf16 v[64:79], v[116:119], v[104:107], v[32:47]\n\t" \
    "v_exp_f32 v80, v80\n\t" \
    "v_exp_f32 v81, v81\n\t" \
    "v_exp_f32 v82, v82\n\t" \
    "v_exp_f32 v83, v83\n\t" \
    "v_exp_f32 v84, v84\n\t" \
    "v_cvt_pk_bf16_f32 v96, v80, v81\n\t" \
    "v_cvt_pk_bf16_f32 v97, v82, v83\n\t" \
    "v_mfma_f32_32x32x16_bf16 v[0:15], v[124:127], v[100:103], v[0:15]\n\t" \
    "v_exp_f32 v85, v85\n\t" \
    "v_exp_f32 v86, v86\n\t" \
    "v_exp_f32 v87, v87\n\t" \
    "v_exp_f32 v88, v88\n\t" \
    "v_cvt_pk_bf16_f32 v98, v84, v85\n\t" \
    "v_cvt_pk_bf16_f32 v99, v86, v87\n\t" \
    "v_exp_f32 v89, v89\n\t" \
    "v_exp_f32 v90, v90\n\t" \
    "v_mfma_f32_32x32x16_bf16 v[16:31], v[120:123], v[96:99], v[16:31]\n\t" \
    "v_exp_f32 v91, v91\n\t" \
    "v_exp_f32 v92, v92\n\t" \
    "v_exp_f32 v93, v93\n\t" \
    "v_exp_f32 v94, v94\n\t" \
    "v_exp_f32 v95, v95\n\t" \
    "v_cvt_pk_bf16_f32 v100, v88, v89\n\t" \
    "v_cvt_pk_bf16_f32 v101, v90, v91\n\t" \
    "v_cvt_pk_bf16_f32 v102, v92, v93\n\t" \
    "v_cvt_pk_bf16_f32 v103, v94, v95\n\t" \
    "v_mfma_f32_32x32x16_bf16 v[80:95], v[116:119], v[108:111], v[48:63]\n\t" \
    "ds_read_b128 v[112:115], %[ka] offset:6144\n\t" \
    "v_exp_f32 v64, v64\n\t" \
    "v_exp_f32 v65, v65\n\t" \
    "v_exp_f32 v66, v66\n\t" \
    "v_exp_f32 v67, v67\n\t" \
    "v_exp_f32 v68, v68\n\t" \
    "v_cvt_pk_bf16_f32 v96, v64, v65\n\t" \
    "v_cvt_pk_bf16_f32 v97, v66, v67\n\t" \
    "v_mfma_f32_32x32x16_bf16 v[16:31], v[124:127], v[100:103], v[16:31]\n\t" \
    "s_mov_b64 exec, %[vm]\n\t" \
    "ds_read_b128 v[120:123], %[va] offset:6144\n\t" \
    "ds_read_b128 v[124:127], %[va] offset:6656\n\t" \
    "s_mov_b64 exec, -1\n\t" \
    "v_exp_f32 v69, v69\n\t" \
    "v_exp_f32 v70, v70\n\t" \
    "v_exp_f32 v71, v71\n\t" \
    "v_exp_f32 v72, v72\n\t" \
    "v_cvt_pk_bf16_f32 v98, v68, v69\n\t" \
    "v_cvt_pk_bf16_f32 v99, v70, v71\n\t" \
    "v_exp_f32 v73, v73\n\t" \
    "v_exp_f32 v74, v74\n\t" \
    "v_mfma_f32_32x32x16_bf16 v[0:15], v[128:131], v[96:99], v[0:15]\n\t" \
    "v_exp_f32 v75, v75\n\t" \
    "v_exp_f32 v76, v76\n\t" \
    "v_exp_f32 v77, v77\n\t" \
    "v_exp_f32 v78, v78\n\t" \
    "v_exp_f32 v79, v79\n\t" \
    "v_cvt_pk_bf16_f32 v100, v72, v73\n\t" \
    "v_cvt_pk_bf16_f32 v101, v74, v75\n\t" \
    "v_cvt_pk_bf16_f32 v102, v76, v77\n\t" \
    "v_cvt_pk_bf16_f32 v103, v78, v79\n\t" \
    "s_waitcnt lgkmcnt(0)\n\t" \
    "v_mfma_f32_32x32x16_bf16 v[64:79], v[112:115], v[104:107], v[32:47]\n\t" \
    "v_exp_f32 v80, v80\n\t" \
    "v_exp_f32 v81, v81\n\t" \
    "v_exp_f32 v82, v82\n\t" \
    "v_exp_f32 v83, v83\n\t" \
    "v_exp_f32 v84, v84\n\t" \
    "v_cvt_pk_bf16_f32 v96, v80, v81\n\t" \
    "v_cvt_pk_bf16_f32 v97, v82, v83\n\t" \
    "v_mfma_f32_32x32x16_bf16 v[0:15], v[132:135], v[100:103], v[0:15]\n\t" \
    "v_exp_f32 v85, v85\n\t" \
    "v_exp_f32 v86, v86\n\t" \
    "v_exp_f32 v87, v87\n\t" \
    "v_exp_f32 v88, v88\n\t" \
    "v_cvt_pk_bf16_f32 v98, v84, v85\n\t" \
    "v_cvt_pk_bf16_f32 v99, v86, v87\n\t" \
    "v_exp_f32 v89, v89\n\t" \
    "v_exp_f32 v90, v90\n\t" \
    "v_mfma_f32_32x32x16_bf16 v[16:31], v[128:131], v[96:99], v[16:31]\n\t" \
    "v_exp_f32 v91, v91\n\t" \
    "v_exp_f32 v92, v92\n\t" \
    "v_exp_f32 v93, v93\n\t" \
    "v_exp_f32 v94, v94\n\t" \
    "v_exp_f32 v95, v95\n\t" \
    "v_cvt_pk_bf16_f32 v100, v88, v89\n\t" \
    "v_cvt_pk_bf16_f32 v101, v90, v91\n\t" \
    "v_cvt_pk_bf16_f32 v102, v92, v93\n\t" \
    "v_cvt_pk_bf16_f32 v103, v94, v95\n\t" \
    "v_mfma_f32_32x32x16_bf16 v[80:95], v[112:115], v[108:111], v[48:63]\n\t" \
    "ds_read_b128 v[116:119], %[ka] offset:7168\n\t" \
    "v_exp_f32 v64, v64\n\t" \
    "v_exp_f32 v65, v65\n\t" \
    "v_exp_f32 v66, v66\n\t" \
    "v_exp_f32 v67, v67\n\t" \
    "v_exp_f32 v68, v68\n\t" \
    "v_cvt_pk_bf16_f32 v96, v64, v65\n\t" \
    "v_cvt_pk_bf16_f32 v97, v66, v67\n\t" \
    "v_mfma_f32_32x32x16_bf16 v[16:31], v[132:135], v[100:103], v[16:31]\n\t" \
    "s_mov_b64 exec, %[vm]\n\t" \
    "ds_read_b128 v[128:131], %[va] offset:7168\n\t" \
    "ds_read_b128 v[132:135], %[va] offset:7680\n\t" \
    "s_mov_b64 exec, -1\n\t" \
    "v_exp_f32 v69, v69\n\t" \
    "v_exp_f32 v70, v70\n\t" \
    "v_exp_f32 v71, v71\n\t" \
    "v_exp_f32 v72, v72\n\t" \
    "v_cvt_pk_bf16_f32 v98, v68, v69\n\t" \
    "v_cvt_pk_bf16_f32 v99, v70, v71\n\t" \
    "v_exp_f32 v73, v73\n\t" \
    "v_exp_f32 v74, v74\n\t" \
    "v_mfma_f32_32x32x16_bf16 v[0:15], v[120:123], v[96:99], v[0:15]\n\t" \
    "v_exp_f32 v75, v75\n\t" \
    "v_exp_f32 v76, v76\n\t" \
    "v_exp_f32 v77, v77\n\t" \
    "v_exp_f32 v78, v78\n\t" \
    "v_exp_f32 v79, v79\n\t" \
    "v_cvt_pk_bf16_f32 v100, v72, v73\n\t" \
    "v_cvt_pk_bf16_f32 v101, v74, v75\n\t" \
    "v_cvt_pk_bf16_f32 v102, v76, v77\n\t" \
    "v_cvt_pk_bf16_f32 v103, v78, v79\n\t" \
    "s_waitcnt lgkmcnt(0)\n\t" \
    "v_mfma_f32_32x32x16_bf16 v[64:79], v[116:119], v[104:107], v[32:47]\n\t" \
    "v_exp_f32 v80, v80\n\t" \
    "v_exp_f32 v81, v81\n\t" \
    "v_exp_f32 v82, v82\n\t" \
    "v_exp_f32 v83, v83\n\t" \
    "v_exp_f32 v84, v84\n\t" \
    "v_cvt_pk_bf16_f32 v96, v80, v81\n\t" \
    "v_cvt_pk_bf16_f32 v97, v82, v83\n\t" \
    "v_mfma_f32_32x32x16_bf16 v[0:15], v[124:127], v[100:103], v[0:15]\n\t" \
    "v_exp_f32 v85, v85\n\t" \
    "v_exp_f32 v86, v86\n\t" \
    "v_exp_f32 v87, v87\n\t" \
    "v_exp_f32 v88, v88\n\t" \
    "v_cvt_pk_bf16_f32 v98, v84, v85\n\t" \
    "v_cvt_pk_bf16_f32 v99, v86, v87\n\t" \
    "v_exp_f32 v89, v89\n\t" \
    "v_exp_f32 v90, v90\n\t" \
    "v_mfma_f32_32x32x16_bf16 v[16:31], v[120:123], v[96:99], v[16:31]\n\t" \
    "v_exp_f32 v91, v91\n\t" \
    "v_exp_f32 v92, v92\n\t" \
    "v_exp_f32 v93, v93\n\t" \
    "v_exp_f32 v94, v94\n\t" \
    "v_exp_f32 v95, v95\n\t" \
    "v_cvt_pk_bf16_f32 v100, v88, v89\n\t" \
    "v_cvt_pk_bf16_f32 v101, v90, v91\n\t" \
    "v_cvt_pk_bf16_f32 v102, v92, v93\n\t" \
    "v_cvt_pk_bf16_f32 v103, v94, v95\n\t" \
    "s_cmp_eq_u32 %[lead], 0\n\t" \
    "s_cbranch_scc1 .Latt_b%=\n\t" \
    "s_barrier\n\t" \
    ".Latt_b%=:\n\t" \
    "v_mfma_f32_32x32x16_bf16 v[80:95], v[116:119], v[108:111], v[48:63]\n\t" \
    "ds_read_b128 v[112:115], %[ka] offset:8192\n\t" \
    "v_exp_f32 v64, v64\n\t" \
    "v_exp_f32 v65, v65\n\t" \
    "v_exp_f32 v66, v66\n\t" \
    "v_exp_f32 v67, v67\n\t" \
    "v_exp_f32 v68, v68\n\t" \
    "v_cvt_pk_bf16_f32 v96, v64, v65\n\t" \
    "v_cvt_pk_bf16_f32 v97, v66, v67\n\t" \
    "v_mfma_f32_32x32x16_bf16 v[16:31], v[124:127], v[100:103], v[16:31]\n\t" \
    "s_mov_b64 exec, %[vm]\n\t" \
    "ds_read_b128 v[120:123], %[va] offset:8192\n\t" \
    "ds_read_b128 v[124:127], %[va] offset:8704\n\t" \
    "s_mov_b64 exec, -1\n\t" \
    "v_exp_f32 v69, v69\n\t" \
    "v_exp_f32 v70, v70\n\t" \
    "v_exp_f32 v71, v71\n\t" \
    "v_exp_f32 v72, v72\n\t" \
    "v_cvt_pk_bf16_f32 v98, v68, v69\n\t" \
    "v_cvt_pk_bf16_f32 v99, v70, v71\n\t" \
    "v_exp_f32 v73, v73\n\t" \
    "v_exp_f32 v74, v74\n\t" \
    "v_mfma_f32_32x32x16_bf16 v[0:15], v[128:131], v[96:99], v[0:15]\n\t" \
    "v_exp_f32 v75, v75\n\t" \
    "v_exp_f32 v76, v76\n\t" \
    "v_exp_f32 v77, v77\n\t" \
    "v_exp_f32 v78, v78\n\t" \
    "v_exp_f32 v79, v79\n\t" \
    "v_cvt_pk_bf16_f32 v100, v72, v73\n\t" \
    "v_cvt_pk_bf16_f32 v101, v74, v75\n\t" \
    "v_cvt_pk_bf16_f32 v102, v76, v77\n\t" \
    "v_cvt_pk_bf16_f32 v103, v78, v79\n\t" \
    "s_waitcnt lgkmcnt(0)\n\t" \
    "v_mfma_f32_32x32x16_bf16 v[64:79], v[112:115], v[104:107], v[32:47]\n\t" \
    "v_exp_f32 v80, v80\n\t" \
    "v_exp_f32 v81, v81\n\t" \
    "v_exp_f32 v82, v82\n\t" \
    "v_exp_f32 v83, v83\n\t" \
    "v_exp_f32 v84, v84\n\t" \
    "v_cvt_pk_bf16_f32 v96, v80, v81\n\t" \
    "v_cvt_pk_bf16_f32 v97, v82, v83\n\t" \
    "v_mfma_f32_32x32x16_bf16 v[0:15], v[132:135], v[100:103], v[0:15]\n\t" \
    "v_exp_f32 v85, v85\n\t" \
    "v_exp_f32 v86, v86\n\t" \
    "v_exp_f32 v87, v87\n\t" \
    "v_exp_f32 v88, v88\n\t" \
    "v_cvt_pk_bf16_f32 v98, v84, v85\n\t" \
    "v_cvt_pk_bf16_f32 v99, v86, v87\n\t" \
    "v_exp_f32 v89, v89\n\t" \
    "v_exp_f32 v90, v90\n\t" \
    "v_mfma_f32_32x32x16_bf16 v[16:31], v[128:131], v[96:99], v[16:31]\n\t" \
    "v_exp_f32 v91, v91\n\t" \
    "v_exp_f32 v92, v92\n\t" \
    "v_exp_f32 v93, v93\n\t" \
    "v_exp_f32 v94, v94\n\t" \
    "v_exp_f32 v95, v95\n\t" \
    "v_cvt_pk_bf16_f32 v100, v88, v89\n\t" \
    "v_cvt_pk_bf16_f32 v101, v90, v91\n\t" \
    "v_cvt_pk_bf16_f32 v102, v92, v93\n\t" \
    "v_cvt_pk_bf16_f32 v103, v94, v95\n\t" \
    "v_mfma_f32_32x32x16_bf16 v[80:95], v[112:115], v[108:111], v[48:63]\n\t" \
    "ds_read_b128 v[116:119], %[ka] offset:9216\n\t" \
    "v_exp_f32 v64, v64\n\t" \
    "v_exp_f32 v65, v65\n\t" \
    "v_exp_f32 v66, v66\n\t" \
    "v_exp_f32 v67, v67\n\t" \
    "v_exp_f32 v68, v68\n\t" \
    "v_cvt_pk_bf16_f32 v96, v64, v65\n\t" \
    "v_cvt_pk_bf16_f32 v97, v66, v67\n\t" \
    "v_mfma_f32_32x32x16_bf16 v[16:31], v[132:135], v[100:103], v[16:31]\n\t" \
    "s_mov_b64 exec, %[vm]\n\t" \
    "ds_read_b128 v[128:131], %[va] offset:9216\n\t" \
    "ds_read_b128 v[132:135], %[va] offset:9728\n\t" \
    "s_mov_b64 exec, -1\n\t" \
    "v_exp_f32 v69, v69\n\t" \
    "v_exp_f32 v70, v70\n\t" \
    "v_exp_f32 v71, v71\n\t" \
    "v_exp_f32 v72, v72\n\t" \
    "v_cvt_pk_bf16_f32 v98, v68, v69\n\t" \
    "v_cvt_pk_bf16_f32 v99, v70, v71\n\t" \
    "v_exp_f32 v73, v73\n\t" \
    "v_exp_f32 v74, v74\n\t" \
    "v_mfma_f32_32x32x16_bf16 v[0:15], v[120:123], v[96:99], v[0:15]\n\t" \
    "v_exp_f32 v75, v75\n\t" \
    "v_exp_f32 v76, v76\n\t" \
    "v_exp_f32 v77, v77\n\t" \
    "v_exp_f32 v78, v78\n\t" \
    "v_exp_f32 v79, v79\n\t" \
    "v_cvt_pk_bf16_f32 v100, v72, v73\n\t" \
    "v_cvt_pk_bf16_f32 v101, v74, v75\n\t" \
    "v_cvt_pk_bf16_f32 v102, v76, v77\n\t" \
    "v_cvt_pk_bf16_f32 v103, v78, v79\n\t" \
    "s_waitcnt lgkmcnt(0)\n\t" \
    "v_mfma_f32_32x32x16_bf16 v[64:79], v[116:119], v[104:107], v[32:47]\n\t" \
    "v_exp_f32 v80, v80\n\t" \
    "v_exp_f32 v81, v81\n\t" \
    "v_exp_f32 v82, v82\n\t" \
    "v_exp_f32 v83, v83\n\t" \
    "v_exp_f32 v84, v84\n\t" \
    "v_cvt_pk_bf16_f32 v96, v80, v81\n\t" \
    "v_cvt_pk_bf16_f32 v97, v82, v83\n\t" \
    "v_mfma_f32_32x32x16_bf16 v[0:15], v[124:127], v[100:103], v[0:15]\n\t" \
    "v_exp_f32 v85, v85\n\t" \
    "v_exp_f32 v86, v86\n\t" \
    "v_exp_f32 v87, v87\n\t" \
    "v_exp_f32 v88, v88\n\t" \
    "v_cvt_pk_bf16_f32 v98, v84, v85\n\t" \
    "v_cvt_pk_bf16_f32 v99, v86, v87\n\t" \
    "v_exp_f32 v89, v89\n\t" \
    "v_exp_f32 v90, v90\n\t" \
    "v_mfma_f32_32x32x16_bf16 v[16:31], v[120:123], v[96:99], v[16:31]\n\t" \
    "v_exp_f32 v91, v91\n\t" \
    "v_exp_f32 v92, v92\n\t" \
    "v_exp_f32 v93, v93\n\t" \
    "v_exp_f32 v94, v94\n\t" \
    "v_exp_f32 v95, v95\n\t" \
    "v_cvt_pk_bf16_f32 v100, v88, v89\n\t" \
    "v_cvt_pk_bf16_f32 v101, v90, v91\n\t" \
    "v_cvt_pk_bf16_f32 v102, v92, v93\n\t" \
    "v_cvt_pk_bf16_f32 v103, v94, v95\n\t" \
    "v_mfma_f32_32x32x16_bf16 v[80:95], v[116:119], v[108:111], v[48:63]\n\t" \
    "ds_read_b128 v[112:115], %[ka] offset:10240\n\t" \
    "v_exp_f32 v64, v64\n\t" \
    "v_exp_f32 v65, v65\n\t" \
    "v_exp_f32 v66, v66\n\t" \
    "v_exp_f32 v67, v67\n\t" \
    "v_exp_f32 v68, v68\n\t" \
    "v_cvt_pk_bf16_f32 v96, v64, v65\n\t" \
    "v_cvt_pk_bf16_f32 v97, v66, v67\n\t" \
    "v_mfma_f32_32x32x16_bf16 v[16:31], v[124:127], v[100:103], v[16:31]\n\t" \
    "s_mov_b64 exec, %[vm]\n\t" \
    "ds_read_b128 v[120:123], %[va] offset:10240\n\t" \
    "ds_read_b128 v[124:127], %[va] offset:10752\n\t" \
    "s_mov_b64 exec, -1\n\t" \
    "v_exp_f32 v69, v69\n\t" \
    "v_exp_f32 v70, v70\n\t" \
    "v_exp_f32 v71, v71\n\t" \
    "v_exp_f32 v72, v72\n\t" \
    "v_cvt_pk_bf16_f32 v98, v68, v69\n\t" \
    "v_cvt_pk_bf16_f32 v99, v70, v71\n\t" \
    "v_exp_f32 v73, v73\n\t" \
    "v_exp_f32 v74, v74\n\t" \
    "v_mfma_f32_32x32x16_bf16 v[0:15], v[128:131], v[96:99], v[0:15]\n\t" \
    "v_exp_f32 v75, v75\n\t" \
    "v_exp_f32 v76, v76\n\t" \
    "v_exp_f32 v77, v77\n\t" \
    "v_exp_f32 v78, v78\n\t" \
    "v_exp_f32 v79, v79\n\t" \
    "v_cvt_pk_bf16_f32 v100, v72, v73\n\t" \
    "v_cvt_pk_bf16_f32 v101, v74, v75\n\t" \
    "v_cvt_pk_bf16_f32 v102, v76, v77\n\t" \
    "v_cvt_pk_bf16_f32 v103, v78, v79\n\t" \
    "s_waitcnt lgkmcnt(0)\n\t" \
    "v_mfma_f32_32x32x16_bf16 v[64:79], v[112:115], v[104:107], v[32:47]\n\t" \
    "v_exp_f32 v80, v80\n\t" \
    "v_exp_f32 v81, v81\n\t" \
    "v_exp_f32 v82, v82\n\t" \
    "v_exp_f32 v83, v83\n\t" \
    "v_exp_f32 v84, v84\n\t" \
    "v_cvt_pk_bf16_f32 v96, v80, v81\n\t" \
    "v_cvt_pk_bf16_f32 v97, v82, v83\n\t" \
    "v_mfma_f32_32x32x16_bf16 v[0:15], v[132:135], v[100:103], v[0:15]\n\t" \
    "v_exp_f32 v85, v85\n\t" \
    "v_exp_f32 v86, v86\n\t" \
    "v_exp_f32 v87, v87\n\t" \
    "v_exp_f32 v88, v88\n\t" \
    "v_cvt_pk_bf16_f32 v98, v84, v85\n\t" \
    "v_cvt_pk_bf16_f32 v99, v86, v87\n\t" \
    "v_exp_f32 v89, v89\n\t" \
    "v_exp_f32 v90, v90\n\t" \
    "v_mfma_f32_32x32x16_bf16 v[16:31], v[128:131], v[96:99], v[16:31]\n\t" \
    "v_exp_f32 v91, v91\n\t" \
    "v_exp_f32 v92, v92\n\t" \
    "v_exp_f32 v93, v93\n\t" \
    "v_exp_f32 v94, v94\n\t" \
    "v_exp_f32 v95, v95\n\t" \
    "v_cvt_pk_bf16_f32 v100, v88, v89\n\t" \
    "v_cvt_pk_bf16_f32 v101, v90, v91\n\t" \
    "v_cvt_pk_bf16_f32 v102, v92, v93\n\t" \
    "v_cvt_pk_bf16_f32 v103, v94, v95\n\t" \
    "v_mfma_f32_32x32x16_bf16 v[80:95], v[112:115], v[108:111], v[48:63]\n\t" \
    "ds_read_b128 v[116:119], %[ka] offset:11264\n\t" \
    "v_exp_f32 v64, v64\n\t" \
    "v_exp_f32 v65, v65\n\t" \
    "v_exp_f32 v66, v66\n\t" \
    "v_exp_f32 v67, v67\n\t" \
    "v_exp_f32 v68, v68\n\t" \
    "v_cvt_pk_bf16_f32 v96, v64, v65\n\t" \
    "v_cvt_pk_bf16_f32 v97, v66, v67\n\t" \
    "v_mfma_f32_32x32x16_bf16 v[16:31], v[132:135], v[100:103], v[16:31]\n\t" \
    "s_mov_b64 exec, %[vm]\n\t" \
    "ds_read_b128 v[128:131], %[va] offset:11264\n\t" \
    "ds_read_b128 v[132:135], %[va] offset:11776\n\t" \
    "s_mov_b64 exec, -1\n\t" \
    "v_exp_f32 v69, v69\n\t" \
    "v_exp_f32 v70, v70\n\t" \
    "v_exp_f32 v71, v71\n\t" \
    "v_exp_f32 v72, v72\n\t" \
    "v_cvt_pk_bf16_f32 v98, v68, v69\n\t" \
    "v_cvt_pk_bf16_f32 v99, v70, v71\n\t" \
    "v_exp_f32 v73, v73\n\t" \
    "v_exp_f32 v74, v74\n\t" \
    "v_mfma_f32_32x32x16_bf16 v[0:15], v[120:123], v[96:99], v[0:15]\n\t" \
    "v_exp_f32 v75, v75\n\t" \
    "v_exp_f32 v76, v76\n\t" \
    "v_exp_f32 v77, v77\n\t" \
    "v_exp_f32 v78, v78\n\t" \
    "v_exp_f32 v79, v79\n\t" \
    "v_cvt_pk_bf16_f32 v100, v72, v73\n\t" \
    "v_cvt_pk_bf16_f32 v101, v74, v75\n\t" \
    "v_cvt_pk_bf16_f32 v102, v76, v77\n\t" \
    "v_cvt_pk_bf16_f32 v103, v78, v79\n\t" \
    "s_waitcnt lgkmcnt(0)\n\t" \
    "v_mfma_f32_32x32x16_bf16 v[64:79], v[116:119], v[104:107], v[32:47]\n\t" \
    "v_exp_f32 v80, v80\n\t" \
    "v_exp_f32 v81, v81\n\t" \
    "v_exp_f32 v82, v82\n\t" \
    "v_exp_f32 v83, v83\n\t" \
    "v_exp_f32 v84, v84\n\t" \
    "v_cvt_pk_bf16_f32 v96, v80, v81\n\t" \
    "v_cvt_pk_bf16_f32 v97, v82, v83\n\t" \
    "v_mfma_f32_32x32x16_bf16 v[0:15], v[124:127], v[100:103], v[0:15]\n\t" \
    "v_exp_f32 v85, v85\n\t" \
    "v_exp_f32 v86, v86\n\t" \
    "v_exp_f32 v87, v87\n\t" \
    "v_exp_f32 v88, v88\n\t" \
    "v_cvt_pk_bf16_f32 v98, v84, v85\n\t" \
    "v_cvt_pk_bf16_f32 v99, v86, v87\n\t" \
    "v_exp_f32 v89, v89\n\t" \
    "v_exp_f32 v90, v90\n\t" \
    "v_mfma_f32_32x32x16_bf16 v[16:31], v[120:123], v[96:99], v[16:31]\n\t" \
    "v_exp_f32 v91, v91\n\t" \
    "v_exp_f32 v92, v92\n\t" \
    "v_exp_f32 v93, v93\n\t" \
    "v_exp_f32 v94, v94\n\t" \
    "v_exp_f32 v95, v95\n\t" \
    "v_cvt_pk_bf16_f32 v100, v88, v89\n\t" \
    "v_cvt_pk_bf16_f32 v101, v90, v91\n\t" \
    "v_cvt_pk_bf16_f32 v102, v92, v93\n\t" \
    "v_cvt_pk_bf16_f32 v103, v94, v95\n\t" \
    "v_mfma_f32_32x32x16_bf16 v[80:95], v[116:119], v[108:111], v[48:63]\n\t" \
    "ds_read_b128 v[112:115], %[ka] offset:12288\n\t" \
    "v_exp_f32 v64, v64\n\t" \
    "v_exp_f32 v65, v65\n\t" \
    "v_exp_f32 v66, v66\n\t" \
    "v_exp_f32 v67, v67\n\t" \
    "v_exp_f32 v68, v68\n\t" \
    "v_cvt_pk_bf16_f32 v96, v64, v65\n\t" \
    "v_cvt_pk_bf16_f32 v97, v66, v67\n\t" \
    "v_mfma_f32_32x32x16_bf16 v[16:31], v[124:127], v[100:103], v[16:31]\n\t" \
    "s_mov_b64 exec, %[vm]\n\t" \
    "ds_read_b128 v[120:123], %[va] offset:12288\n\t" \
    "ds_read_b128 v[124:127], %[va] offset:12800\n\t" \
    "s_mov_b64 exec, -1\n\t" \
    "v_exp_f32 v69, v69\n\t" \
    "v_exp_f32 v70, v70\n\t" \
    "v_exp_f32 v71, v71\n\t" \
    "v_exp_f32 v72, v72\n\t" \
    "v_cvt_pk_bf16_f32 v98, v68, v69\n\t" \
    "v_cvt_pk_bf16_f32 v99, v70, v71\n\t" \
    "v_exp_f32 v73, v73\n\t" \
    "v_exp_f32 v74, v74\n\t" \
    "v_mfma_f32_32x32x16_bf16 v[0:15], v[128:131], v[96:99], v[0:15]\n\t" \
    "v_exp_f32 v75, v75\n\t" \
    "v_exp_f32 v76, v76\n\t" \
    "v_exp_f32 v77, v77\n\t" \
    "v_exp_f32 v78, v78\n\t" \
    "v_exp_f32 v79, v79\n\t" \
    "v_cvt_pk_bf16_f32 v100, v72, v73\n\t" \
    "v_cvt_pk_bf16_f32 v101, v74, v75\n\t" \
    "v_cvt_pk_bf16_f32 v102, v76, v77\n\t" \
    "v_cvt_pk_bf16_f32 v103, v78, v79\n\t" \
    "s_waitcnt lgkmcnt(0)\n\t" \
    "v_mfma_f32_32x32x16_bf16 v[64:79], v[112:115], v[104:107], v[32:47]\n\t" \
    "v_exp_f32 v80, v80\n\t" \
    "v_exp_f32 v81, v81\n\t" \
    "v_exp_f32 v82, v82\n\t" \
    "v_exp_f32 v83, v83\n\t" \
    "v_exp_f32 v84, v84\n\t" \
    "v_cvt_pk_bf16_f32 v96, v80, v81\n\t" \
    "v_cvt_pk_bf16_f32 v97, v82, v83\n\t" \
    "v_mfma_f32_32x32x16_bf16 v[0:15], v[132:135], v[100:103], v[0:15]\n\t" \
    "v_exp_f32 v85, v85\n\t" \
    "v_exp_f32 v86, v86\n\t" \
    "v_exp_f32 v87, v87\n\t" \
    "v_exp_f32 v88, v88\n\t" \
    "v_cvt_pk_bf16_f32 v98, v84, v85\n\t" \
    "v_cvt_pk_bf16_f32 v99, v86, v87\n\t" \
    "v_exp_f32 v89, v89\n\t" \
    "v_exp_f32 v90, v90\n\t" \
    "v_mfma_f32_32x32x16_bf16 v[16:31], v[128:131], v[96:99], v[16:31]\n\t" \
    "v_exp_f32 v91, v91\n\t" \
    "v_exp_f32 v92, v92\n\t" \
    "v_exp_f32 v93, v93\n\t" \
    "v_exp_f32 v94, v94\n\t" \
    "v_exp_f32 v95, v95\n\t" \
    "v_cvt_pk_bf16_f32 v100, v88, v89\n\t" \
    "v_cvt_pk_bf16_f32 v101, v90, v91\n\t" \
    "v_cvt_pk_bf16_f32 v102, v92, v93\n\t" \
    "v_cvt_pk_bf16_f32 v103, v94, v95\n\t" \
    "v_mfma_f32_32x32x16_bf16 v[80:95], v[112:115], v[108:111], v[48:63]\n\t" \
    "ds_read_b128 v[116:119], %[ka] offset:13312\n\t" \
    "v_exp_f32 v64, v64\n\t" \
    "v_exp_f32 v65, v65\n\t" \
    "v_exp_f32 v66, v66\n\t" \
    "v_exp_f32 v67, v67\n\t" \
    "v_exp_f32 v68, v68\n\t" \
    "v_cvt_pk_bf16_f32 v96, v64, v65\n\t" \
    "v_cvt_pk_bf16_f32 v97, v66, v67\n\t" \
    "v_mfma_f32_32x32x16_bf16 v[16:31], v[132:135], v[100:103], v[16:31]\n\t" \
    "s_mov_b64 exec, %[vm]\n\t" \
    "ds_read_b128 v[128:131], %[va] offset:13312\n\t" \
    "ds_read_b128 v[132:135], %[va] offset:13824\n\t" \
    "s_mov_b64 exec, -1\n\t" \
    "v_exp_f32 v69, v69\n\t" \
    "v_exp_f32 v70, v70\n\t" \
    "v_exp_f32 v71, v71\n\t" \
    "v_exp_f32 v72, v72\n\t" \
    "v_cvt_pk_bf16_f32 v98, v68, v69\n\t" \
    "v_cvt_pk_bf16_f32 v99, v70, v71\n\t" \
    "v_exp_f32 v73, v73\n\t" \
    "v_exp_f32 v74, v74\n\t" \
    "v_mfma_f32_32x32x16_bf16 v[0:15], v[120:123], v[96:99], v[0:15]\n\t" \
    "v_exp_f32 v75, v75\n\t" \
    "v_exp_f32 v76, v76\n\t" \
    "v_exp_f32 v77, v77\n\t" \
    "v_exp_f32 v78, v78\n\t" \
    "v_exp_f32 v79, v79\n\t" \
    "v_cvt_pk_bf16_f32 v100, v72, v73\n\t" \
    "v_cvt_pk_bf16_f32 v101, v74, v75\n\t" \
    "v_cvt_pk_bf16_f32 v102, v76, v77\n\t" \
    "v_cvt_pk_bf16_f32 v103, v78, v79\n\t" \
    "s_waitcnt lgkmcnt(0)\n\t" \
    "v_mfma_f32_32x32x16_bf16 v[64:79], v[116:119], v[104:107], v[32:47]\n\t" \
    "v_exp_f32 v80, v80\n\t" \
    "v_exp_f32 v81, v81\n\t" \
    "v_exp_f32 v82, v82\n\t" \
    "v_exp_f32 v83, v83\n\t" \
    "v_exp_f32 v84, v84\n\t" \
    "v_cvt_pk_bf16_f32 v96, v80, v81\n\t" \
    "v_cvt_pk_bf16_f32 v97, v82, v83\n\t" \
    "v_mfma_f32_32x32x16_bf16 v[0:15], v[124:127], v[100:103], v[0:15]\n\t" \
    "v_exp_f32 v85, v85\n\t" \
    "v_exp_f32 v86, v86\n\t" \
    "v_exp_f32 v87, v87\n\t" \
    "v_exp_f32 v88, v88\n\t" \
    "v_cvt_pk_bf16_f32 v98, v84, v85\n\t" \
    "v_cvt_pk_bf16_f32 v99, v86, v87\n\t" \
    "v_exp_f32 v89, v89\n\t" \
    "v_exp_f32 v90, v90\n\t" \
    "v_mfma_f32_32x32x16_bf16 v[16:31], v[120:123], v[96:99], v[16:31]\n\t" \
    "v_exp_f32 v91, v91\n\t" \
    "v_exp_f32 v92, v92\n\t" \
    "v_exp_f32 v93, v93\n\t" \
    "v_exp_f32 v94, v94\n\t" \
    "v_exp_f32 v95, v95\n\t" \
    "v_cvt_pk_bf16_f32 v100, v88, v89\n\t" \
    "v_cvt_pk_bf16_f32 v101, v90, v91\n\t" \
    "v_cvt_pk_bf16_f32 v102, v92, v93\n\t" \
    "v_cvt_pk_bf16_f32 v103, v94, v95\n\t" \
    "v_mfma_f32_32x32x16_bf16 v[80:95], v[116:119], v[108:111], v[48:63]\n\t" \
    "ds_read_b128 v[112:115], %[ka] offset:14336\n\t" \
    "v_exp_f32 v64, v64\n\t" \
    "v_exp_f32 v65, v65\n\t" \
    "v_exp_f32 v66, v66\n\t" \
    "v_exp_f32 v67, v67\n\t" \
    "v_exp_f32 v68, v68\n\t" \
    "v_cvt_pk_bf16_f32 v96, v64, v65\n\t" \
    "v_cvt_pk_bf16_f32 v97, v66, v67\n\t" \
    "v_mfma_f32_32x32x16_bf16 v[16:31], v[124:127], v[100:103], v[16:31]\n\t" \
    "s_mov_b64 exec, %[vm]\n\t" \
    "ds_read_b128 v[120:123], %[va] offset:14336\n\t" \
    "ds_read_b128 v[124:127], %[va] offset:14848\n\t" \
    "s_mov_b64 exec, -1\n\t" \
    "v_exp_f32 v69, v69\n\t" \
    "v_exp_f32 v70, v70\n\t" \
    "v_exp_f32 v71, v71\n\t" \
    "v_exp_f32 v72, v72\n\t" \
    "v_cvt_pk_bf16_f32 v98, v68, v69\n\t" \
    "v_cvt_pk_bf16_f32 v99, v70, v71\n\t" \
    "v_exp_f32 v73, v73\n\t" \
    "v_exp_f32 v74, v74\n\t" \
    "v_mfma_f32_32x32x16_bf16 v[0:15], v[128:131], v[96:99], v[0:15]\n\t" \
    "v_exp_f32 v75, v75\n\t" \
    "v_exp_f32 v76, v76\n\t" \
    "v_exp_f32 v77, v77\n\t" \
    "v_exp_f32 v78, v78\n\t" \
    "v_exp_f32 v79, v79\n\t" \
    "v_cvt_pk_bf16_f32 v100, v72, v73\n\t" \
    "v_cvt_pk_bf16_f32 v101, v74, v75\n\t" \
    "v_cvt_pk_bf16_f32 v102, v76, v77\n\t" \
    "v_cvt_pk_bf16_f32 v103, v78, v79\n\t" \
    "s_waitcnt lgkmcnt(0)\n\t" \
    "v_mfma_f32_32x32x16_bf16 v[64:79], v[112:115], v[104:107], v[32:47]\n\t" \
    "v_exp_f32 v80, v80\n\t" \
    "v_exp_f32 v81, v81\n\t" \
    "v_exp_f32 v82, v82\n\t" \
    "v_exp_f32 v83, v83\n\t" \
    "v_exp_f32 v84, v84\n\t" \
    "v_cvt_pk_bf16_f32 v96, v80, v81\n\t" \
    "v_cvt_pk_bf16_f32 v97, v82, v83\n\t" \
    "v_mfma_f32_32x32x16_bf16 v[0:15], v[132:135], v[100:103], v[0:15]\n\t" \
    "v_exp_f32 v85, v85\n\t" \
    "v_exp_f32 v86, v86\n\t" \
    "v_exp_f32 v87, v87\n\t" \
    "v_exp_f32 v88, v88\n\t" \
    "v_cvt_pk_bf16_f32 v98, v84, v85\n\t" \
    "v_cvt_pk_bf16_f32 v99, v86, v87\n\t" \
    "v_exp_f32 v89, v89\n\t" \
    "v_exp_f32 v90, v90\n\t" \
    "v_mfma_f32_32x32x16_bf16 v[16:31], v[128:131], v[96:99], v[16:31]\n\t" \
    "v_exp_f32 v91, v91\n\t" \
    "v_exp_f32 v92, v92\n\t" \
    "v_exp_f32 v93, v93\n\t" \
    "v_exp_f32 v94, v94\n\t" \
    "v_exp_f32 v95, v95\n\t" \
    "v_cvt_pk_bf16_f32 v100, v88, v89\n\t" \
    "v_cvt_pk_bf16_f32 v101, v90, v91\n\t" \
    "v_cvt_pk_bf16_f32 v102, v92, v93\n\t" \
    "v_cvt_pk_bf16_f32 v103, v94, v95\n\t" \
    "v_mfma_f32_32x32x16_bf16 v[80:95], v[112:115], v[108:111], v[48:63]\n\t" \
    "ds_read_b128 v[116:119], %[ka] offset:15360\n\t" \
    "v_exp_f32 v64, v64\n\t" \
    "v_exp_f32 v65, v65\n\t" \
    "v_exp_f32 v66, v66\n\t" \
    "v_exp_f32 v67, v67\n\t" \
    "v_exp_f32 v68, v68\n\t" \
    "v_cvt_pk_bf16_f32 v96, v64, v65\n\t" \
    "v_cvt_pk_bf16_f32 v97, v66, v67\n\t" \
    "v_mfma_f32_32x32x16_bf16 v[16:31], v[132:135], v[100:103], v[16:31]\n\t" \
    "s_mov_b64 exec, %[vm]\n\t" \
    "ds_read_b128 v[128:131], %[va] offset:15360\n\t" \
    "ds_read_b128 v[132:135], %[va] offset:15872\n\t" \
    "s_mov_b64 exec, -1\n\t" \
    "v_exp_f32 v69, v69\n\t" \
    "v_exp_f32 v70, v70\n\t" \
    "v_exp_f32 v71, v71\n\t" \
    "v_exp_f32 v72, v72\n\t" \
    "v_cvt_pk_bf16_f32 v98, v68, v69\n\t" \
    "v_cvt_pk_bf16_f32 v99, v70, v71\n\t" \
    "v_exp_f32 v73, v73\n\t" \
    "v_exp_f32 v74, v74\n\t" \
    "v_mfma_f32_32x32x16_bf16 v[0:15], v[120:123], v[96:99], v[0:15]\n\t" \
    "v_exp_f32 v75, v75\n\t" \
    "v_exp_f32 v76, v76\n\t" \
    "v_exp_f32 v77, v77\n\t" \
    "v_exp_f32 v78, v78\n\t" \
    "v_exp_f32 v79, v79\n\t" \
    "v_cvt_pk_bf16_f32 v100, v72, v73\n\t" \
    "v_cvt_pk_bf16_f32 v101, v74, v75\n\t" \
    "v_cvt_pk_bf16_f32 v102, v76, v77\n\t" \
    "v_cvt_pk_bf16_f32 v103, v78, v79\n\t" \
    "s_waitcnt lgkmcnt(0)\n\t" \
    "v_mfma_f32_32x32x16_bf16 v[64:79], v[116:119], v[104:107], v[32:47]\n\t" \
    "v_exp_f32 v80, v80\n\t" \
    "v_exp_f32 v81, v81\n\t" \
    "v_exp_f32 v82, v82\n\t" \
    "v_exp_f32 v83, v83\n\t" \
    "v_exp_f32 v84, v84\n\t" \
    "v_cvt_pk_bf16_f32 v96, v80, v81\n\t" \
    "v_cvt_pk_bf16_f32 v97, v82, v83\n\t" \
    "v_mfma_f32_32x32x16_bf16 v[0:15], v[124:127], v[100:103], v[0:15]\n\t" \
    "v_exp_f32 v85, v85\n\t" \
    "v_exp_f32 v86, v86\n\t" \
    "v_exp_f32 v87, v87\n\t" \
    "v_exp_f32 v88, v88\n\t" \
    "v_cvt_pk_bf16_f32 v98, v84, v85\n\t" \
    "v_cvt_pk_bf16_f32 v99, v86, v87\n\t" \
    "v_exp_f32 v89, v89\n\t" \
    "v_exp_f32 v90, v90\n\t" \
    "v_mfma_f32_32x32x16_bf16 v[16:31], v[120:123], v[96:99], v[16:31]\n\t" \
    "v_exp_f32 v91, v91\n\t" \
    "v_exp_f32 v92, v92\n\t" \
    "v_exp_f32 v93, v93\n\t" \
    "v_exp_f32 v94, v94\n\t" \
    "v_exp_f32 v95, v95\n\t" \
    "v_cvt_pk_bf16_f32 v100, v88, v89\n\t" \
    "v_cvt_pk_bf16_f32 v101, v90, v91\n\t" \
    "v_cvt_pk_bf16_f32 v102, v92, v93\n\t" \
    "v_cvt_pk_bf16_f32 v103, v94, v95\n\t" \
    "v_mfma_f32_32x32x16_bf16 v[80:95], v[116:119], v[108:111], v[48:63]\n\t" \
    "ds_read_b128 v[112:115], %[ka] offset:16384\n\t" \
    "v_exp_f32 v64, v64\n\t" \
    "v_exp_f32 v65, v65\n\t" \
    "v_exp_f32 v66, v66\n\t" \
    "v_exp_f32 v67, v67\n\t" \
    "v_exp_f32 v68, v68\n\t" \
    "v_cvt_pk_bf16_f32 v96, v64, v65\n\t" \
    "v_cvt_pk_bf16_f32 v97, v66, v67\n\t" \
    "v_mfma_f32_32x32x16_bf16 v[16:31], v[124:127], v[100:103], v[16:31]\n\t" \
    "s_mov_b64 exec, %[vm]\n\t" \
    "ds_read_b128 v[120:123], %[va] offset:16384\n\t" \
    "ds_read_b128 v[124:127], %[va] offset:16896\n\t" \
    "s_mov_b64 exec, -1\n\t" \
    "v_exp_f32 v69, v69\n\t" \
    "v_exp_f32 v70, v70\n\t" \
    "v_exp_f32 v71, v71\n\t" \
    "v_exp_f32 v72, v72\n\t" \
    "v_cvt_pk_bf16_f32 v98, v68, v69\n\t" \
    "v_cvt_pk_bf16_f32 v99, v70, v71\n\t" \
    "v_exp_f32 v73, v73\n\t" \
    "v_exp_f32 v74, v74\n\t" \
    "v_mfma_f32_32x32x16_bf16 v[0:15], v[128:131], v[96:99], v[0:15]\n\t" \
    "v_exp_f32 v75, v75\n\t" \
    "v_exp_f32 v76, v76\n\t" \
    "v_exp_f32 v77, v77\n\t" \
    "v_exp_f32 v78, v78\n\t" \
    "v_exp_f32 v79, v79\n\t" \
    "v_cvt_pk_bf16_f32 v100, v72, v73\n\t" \
    "v_cvt_pk_bf16_f32 v101, v74, v75\n\t" \
    "v_cvt_pk_bf16_f32 v102, v76, v77\n\t" \
    "v_cvt_pk_bf16_f32 v103, v78, v79\n\t" \
    "s_waitcnt lgkmcnt(0)\n\t" \
    "v_mfma_f32_32x32x16_bf16 v[64:79], v[112:115], v[104:107], v[32:47]\n\t" \
    "v_exp_f32 v80, v80\n\t" \
    "v_exp_f32 v81, v81\n\t" \
    "v_exp_f32 v82, v82\n\t" \
    "v_exp_f32 v83, v83\n\t" \
    "v_exp_f32 v84, v84\n\t" \
    "v_cvt_pk_bf16_f32 v96, v80, v81\n\t" \
    "v_cvt_pk_bf16_f32 v97, v82, v83\n\t" \
    "v_mfma_f32_32x32x16_bf16 v[0:15], v[132:135], v[100:103], v[0:15]\n\t" \
    "v_exp_f32 v85, v85\n\t" \
    "v_exp_f32 v86, v86\n\t" \
    "v_exp_f32 v87, v87\n\t" \
    "v_exp_f32 v88, v88\n\t" \
    "v_cvt_pk_bf16_f32 v98, v84, v85\n\t" \
    "v_cvt_pk_bf16_f32 v99, v86, v87\n\t" \
    "v_exp_f32 v89, v89\n\t" \
    "v_exp_f32 v90, v90\n\t" \
    "v_mfma_f32_32x32x16_bf16 v[16:31], v[128:131], v[96:99], v[16:31]\n\t" \
    "v_exp_f32 v91, v91\n\t" \
    "v_exp_f32 v92, v92\n\t" \
    "v_exp_f32 v93, v93\n\t" \
    "v_exp_f32 v94, v94\n\t" \
    "v_exp_f32 v95, v95\n\t" \
    "v_cvt_pk_bf16_f32 v100, v88, v89\n\t" \
    "v_cvt_pk_bf16_f32 v101, v90, v91\n\t" \
    "v_cvt_pk_bf16_f32 v102, v92, v93\n\t" \
    "v_cvt_pk_bf16_f32 v103, v94, v95\n\t" \
    "v_mfma_f32_32x32x16_bf16 v[80:95], v[112:115], v[108:111], v[48:63]\n\t" \
    "ds_read_b128 v[116:119], %[ka] offset:17408\n\t" \
    "v_exp_f32 v64, v64\n\t" \
    "v_exp_f32 v65, v65\n\t" \
    "v_exp_f32 v66, v66\n\t" \
    "v_exp_f32 v67, v67\n\t" \
    "v_exp_f32 v68, v68\n\t" \
    "v_cvt_pk_bf16_f32 v96, v64, v65\n\t" \
    "v_cvt_pk_bf16_f32 v97, v66, v67\n\t" \
    "v_mfma_f32_32x32x16_bf16 v[16:31], v[132:135], v[100:103], v[16:31]\n\t" \
    "s_mov_b64 exec, %[vm]\n\t" \
    "ds_read_b128 v[128:131], %[va] offset:17408\n\t" \
    "ds_read_b128 v[132:135], %[va] offset:17920\n\t" \
    "s_mov_b64 exec, -1\n\t" \
    "v_exp_f32 v69, v69\n\t" \
    "v_exp_f32 v70, v70\n\t" \
    "v_exp_f32 v71, v71\n\t" \
    "v_exp_f32 v72, v72\n\t" \
    "v_cvt_pk_bf16_f32 v98, v68, v69\n\t" \
    "v_cvt_pk_bf16_f32 v99, v70, v71\n\t" \
    "v_exp_f32 v73, v73\n\t" \
    "v_exp_f32 v74, v74\n\t" \
    "v_mfma_f32_32x32x16_bf16 v[0:15], v[120:123], v[96:99], v[0:15]\n\t" \
    "v_exp_f32 v75, v75\n\t" \
    "v_exp_f32 v76, v76\n\t" \
    "v_exp_f32 v77, v77\n\t" \
    "v_exp_f32 v78, v78\n\t" \
    "v_exp_f32 v79, v79\n\t" \
    "v_cvt_pk_bf16_f32 v100, v72, v73\n\t" \
    "v_cvt_pk_bf16_f32 v101, v74, v75\n\t" \
    "v_cvt_pk_bf16_f32 v102, v76, v77\n\t" \
    "v_cvt_pk_bf16_f32 v103, v78, v79\n\t" \
    "s_waitcnt lgkmcnt(0)\n\t" \
    "v_mfma_f32_32x32x16_bf16 v[64:79], v[116:119], v[104:107], v[32:47]\n\t" \
    "v_exp_f32 v80, v80\n\t" \
    "v_exp_f32 v81, v81\n\t" \
    "v_exp_f32 v82, v82\n\t" \
    "v_exp_f32 v83, v83\n\t" \
    "v_exp_f32 v84, v84\n\t" \
    "v_cvt_pk_bf16_f32 v96, v80, v81\n\t" \
    "v_cvt_pk_bf16_f32 v97, v82, v83\n\t" \
    "v_mfma_f32_32x32x16_bf16 v[0:15], v[124:127], v[100:103], v[0:15]\n\t" \
    "v_exp_f32 v85, v85\n\t" \
    "v_exp_f32 v86, v86\n\t" \
    "v_exp_f32 v87, v87\n\t" \
    "v_exp_f32 v88, v88\n\t" \
    "v_cvt_pk_bf16_f32 v98, v84, v85\n\t" \
    "v_cvt_pk_bf16_f32 v99, v86, v87\n\t" \
    "v_exp_f32 v89, v89\n\t" \
    "v_exp_f32 v90, v90\n\t" \
    "v_mfma_f32_32x32x16_bf16 v[16:31], v[120:123], v[96:99], v[16:31]\n\t" \
    "v_exp_f32 v91, v91\n\t" \
    "v_exp_f32 v92, v92\n\t" \
    "v_exp_f32 v93, v93\n\t" \
    "v_exp_f32 v94, v94\n\t" \
    "v_exp_f32 v95, v95\n\t" \
    "v_cvt_pk_bf16_f32 v100, v88, v89\n\t" \
    "v_cvt_pk_bf16_f32 v101, v90, v91\n\t" \
    "v_cvt_pk_bf16_f32 v102, v92, v93\n\t" \
    "v_cvt_pk_bf16_f32 v103, v94, v95\n\t" \
    "v_mfma_f32_32x32x16_bf16 v[80:95], v[116:119], v[108:111], v[48:63]\n\t" \
    "ds_read_b128 v[112:115], %[ka] offset:18432\n\t" \
    "v_exp_f32 v64, v64\n\t" \
    "v_exp_f32 v65, v65\n\t" \
    "v_exp_f32 v66, v66\n\t" \
    "v_exp_f32 v67, v67\n\t" \
    "v_exp_f32 v68, v68\n\t" \
    "v_cvt_pk_bf16_f32 v96, v64, v65\n\t" \
    "v_cvt_pk_bf16_f32 v97, v66, v67\n\t" \
    "v_mfma_f32_32x32x16_bf16 v[16:31], v[124:127], v[100:103], v[16:31]\n\t" \
    "s_mov_b64 exec, %[vm]\n\t" \
    "ds_read_b128 v[120:123], %[va] offset:18432\n\t" \
    "ds_read_b128 v[124:127], %[va] offset:18944\n\t" \
    "s_mov_b64 exec, -1\n\t" \
    "v_exp_f32 v69, v69\n\t" \
    "v_exp_f32 v70, v70\n\t" \
    "v_exp_f32 v71, v71\n\t" \
    "v_exp_f32 v72, v72\n\t" \
    "v_cvt_pk_bf16_f32 v98, v68, v69\n\t" \
    "v_cvt_pk_bf16_f32 v99, v70, v71\n\t" \
    "v_exp_f32 v73, v73\n\t" \
    "v_exp_f32 v74, v74\n\t" \
    "v_mfma_f32_32x32x16_bf16 v[0:15], v[128:131], v[96:99], v[0:15]\n\t" \
    "v_exp_f32 v75, v75\n\t" \
    "v_exp_f32 v76, v76\n\t" \
    "v_exp_f32 v77, v77\n\t" \
    "v_exp_f32 v78, v78\n\t" \
    "v_exp_f32 v79, v79\n\t" \
    "v_cvt_pk_bf16_f32 v100, v72, v73\n\t" \
    "v_cvt_pk_bf16_f32 v101, v74, v75\n\t" \
    "v_cvt_pk_bf16_f32 v102, v76, v77\n\t" \
    "v_cvt_pk_bf16_f32 v103, v78, v79\n\t" \
    "s_waitcnt lgkmcnt(0)\n\t" \
    "v_mfma_f32_32x32x16_bf16 v[64:79], v[112:115], v[104:107], v[32:47]\n\t" \
    "v_exp_f32 v80, v80\n\t" \
    "v_exp_f32 v81, v81\n\t" \
    "v_exp_f32 v82, v82\n\t" \
    "v_exp_f32 v83, v83\n\t" \
    "v_exp_f32 v84, v84\n\t" \
    "v_cvt_pk_bf16_f32 v96, v80, v81\n\t" \
    "v_cvt_pk_bf16_f32 v97, v82, v83\n\t" \
    "v_mfma_f32_32x32x16_bf16 v[0:15], v[132:135], v[100:103], v[0:15]\n\t" \
    "v_exp_f32 v85, v85\n\t" \
    "v_exp_f32 v86, v86\n\t" \
    "v_exp_f32 v87, v87\n\t" \
    "v_exp_f32 v88, v88\n\t" \
    "v_cvt_pk_bf16_f32 v98, v84, v85\n\t" \
    "v_cvt_pk_bf16_f32 v99, v86, v87\n\t" \
    "v_exp_f32 v89, v89\n\t" \
    "v_exp_f32 v90, v90\n\t" \
    "v_mfma_f32_32x32x16_bf16 v[16:31], v[128:131], v[96:99], v[16:31]\n\t" \
    "v_exp_f32 v91, v91\n\t" \
    "v_exp_f32 v92, v92\n\t" \
    "v_exp_f32 v93, v93\n\t" \
    "v_exp_f32 v94, v94\n\t" \
    "v_exp_f32 v95, v95\n\t" \
    "v_cvt_pk_bf16_f32 v100, v88, v89\n\t" \
    "v_cvt_pk_bf16_f32 v101, v90, v91\n\t" \
    "v_cvt_pk_bf16_f32 v102, v92, v93\n\t" \
    "v_cvt_pk_bf16_f32 v103, v94, v95\n\t" \
    "v_mfma_f32_32x32x16_bf16 v[80:95], v[112:115], v[108:111], v[48:63]\n\t" \
    "ds_read_b128 v[116:119], %[ka] offset:19456\n\t" \
    "v_exp_f32 v64, v64\n\t" \
    "v_exp_f32 v65, v65\n\t" \
    "v_exp_f32 v66, v66\n\t" \
    "v_exp_f32 v67, v67\n\t" \
    "v_exp_f32 v68, v68\n\t" \
    "v_cvt_pk_bf16_f32 v96, v64, v65\n\t" \
    "v_cvt_pk_bf16_f32 v97, v66, v67\n\t" \
    "v_mfma_f32_32x32x16_bf16 v[16:31], v[132:135], v[100:103], v[16:31]\n\t" \
    "s_mov_b64 exec, %[vm]\n\t" \
    "ds_read_b128 v[128:131], %[va] offset:19456\n\t" \
    "ds_read_b128 v[132:135], %[va] offset:19968\n\t" \
    "s_mov_b64 exec, -1\n\t" \
    "v_exp_f32 v69, v69\n\t" \
    "v_exp_f32 v70, v70\n\t" \
    "v_exp_f32 v71, v71\n\t" \
    "v_exp_f32 v72, v72\n\t" \
    "v_cvt_pk_bf16_f32 v98, v68, v69\n\t" \
    "v_cvt_pk_bf16_f32 v99, v70, v71\n\t" \
    "v_exp_f32 v73, v73\n\t" \
    "v_exp_f32 v74, v74\n\t" \
    "v_mfma_f32_32x32x16_bf16 v[0:15], v[120:123], v[96:99], v[0:15]\n\t" \
    "v_exp_f32 v75, v75\n\t" \
    "v_exp_f32 v76, v76\n\t" \
    "v_exp_f32 v77, v77\n\t" \
    "v_exp_f32 v78, v78\n\t" \
    "v_exp_f32 v79, v79\n\t" \
    "v_cvt_pk_bf16_f32 v100, v72, v73\n\t" \
    "v_cvt_pk_bf16_f32 v101, v74, v75\n\t" \
    "v_cvt_pk_bf16_f32 v102, v76, v77\n\t" \
    "v_cvt_pk_bf16_f32 v103, v78, v79\n\t" \
    "s_waitcnt lgkmcnt(0)\n\t" \
    "v_mfma_f32_32x32x16_bf16 v[64:79], v[116:119], v[104:107], v[32:47]\n\t" \
    "v_exp_f32 v80, v80\n\t" \
    "v_exp_f32 v81, v81\n\t" \
    "v_exp_f32 v82, v82\n\t" \
    "v_exp_f32 v83, v83\n\t" \
    "v_exp_f32 v84, v84\n\t" \
    "v_cvt_pk_bf16_f32 v96, v80, v81\n\t" \
    "v_cvt_pk_bf16_f32 v97, v82, v83\n\t" \
    "v_mfma_f32_32x32x16_bf16 v[0:15], v[124:127], v[100:103], v[0:15]\n\t" \
    "v_exp_f32 v85, v85\n\t" \
    "v_exp_f32 v86, v86\n\t" \
    "v_exp_f32 v87, v87\n\t" \
    "v_exp_f32 v88, v88\n\t" \
    "v_cvt_pk_bf16_f32 v98, v84, v85\n\t" \
    "v_cvt_pk_bf16_f32 v99, v86, v87\n\t" \
    "v_exp_f32 v89, v89\n\t" \
    "v_exp_f32 v90, v90\n\t" \
    "v_mfma_f32_32x32x16_bf16 v[16:31], v[120:123], v[96:99], v[16:31]\n\t" \
    "v_exp_f32 v91, v91\n\t" \
    "v_exp_f32 v92, v92\n\t" \
    "v_exp_f32 v93, v93\n\t" \
    "v_exp_f32 v94, v94\n\t" \
    "v_exp_f32 v95, v95\n\t" \
    "v_cvt_pk_bf16_f32 v100, v88, v89\n\t" \
    "v_cvt_pk_bf16_f32 v101, v90, v91\n\t" \
    "v_cvt_pk_bf16_f32 v102, v92, v93\n\t" \
    "v_cvt_pk_bf16_f32 v103, v94, v95\n\t" \
    "v_mfma_f32_32x32x16_bf16 v[80:95], v[116:119], v[108:111], v[48:63]\n\t" \
    "ds_read_b128 v[112:115], %[ka] offset:20480\n\t" \
    "v_exp_f32 v64, v64\n\t" \
    "v_exp_f32 v65, v65\n\t" \
    "v_exp_f32 v66, v66\n\t" \
    "v_exp_f32 v67, v67\n\t" \
    "v_exp_f32 v68, v68\n\t" \
    "v_cvt_pk_bf16_f32 v96, v64, v65\n\t" \
    "v_cvt_pk_bf16_f32 v97, v66, v67\n\t" \
    "v_mfma_f32_32x32x16_bf16 v[16:31], v[124:127], v[100:103], v[16:31]\n\t" \
    "s_mov_b64 exec, %[vm]\n\t" \
    "ds_read_b128 v[120:123], %[va] offset:20480\n\t" \
    "ds_read_b128 v[124:127], %[va] offset:20992\n\t" \
    "s_mov_b64 exec, -1\n\t" \
    "v_exp_f32 v69, v69\n\t" \
    "v_exp_f32 v70, v70\n\t" \
    "v_exp_f32 v71, v71\n\t" \
    "v_exp_f32 v72, v72\n\t" \
    "v_cvt_pk_bf16_f32 v98, v68, v69\n\t" \
    "v_cvt_pk_bf16_f32 v99, v70, v71\n\t" \
    "v_exp_f32 v73, v73\n\t" \
    "v_exp_f32 v74, v74\n\t" \
    "v_mfma_f32_32x32x16_bf16 v[0:15], v[128:131], v[96:99], v[0:15]\n\t" \
    "v_exp_f32 v75, v75\n\t" \
    "v_exp_f32 v76, v76\n\t" \
    "v_exp_f32 v77, v77\n\t" \
    "v_exp_f32 v78, v78\n\t" \
    "v_exp_f32 v79, v79\n\t" \
    "v_cvt_pk_bf16_f32 v100, v72, v73\n\t" \
    "v_cvt_pk_bf16_f32 v101, v74, v75\n\t" \
    "v_cvt_pk_bf16_f32 v102, v76, v77\n\t" \
    "v_cvt_pk_bf16_f32 v103, v78, v79\n\t" \
    "s_waitcnt lgkmcnt(0)\n\t" \
    "v_mfma_f32_32x32x16_bf16 v[64:79], v[112:115], v[104:107], v[32:47]\n\t" \
    "v_exp_f32 v80, v80\n\t" \
    "v_exp_f32 v81, v81\n\t" \
    "v_exp_f32 v82, v82\n\t" \
    "v_exp_f32 v83, v83\n\t" \
    "v_exp_f32 v84, v84\n\t" \
    "v_cvt_pk_bf16_f32 v96, v80, v81\n\t" \
    "v_cvt_pk_bf16_f32 v97, v82, v83\n\t" \
    "v_mfma_f32_32x32x16_bf16 v[0:15], v[132:135], v[100:103], v[0:15]\n\t" \
    "v_exp_f32 v85, v85\n\t" \
    "v_exp_f32 v86, v86\n\t" \
    "v_exp_f32 v87, v87\n\t" \
    "v_exp_f32 v88, v88\n\t" \
    "v_cvt_pk_bf16_f32 v98, v84, v85\n\t" \
    "v_cvt_pk_bf16_f32 v99, v86, v87\n\t" \
    "v_exp_f32 v89, v89\n\t" \
    "v_exp_f32 v90, v90\n\t" \
    "v_mfma_f32_32x32x16_bf16 v[16:31], v[128:131], v[96:99], v[16:31]\n\t" \
    "v_exp_f32 v91, v91\n\t" \
    "v_exp_f32 v92, v92\n\t" \
    "v_exp_f32 v93, v93\n\t" \
    "v_exp_f32 v94, v94\n\t" \
    "v_exp_f32 v95, v95\n\t" \
    "v_cvt_pk_bf16_f32 v100, v88, v89\n\t" \
    "v_cvt_pk_bf16_f32 v101, v90, v91\n\t" \
    "v_cvt_pk_bf16_f32 v102, v92, v93\n\t" \
    "v_cvt_pk_bf16_f32 v103, v94, v95\n\t" \
    "v_mfma_f32_32x32x16_bf16 v[80:95], v[112:115], v[108:111], v[48:63]\n\t" \
    "ds_read_b128 v[116:119], %[ka] offset:21504\n\t" \
    "v_exp_f32 v64, v64\n\t" \
    "v_exp_f32 v65, v65\n\t" \
    "v_exp_f32 v66, v66\n\t" \
    "v_exp_f32 v67, v67\n\t" \
    "v_exp_f32 v68, v68\n\t" \
    "v_cvt_pk_bf16_f32 v96, v64, v65\n\t" \
    "v_cvt_pk_bf16_f32 v97, v66, v67\n\t" \
    "v_mfma_f32_32x32x16_bf16 v[16:31], v[132:135], v[100:103], v[16:31]\n\t" \
    "s_mov_b64 exec, %[vm]\n\t" \
    "ds_read_b128 v[128:131], %[va] offset:21504\n\t" \
    "ds_read_b128 v[132:135], %[va] offset:22016\n\t" \
    "s_mov_b64 exec, -1\n\t" \
    "v_exp_f32 v69, v69\n\t" \
    "v_exp_f32 v70, v70\n\t" \
    "v_exp_f32 v71, v71\n\t" \
    "v_exp_f32 v72, v72\n\t" \
    "v_cvt_pk_bf16_f32 v98, v68, v69\n\t" \
    "v_cvt_pk_bf16_f32 v99, v70, v71\n\t" \
    "v_exp_f32 v73, v73\n\t" \
    "v_exp_f32 v74, v74\n\t" \
    "v_mfma_f32_32x32x16_bf16 v[0:15], v[120:123], v[96:99], v[0:15]\n\t" \
    "v_exp_f32 v75, v75\n\t" \
    "v_exp_f32 v76, v76\n\t" \
    "v_exp_f32 v77, v77\n\t" \
    "v_exp_f32 v78, v78\n\t" \
    "v_exp_f32 v79, v79\n\t" \
    "v_cvt_pk_bf16_f32 v100, v72, v73\n\t" \
    "v_cvt_pk_bf16_f32 v101, v74, v75\n\t" \
    "v_cvt_pk_bf16_f32 v102, v76, v77\n\t" \
    "v_cvt_pk_bf16_f32 v103, v78, v79\n\t" \
    "s_waitcnt lgkmcnt(0)\n\t" \
    "v_mfma_f32_32x32x16_bf16 v[64:79], v[116:119], v[104:107], v[32:47]\n\t" \
    "v_exp_f32 v80, v80\n\t" \
    "v_exp_f32 v81, v81\n\t" \
    "v_exp_f32 v82, v82\n\t" \
    "v_exp_f32 v83, v83\n\t" \
    "v_exp_f32 v84, v84\n\t" \
    "v_cvt_pk_bf16_f32 v96, v80, v81\n\t" \
    "v_cvt_pk_bf16_f32 v97, v82, v83\n\t" \
    "v_mfma_f32_32x32x16_bf16 v[0:15], v[124:127], v[100:103], v[0:15]\n\t" \
    "v_exp_f32 v85, v85\n\t" \
    "v_exp_f32 v86, v86\n\t" \
    "v_exp_f32 v87, v87\n\t" \
    "v_exp_f32 v88, v88\n\t" \
    "v_cvt_pk_bf16_f32 v98, v84, v85\n\t" \
    "v_cvt_pk_bf16_f32 v99, v86, v87\n\t" \
    "v_exp_f32 v89, v89\n\t" \
    "v_exp_f32 v90, v90\n\t" \
    "v_mfma_f32_32x32x16_bf16 v[16:31], v[120:123], v[96:99], v[16:31]\n\t" \
    "v_exp_f32 v91, v91\n\t" \
    "v_exp_f32 v92, v92\n\t" \
    "v_exp_f32 v93, v93\n\t" \
    "v_exp_f32 v94, v94\n\t" \
    "v_exp_f32 v95, v95\n\t" \
    "v_cvt_pk_bf16_f32 v100, v88, v89\n\t" \
    "v_cvt_pk_bf16_f32 v101, v90, v91\n\t" \
    "v_cvt_pk_bf16_f32 v102, v92, v93\n\t" \
    "v_cvt_pk_bf16_f32 v103, v94, v95\n\t" \
    "v_mfma_f32_32x32x16_bf16 v[80:95], v[116:119], v[108:111], v[48:63]\n\t" \
    "ds_read_b128 v[112:115], %[ka] offset:22528\n\t" \
    "v_exp_f32 v64, v64\n\t" \
    "v_exp_f32 v65, v65\n\t" \
    "v_exp_f32 v66, v66\n\t" \
    "v_exp_f32 v67, v67\n\t" \
    "v_exp_f32 v68, v68\n\t" \
    "v_cvt_pk_bf16_f32 v96, v64, v65\n\t" \
    "v_cvt_pk_bf16_f32 v97, v66, v67\n\t" \
    "v_mfma_f32_32x32x16_bf16 v[16:31], v[124:127], v[100:103], v[16:31]\n\t" \
    "s_mov_b64 exec, %[vm]\n\t" \
    "ds_read_b128 v[120:123], %[va] offset:22528\n\t" \
    "ds_read_b128 v[124:127], %[va] offset:23040\n\t" \
    "s_mov_b64 exec, -1\n\t" \
    "v_exp_f32 v69, v69\n\t" \
    "v_exp_f32 v70, v70\n\t" \
    "v_exp_f32 v71, v71\n\t" \
    "v_exp_f32 v72, v72\n\t" \
    "v_cvt_pk_bf16_f32 v98, v68, v69\n\t" \
    "v_cvt_pk_bf16_f32 v99, v70, v71\n\t" \
    "v_exp_f32 v73, v73\n\t" \
    "v_exp_f32 v74, v74\n\t" \
    "v_mfma_f32_32x32x16_bf16 v[0:15], v[128:131], v[96:99], v[0:15]\n\t" \
    "v_exp_f32 v75, v75\n\t" \
    "v_exp_f32 v76, v76\n\t" \
    "v_exp_f32 v77, v77\n\t" \
    "v_exp_f32 v78, v78\n\t" \
    "v_exp_f32 v79, v79\n\t" \
    "v_cvt_pk_bf16_f32 v100, v72, v73\n\t" \
    "v_cvt_pk_bf16_f32 v101, v74, v75\n\t" \
    "v_cvt_pk_bf16_f32 v102, v76, v77\n\t" \
    "v_cvt_pk_bf16_f32 v103, v78, v79\n\t" \
    "s_waitcnt lgkmcnt(0)\n\t" \
    "v_mfma_f32_32x32x16_bf16 v[64:79], v[112:115], v[104:107], v[32:47]\n\t" \
    "v_exp_f32 v80, v80\n\t" \
    "v_exp_f32 v81, v81\n\t" \
    "v_exp_f32 v82, v82\n\t" \
    "v_exp_f32 v83, v83\n\t" \
    "v_exp_f32 v84, v84\n\t" \
    "v_cvt_pk_bf16_f32 v96, v80, v81\n\t" \
    "v_cvt_pk_bf16_f32 v97, v82, v83\n\t" \
    "v_mfma_f32_32x32x16_bf16 v[0:15], v[132:135], v[100:103], v[0:15]\n\t" \
    "v_exp_f32 v85, v85\n\t" \
    "v_exp_f32 v86, v86\n\t" \
    "v_exp_f32 v87, v87\n\t" \
    "v_exp_f32 v88, v88\n\t" \
    "v_cvt_pk_bf16_f32 v98, v84, v85\n\t" \
    "v_cvt_pk_bf16_f32 v99, v86, v87\n\t" \
    "v_exp_f32 v89, v89\n\t" \
    "v_exp_f32 v90, v90\n\t" \
    "v_mfma_f32_32x32x16_bf16 v[16:31], v[128:131], v[96:99], v[16:31]\n\t" \
    "v_exp_f32 v91, v91\n\t" \
    "v_exp_f32 v92, v92\n\t" \
    "v_exp_f32 v93, v93\n\t" \
    "v_exp_f32 v94, v94\n\t" \
    "v_exp_f32 v95, v95\n\t" \
    "v_cvt_pk_bf16_f32 v100, v88, v89\n\t" \
    "v_cvt_pk_bf16_f32 v101, v90, v91\n\t" \
    "v_cvt_pk_bf16_f32 v102, v92, v93\n\t" \
    "v_cvt_pk_bf16_f32 v103, v94, v95\n\t" \
    "v_mfma_f32_32x32x16_bf16 v[80:95], v[112:115], v[108:111], v[48:63]\n\t" \
    "ds_read_b128 v[116:119], %[ka] offset:23552\n\t" \
    "v_exp_f32 v64, v64\n\t" \
    "v_exp_f32 v65, v65\n\t" \
    "v_exp_f32 v66, v66\n\t" \
    "v_exp_f32 v67, v67\n\t" \
    "v_exp_f32 v68, v68\n\t" \
    "v_cvt_pk_bf16_f32 v96, v64, v65\n\t" \
    "v_cvt_pk_bf16_f32 v97, v66, v67\n\t" \
    "v_mfma_f32_32x32x16_bf16 v[16:31], v[132:135], v[100:103], v[16:31]\n\t" \
    "s_mov_b64 exec, %[vm]\n\t" \
    "ds_read_b128 v[128:131], %[va] offset:23552\n\t" \
    "ds_read_b128 v[132:135], %[va] offset:24064\n\t" \
    "s_mov_b64 exec, -1\n\t" \
    "v_exp_f32 v69, v69\n\t" \
    "v_exp_f32 v70, v70\n\t" \
    "v_exp_f32 v71, v71\n\t" \
    "v_exp_f32 v72, v72\n\t" \
    "v_cvt_pk_bf16_f32 v98, v68, v69\n\t" \
    "v_cvt_pk_bf16_f32 v99, v70, v71\n\t" \
    "v_exp_f32 v73, v73\n\t" \
    "v_exp_f32 v74, v74\n\t" \
    "v_mfma_f32_32x32x16_bf16 v[0:15], v[120:123], v[96:99], v[0:15]\n\t" \
    "v_exp_f32 v75, v75\n\t" \
    "v_exp_f32 v76, v76\n\t" \
    "v_exp_f32 v77, v77\n\t" \
    "v_exp_f32 v78, v78\n\t" \
    "v_exp_f32 v79, v79\n\t" \
    "v_cvt_pk_bf16_f32 v100, v72, v73\n\t" \
    "v_cvt_pk_bf16_f32 v101, v74, v75\n\t" \
    "v_cvt_pk_bf16_f32 v102, v76, v77\n\t" \
    "v_cvt_pk_bf16_f32 v103, v78, v79\n\t" \
    "s_waitcnt lgkmcnt(0)\n\t" \
    "v_mfma_f32_32x32x16_bf16 v[64:79], v[116:119], v[104:107], v[32:47]\n\t" \
    "v_exp_f32 v80, v80\n\t" \
    "v_exp_f32 v81, v81\n\t" \
    "v_exp_f32 v82, v82\n\t" \
    "v_exp_f32 v83, v83\n\t" \
    "v_exp_f32 v84, v84\n\t" \
    "v_cvt_pk_bf16_f32 v96, v80, v81\n\t" \
    "v_cvt_pk_bf16_f32 v97, v82, v83\n\t" \
    "v_mfma_f32_32x32x16_bf16 v[0:15], v[124:127], v[100:103], v[0:15]\n\t" \
    "v_exp_f32 v85, v85\n\t" \
    "v_exp_f32 v86, v86\n\t" \
    "v_exp_f32 v87, v87\n\t" \
    "v_exp_f32 v88, v88\n\t" \
    "v_cvt_pk_bf16_f32 v98, v84, v85\n\t" \
    "v_cvt_pk_bf16_f32 v99, v86, v87\n\t" \
    "v_exp_f32 v89, v89\n\t" \
    "v_exp_f32 v90, v90\n\t" \
    "v_mfma_f32_32x32x16_bf16 v[16:31], v[120:123], v[96:99], v[16:31]\n\t" \
    "v_exp_f32 v91, v91\n\t" \
    "v_exp_f32 v92, v92\n\t" \
    "v_exp_f32 v93, v93\n\t" \
    "v_exp_f32 v94, v94\n\t" \
    "v_exp_f32 v95, v95\n\t" \
    "v_cvt_pk_bf16_f32 v100, v88, v89\n\t" \
    "v_cvt_pk_bf16_f32 v101, v90, v91\n\t" \
    "v_cvt_pk_bf16_f32 v102, v92, v93\n\t" \
    "v_cvt_pk_bf16_f32 v103, v94, v95\n\t" \
    "v_mfma_f32_32x32x16_bf16 v[80:95], v[116:119], v[108:111], v[48:63]\n\t" \
    "ds_read_b128 v[112:115], %[ka] offset:24576\n\t" \
    "v_exp_f32 v64, v64\n\t" \
    "v_exp_f32 v65, v65\n\t" \
    "v_exp_f32 v66, v66\n\t" \
    "v_exp_f32 v67, v67\n\t" \
    "v_exp_f32 v68, v68\n\t" \
    "v_cvt_pk_bf16_f32 v96, v64, v65\n\t" \
    "v_cvt_pk_bf16_f32 v97, v66, v67\n\t" \
    "v_mfma_f32_32x32x16_bf16 v[16:31], v[124:127], v[100:103], v[16:31]\n\t" \
    "s_mov_b64 exec, %[vm]\n\t" \
    "ds_read_b128 v[120:123], %[va] offset:24576\n\t" \
    "ds_read_b128 v[124:127], %[va] offset:25088\n\t" \
    "s_mov_b64 exec, -1\n\t" \
    "v_exp_f32 v69, v69\n\t" \
    "v_exp_f32 v70, v70\n\t" \
    "v_exp_f32 v71, v71\n\t" \
    "v_exp_f32 v72, v72\n\t" \
    "v_cvt_pk_bf16_f32 v98, v68, v69\n\t" \
    "v_cvt_pk_bf16_f32 v99, v70, v71\n\t" \
    "v_exp_f32 v73, v73\n\t" \
    "v_exp_f32 v74, v74\n\t" \
    "v_mfma_f32_32x32x16_bf16 v[0:15], v[128:131], v[96:99], v[0:15]\n\t" \
    "v_exp_f32 v75, v75\n\t" \
    "v_exp_f32 v76, v76\n\t" \
    "v_exp_f32 v77, v77\n\t" \
    "v_exp_f32 v78, v78\n\t" \
    "v_exp_f32 v79, v79\n\t" \
    "v_cvt_pk_bf16_f32 v100, v72, v73\n\t" \
    "v_cvt_pk_bf16_f32 v101, v74, v75\n\t" \
    "v_cvt_pk_bf16_f32 v102, v76, v77\n\t" \
    "v_cvt_pk_bf16_f32 v103, v78, v79\n\t" \
    "s_waitcnt lgkmcnt(0)\n\t" \
    "v_mfma_f32_32x32x16_bf16 v[64:79], v[112:115], v[104:107], v[32:47]\n\t" \
    "v_exp_f32 v80, v80\n\t" \
    "v_exp_f32 v81, v81\n\t" \
    "v_exp_f32 v82, v82\n\t" \
    "v_exp_f32 v83, v83\n\t" \
    "v_exp_f32 v84, v84\n\t" \
    "v_cvt_pk_bf16_f32 v96, v80, v81\n\t" \
    "v_cvt_pk_bf16_f32 v97, v82, v83\n\t" \
    "v_mfma_f32_32x32x16_bf16 v[0:15], v[132:135], v[100:103], v[0:15]\n\t" \
    "v_exp_f32 v85, v85\n\t" \
    "v_exp_f32 v86, v86\n\t" \
    "v_exp_f32 v87, v87\n\t" \
    "v_exp_f32 v88, v88\n\t" \
    "v_cvt_pk_bf16_f32 v98, v84, v85\n\t" \
    "v_cvt_pk_bf16_f32 v99, v86, v87\n\t" \
    "v_exp_f32 v89, v89\n\t" \
    "v_exp_f32 v90, v90\n\t" \
    "v_mfma_f32_32x32x16_bf16 v[16:31], v[128:131], v[96:99], v[16:31]\n\t" \
    "v_exp_f32 v91, v91\n\t" \
    "v_exp_f32 v92, v92\n\t" \
    "v_exp_f32 v93, v93\n\t" \
    "v_exp_f32 v94, v94\n\t" \
    "v_exp_f32 v95, v95\n\t" \
    "v_cvt_pk_bf16_f32 v100, v88, v89\n\t" \
    "v_cvt_pk_bf16_f32 v101, v90, v91\n\t" \
    "v_cvt_pk_bf16_f32 v102, v92, v93\n\t" \
    "v_cvt_pk_bf16_f32 v103, v94, v95\n\t" \
    "v_mfma_f32_32x32x16_bf16 v[80:95], v[112:115], v[108:111], v[48:63]\n\t" \
    "ds_read_b128 v[116:119], %[ka] offset:25600\n\t" \
    "v_exp_f32 v64, v64\n\t" \
    "v_exp_f32 v65, v65\n\t" \
    "v_exp_f32 v66, v66\n\t" \
    "v_exp_f32 v67, v67\n\t" \
    "v_exp_f32 v68, v68\n\t" \
    "v_cvt_pk_bf16_f32 v96, v64, v65\n\t" \
    "v_cvt_pk_bf16_f32 v97, v66, v67\n\t" \
    "v_mfma_f32_32x32x16_bf16 v[16:31], v[132:135], v[100:103], v[16:31]\n\t" \
    "s_mov_b64 exec, %[vm]\n\t" \
    "ds_read_b128 v[128:131], %[va] offset:25600\n\t" \
    "ds_read_b128 v[132:135], %[va] offset:26112\n\t" \
    "s_mov_b64 exec, -1\n\t" \
    "v_exp_f32 v69, v69\n\t" \
    "v_exp_f32 v70, v70\n\t" \
    "v_exp_f32 v71, v71\n\t" \
    "v_exp_f32 v72, v72\n\t" \
    "v_cvt_pk_bf16_f32 v98, v68, v69\n\t" \
    "v_cvt_pk_bf16_f32 v99, v70, v71\n\t" \
    "v_exp_f32 v73, v73\n\t" \
    "v_exp_f32 v74, v74\n\t" \
    "v_mfma_f32_32x32x16_bf16 v[0:15], v[120:123], v[96:99], v[0:15]\n\t" \
    "v_exp_f32 v75, v75\n\t" \
    "v_exp_f32 v76, v76\n\t" \
    "v_exp_f32 v77, v77\n\t" \
    "v_exp_f32 v78, v78\n\t" \
    "v_exp_f32 v79, v79\n\t" \
    "v_cvt_pk_bf16_f32 v100, v72, v73\n\t" \
    "v_cvt_pk_bf16_f32 v101, v74, v75\n\t" \
    "v_cvt_pk_bf16_f32 v102, v76, v77\n\t" \
    "v_cvt_pk_bf16_f32 v103, v78, v79\n\t" \
    "s_waitcnt lgkmcnt(0)\n\t" \
    "v_mfma_f32_32x32x16_bf16 v[64:79], v[116:119], v[104:107], v[32:47]\n\t" \
    "v_exp_f32 v80, v80\n\t" \
    "v_exp_f32 v81, v81\n\t" \
    "v_exp_f32 v82, v82\n\t" \
    "v_exp_f32 v83, v83\n\t" \
    "v_exp_f32 v84, v84\n\t" \
    "v_cvt_pk_bf16_f32 v96, v80, v81\n\t" \
    "v_cvt_pk_bf16_f32 v97, v82, v83\n\t" \
    "v_mfma_f32_32x32x16_bf16 v[0:15], v[124:127], v[100:103], v[0:15]\n\t" \
    "v_exp_f32 v85, v85\n\t" \
    "v_exp_f32 v86, v86\n\t" \
    "v_exp_f32 v87, v87\n\t" \
    "v_exp_f32 v88, v88\n\t" \
    "v_cvt_pk_bf16_f32 v98, v84, v85\n\t" \
    "v_cvt_pk_bf16_f32 v99, v86, v87\n\t" \
    "v_exp_f32 v89, v89\n\t" \
    "v_exp_f32 v90, v90\n\t" \
    "v_mfma_f32_32x32x16_bf16 v[16:31], v[120:123], v[96:99], v[16:31]\n\t" \
    "v_exp_f32 v91, v91\n\t" \
    "v_exp_f32 v92, v92\n\t" \
    "v_exp_f32 v93, v93\n\t" \
    "v_exp_f32 v94, v94\n\t" \
    "v_exp_f32 v95, v95\n\t" \
    "v_cvt_pk_bf16_f32 v100, v88, v89\n\t" \
    "v_cvt_pk_bf16_f32 v101, v90, v91\n\t" \
    "v_cvt_pk_bf16_f32 v102, v92, v93\n\t" \
    "v_cvt_pk_bf16_f32 v103, v94, v95\n\t" \
    "v_mfma_f32_32x32x16_bf16 v[80:95], v[116:119], v[108:111], v[48:63]\n\t" \
    "ds_read_b128 v[112:115], %[ka] offset:26624\n\t" \
    "v_exp_f32 v64, v64\n\t" \
    "v_exp_f32 v65, v65\n\t" \
    "v_exp_f32 v66, v66\n\t" \
    "v_exp_f32 v67, v67\n\t" \
    "v_exp_f32 v68, v68\n\t" \
    "v_cvt_pk_bf16_f32 v96, v64, v65\n\t" \
    "v_cvt_pk_bf16_f32 v97, v66, v67\n\t" \
    "v_mfma_f32_32x32x16_bf16 v[16:31], v[124:127], v[100:103], v[16:31]\n\t" \
    "s_mov_b64 exec, %[vm]\n\t" \
    "ds_read_b128 v[120:123], %[va] offset:26624\n\t" \
    "ds_read_b128 v[124:127], %[va] offset:27136\n\t" \
    "s_mov_b64 exec, -1\n\t" \
    "v_exp_f32 v69, v69\n\t" \
    "v_exp_f32 v70, v70\n\t" \
    "v_exp_f32 v71, v71\n\t" \
    "v_exp_f32 v72, v72\n\t" \
    "v_cvt_pk_bf16_f32 v98, v68, v69\n\t" \
    "v_cvt_pk_bf16_f32 v99, v70, v71\n\t" \
    "v_exp_f32 v73, v73\n\t" \
    "v_exp_f32 v74, v74\n\t" \
    "v_mfma_f32_32x32x16_bf16 v[0:15], v[128:131], v[96:99], v[0:15]\n\t" \
    "v_exp_f32 v75, v75\n\t" \
    "v_exp_f32 v76, v76\n\t" \
    "v_exp_f32 v77, v77\n\t" \
    "v_exp_f32 v78, v78\n\t" \
    "v_exp_f32 v79, v79\n\t" \
    "v_cvt_pk_bf16_f32 v100, v72, v73\n\t" \
    "v_cvt_pk_bf16_f32 v101, v74, v75\n\t" \
    "v_cvt_pk_bf16_f32 v102, v76, v77\n\t" \
    "v_cvt_pk_bf16_f32 v103, v78, v79\n\t" \
    "s_waitcnt lgkmcnt(0)\n\t" \
    "v_mfma_f32_32x32x16_bf16 v[64:79], v[112:115], v[104:107], v[32:47]\n\t" \
    "v_exp_f32 v80, v80\n\t" \
    "v_exp_f32 v81, v81\n\t" \
    "v_exp_f32 v82, v82\n\t" \
    "v_exp_f32 v83, v83\n\t" \
    "v_exp_f32 v84, v84\n\t" \
    "v_cvt_pk_bf16_f32 v96, v80, v81\n\t" \
    "v_cvt_pk_bf16_f32 v97, v82, v83\n\t" \
    "v_mfma_f32_32x32x16_bf16 v[0:15], v[132:135], v[100:103], v[0:15]\n\t" \
    "v_exp_f32 v85, v85\n\t" \
    "v_exp_f32 v86, v86\n\t" \
    "v_exp_f32 v87, v87\n\t" \
    "v_exp_f32 v88, v88\n\t" \
    "v_cvt_pk_bf16_f32 v98, v84, v85\n\t" \
    "v_cvt_pk_bf16_f32 v99, v86, v87\n\t" \
    "v_exp_f32 v89, v89\n\t" \
    "v_exp_f32 v90, v90\n\t" \
    "v_mfma_f32_32x32x16_bf16 v[16:31], v[128:131], v[96:99], v[16:31]\n\t" \
    "v_exp_f32 v91, v91\n\t" \
    "v_exp_f32 v92, v92\n\t" \
    "v_exp_f32 v93, v93\n\t" \
    "v_exp_f32 v94, v94\n\t" \
    "v_exp_f32 v95, v95\n\t" \
    "v_cvt_pk_bf16_f32 v100, v88, v89\n\t" \
    "v_cvt_pk_bf16_f32 v101, v90, v91\n\t" \
    "v_cvt_pk_bf16_f32 v102, v92, v93\n\t" \
    "v_cvt_pk_bf16_f32 v103, v94, v95\n\t" \
    "v_mfma_f32_32x32x16_bf16 v[80:95], v[112:115], v[108:111], v[48:63]\n\t" \
    "ds_read_b128 v[116:119], %[ka] offset:27648\n\t" \
    "v_exp_f32 v64, v64\n\t" \
    "v_exp_f32 v65, v65\n\t" \
    "v_exp_f32 v66, v66\n\t" \
    "v_exp_f32 v67, v67\n\t" \
    "v_exp_f32 v68, v68\n\t" \
    "v_cvt_pk_bf16_f32 v96, v64, v65\n\t" \
    "v_cvt_pk_bf16_f32 v97, v66, v67\n\t" \
    "v_mfma_f32_32x32x16_bf16 v[16:31], v[132:135], v[100:103], v[16:31]\n\t" \
    "s_mov_b64 exec, %[vm]\n\t" \
    "ds_read_b128 v[128:131], %[va] offset:27648\n\t" \
    "ds_read_b128 v[132:135], %[va] offset:28160\n\t" \
    "s_mov_b64 exec, -1\n\t" \
    "v_exp_f32 v69, v69\n\t" \
    "v_exp_f32 v70, v70\n\t" \
    "v_exp_f32 v71, v71\n\t" \
    "v_exp_f32 v72, v72\n\t" \
    "v_cvt_pk_bf16_f32 v98, v68, v69\n\t" \
    "v_cvt_pk_bf16_f32 v99, v70, v71\n\t" \
    "v_exp_f32 v73, v73\n\t" \
    "v_exp_f32 v74, v74\n\t" \
    "v_mfma_f32_32x32x16_bf16 v[0:15], v[120:123], v[96:99], v[0:15]\n\t" \
    "v_exp_f32 v75, v75\n\t" \
    "v_exp_f32 v76, v76\n\t" \
    "v_exp_f32 v77, v77\n\t" \
    "v_exp_f32 v78, v78\n\t" \
    "v_exp_f32 v79, v79\n\t" \
    "v_cvt_pk_bf16_f32 v100, v72, v73\n\t" \
    "v_cvt_pk_bf16_f32 v101, v74, v75\n\t" \
    "v_cvt_pk_bf16_f32 v102, v76, v77\n\t" \
    "v_cvt_pk_bf16_f32 v103, v78, v79\n\t" \
    "s_waitcnt lgkmcnt(0)\n\t" \
    "v_mfma_f32_32x32x16_bf16 v[64:79], v[116:119], v[104:107], v[32:47]\n\t" \
    "v_exp_f32 v80, v80\n\t" \
    "v_exp_f32 v81, v81\n\t" \
    "v_exp_f32 v82, v82\n\t" \
    "v_exp_f32 v83, v83\n\t" \
    "v_exp_f32 v84, v84\n\t" \
    "v_cvt_pk_bf16_f32 v96, v80, v81\n\t" \
    "v_cvt_pk_bf16_f32 v97, v82, v83\n\t" \
    "v_mfma_f32_32x32x16_bf16 v[0:15], v[124:127], v[100:103], v[0:15]\n\t" \
    "v_exp_f32 v85, v85\n\t" \
    "v_exp_f32 v86, v86\n\t" \
    "v_exp_f32 v87, v87\n\t" \
    "v_exp_f32 v88, v88\n\t" \
    "v_cvt_pk_bf16_f32 v98, v84, v85\n\t" \
    "v_cvt_pk_bf16_f32 v99, v86, v87\n\t" \
    "v_exp_f32 v89, v89\n\t" \
    "v_exp_f32 v90, v90\n\t" \
    "v_mfma_f32_32x32x16_bf16 v[16:31], v[120:123], v[96:99], v[16:31]\n\t" \
    "v_exp_f32 v91, v91\n\t" \
    "v_exp_f32 v92, v92\n\t" \
    "v_exp_f32 v93, v93\n\t" \
    "v_exp_f32 v94, v94\n\t" \
    "v_exp_f32 v95, v95\n\t" \
    "v_cvt_pk_bf16_f32 v100, v88, v89\n\t" \
    "v_cvt_pk_bf16_f32 v101, v90, v91\n\t" \
    "v_cvt_pk_bf16_f32 v102, v92, v93\n\t" \
    "v_cvt_pk_bf16_f32 v103, v94, v95\n\t" \
    "v_mfma_f32_32x32x16_bf16 v[80:95], v[116:119], v[108:111], v[48:63]\n\t" \
    "ds_read_b128 v[112:115], %[ka] offset:28672\n\t" \
    "v_exp_f32 v64, v64\n\t" \
    "v_exp_f32 v65, v65\n\t" \
    "v_exp_f32 v66, v66\n\t" \
    "v_exp_f32 v67, v67\n\t" \
    "v_exp_f32 v68, v68\n\t" \
    "v_cvt_pk_bf16_f32 v96, v64, v65\n\t" \
    "v_cvt_pk_bf16_f32 v97, v66, v67\n\t" \
    "v_mfma_f32_32x32x16_bf16 v[16:31], v[124:127], v[100:103], v[16:31]\n\t" \
    "s_mov_b64 exec, %[vm]\n\t" \
    "ds_read_b128 v[120:123], %[va] offset:28672\n\t" \
    "ds_read_b128 v[124:127], %[va] offset:29184\n\t" \
    "s_mov_b64 exec, -1\n\t" \
    "v_exp_f32 v69, v69\n\t" \
    "v_exp_f32 v70, v70\n\t" \
    "v_exp_f32 v71, v71\n\t" \
    "v_exp_f32 v72, v72\n\t" \
    "v_cvt_pk_bf16_f32 v98, v68, v69\n\t" \
    "v_cvt_pk_bf16_f32 v99, v70, v71\n\t" \
    "v_exp_f32 v73, v73\n\t" \
    "v_exp_f32 v74, v74\n\t" \
    "v_mfma_f32_32x32x16_bf16 v[0:15], v[128:131], v[96:99], v[0:15]\n\t" \
    "v_exp_f32 v75, v75\n\t" \
    "v_exp_f32 v76, v76\n\t" \
    "v_exp_f32 v77, v77\n\t" \
    "v_exp_f32 v78, v78\n\t" \
    "v_exp_f32 v79, v79\n\t" \
    "v_cvt_pk_bf16_f32 v100, v72, v73\n\t" \
    "v_cvt_pk_bf16_f32 v101, v74, v75\n\t" \
    "v_cvt_pk_bf16_f32 v102, v76, v77\n\t" \
    "v_cvt_pk_bf16_f32 v103, v78, v79\n\t" \
    "s_waitcnt lgkmcnt(0)\n\t" \
    "v_mfma_f32_32x32x16_bf16 v[64:79], v[112:115], v[104:107], v[32:47]\n\t" \
    "v_exp_f32 v80, v80\n\t" \
    "v_exp_f32 v81, v81\n\t" \
    "v_exp_f32 v82, v82\n\t" \
    "v_exp_f32 v83, v83\n\t" \
    "v_exp_f32 v84, v84\n\t" \
    "v_cvt_pk_bf16_f32 v96, v80, v81\n\t" \
    "v_cvt_pk_bf16_f32 v97, v82, v83\n\t" \
    "v_mfma_f32_32x32x16_bf16 v[0:15], v[132:135], v[100:103], v[0:15]\n\t" \
    "v_exp_f32 v85, v85\n\t" \
    "v_exp_f32 v86, v86\n\t" \
    "v_exp_f32 v87, v87\n\t" \
    "v_exp_f32 v88, v88\n\t" \
    "v_cvt_pk_bf16_f32 v98, v84, v85\n\t" \
    "v_cvt_pk_bf16_f32 v99, v86, v87\n\t" \
    "v_exp_f32 v89, v89\n\t" \
    "v_exp_f32 v90, v90\n\t" \
    "v_mfma_f32_32x32x16_bf16 v[16:31], v[128:131], v[96:99], v[16:31]\n\t" \
    "v_exp_f32 v91, v91\n\t" \
    "v_exp_f32 v92, v92\n\t" \
    "v_exp_f32 v93, v93\n\t" \
    "v_exp_f32 v94, v94\n\t" \
    "v_exp_f32 v95, v95\n\t" \
    "v_cvt_pk_bf16_f32 v100, v88, v89\n\t" \
    "v_cvt_pk_bf16_f32 v101, v90, v91\n\t" \
    "v_cvt_pk_bf16_f32 v102, v92, v93\n\t" \
    "v_cvt_pk_bf16_f32 v103, v94, v95\n\t" \
    "v_mfma_f32_32x32x16_bf16 v[80:95], v[112:115], v[108:111], v[48:63]\n\t" \
    "ds_read_b128 v[116:119], %[ka] offset:29696\n\t" \
    "v_exp_f32 v64, v64\n\t" \
    "v_exp_f32 v65, v65\n\t" \
    "v_exp_f32 v66, v66\n\t" \
    "v_exp_f32 v67, v67\n\t" \
    "v_exp_f32 v68, v68\n\t" \
    "v_cvt_pk_bf16_f32 v96, v64, v65\n\t" \
    "v_cvt_pk_bf16_f32 v97, v66, v67\n\t" \
    "v_mfma_f32_32x32x16_bf16 v[16:31], v[132:135], v[100:103], v[16:31]\n\t" \
    "s_mov_b64 exec, %[vm]\n\t" \
    "ds_read_b128 v[128:131], %[va] offset:29696\n\t" \
    "ds_read_b128 v[132:135], %[va] offset:30208\n\t" \
    "s_mov_b64 exec, -1\n\t" \
    "v_exp_f32 v69, v69\n\t" \
    "v_exp_f32 v70, v70\n\t" \
    "v_exp_f32 v71, v71\n\t" \
    "v_exp_f32 v72, v72\n\t" \
    "v_cvt_pk_bf16_f32 v98, v68, v69\n\t" \
    "v_cvt_pk_bf16_f32 v99, v70, v71\n\t" \
    "v_exp_f32 v73, v73\n\t" \
    "v_exp_f32 v74, v74\n\t" \
    "v_mfma_f32_32x32x16_bf16 v[0:15], v[120:123], v[96:99], v[0:15]\n\t" \
    "v_exp_f32 v75, v75\n\t" \
    "v_exp_f32 v76, v76\n\t" \
    "v_exp_f32 v77, v77\n\t" \
    "v_exp_f32 v78, v78\n\t" \
    "v_exp_f32 v79, v79\n\t" \
    "v_cvt_pk_bf16_f32 v100, v72, v73\n\t" \
    "v_cvt_pk_bf16_f32 v101, v74, v75\n\t" \
    "v_cvt_pk_bf16_f32 v102, v76, v77\n\t" \
    "v_cvt_pk_bf16_f32 v103, v78, v79\n\t" \
    "s_waitcnt lgkmcnt(0)\n\t" \
    "v_mfma_f32_32x32x16_bf16 v[64:79], v[116:119], v[104:107], v[32:47]\n\t" \
    "v_exp_f32 v80, v80\n\t" \
    "v_exp_f32 v81, v81\n\t" \
    "v_exp_f32 v82, v82\n\t" \
    "v_exp_f32 v83, v83\n\t" \
    "v_exp_f32 v84, v84\n\t" \
    "v_cvt_pk_bf16_f32 v96, v80, v81\n\t" \
    "v_cvt_pk_bf16_f32 v97, v82, v83\n\t" \
    "v_mfma_f32_32x32x16_bf16 v[0:15], v[124:127], v[100:103], v[0:15]\n\t" \
    "v_exp_f32 v85, v85\n\t" \
    "v_exp_f32 v86, v86\n\t" \
    "v_exp_f32 v87, v87\n\t" \
    "v_exp_f32 v88, v88\n\t" \
    "v_cvt_pk_bf16_f32 v98, v84, v85\n\t" \
    "v_cvt_pk_bf16_f32 v99, v86, v87\n\t" \
    "v_exp_f32 v89, v89\n\t" \
    "v_exp_f32 v90, v90\n\t" \
    "v_mfma_f32_32x32x16_bf16 v[16:31], v[120:123], v[96:99], v[16:31]\n\t" \
    "v_exp_f32 v91, v91\n\t" \
    "v_exp_f32 v92, v92\n\t" \
    "v_exp_f32 v93, v93\n\t" \
    "v_exp_f32 v94, v94\n\t" \
    "v_exp_f32 v95, v95\n\t" \
    "v_cvt_pk_bf16_f32 v100, v88, v89\n\t" \
    "v_cvt_pk_bf16_f32 v101, v90, v91\n\t" \
    "v_cvt_pk_bf16_f32 v102, v92, v93\n\t" \
    "v_cvt_pk_bf16_f32 v103, v94, v95\n\t" \
    "s_setprio 0\n\t" \
    "v_mfma_f32_32x32x16_bf16 v[80:95], v[116:119], v[108:111], v[48:63]\n\t" \
    "ds_read_b128 v[112:115], %[ka] offset:30720\n\t" \
    "v_exp_f32 v64, v64\n\t" \
    "v_exp_f32 v65, v65\n\t" \
    "v_exp_f32 v66, v66\n\t" \
    "v_exp_f32 v67, v67\n\t" \
    "v_exp_f32 v68, v68\n\t" \
    "v_cvt_pk_bf16_f32 v96, v64, v65\n\t" \
    "v_cvt_pk_bf16_f32 v97, v66, v67\n\t" \
    "v_mfma_f32_32x32x16_bf16 v[16:31], v[124:127], v[100:103], v[16:31]\n\t" \
    "s_mov_b64 exec, %[vm]\n\t" \
    "ds_read_b128 v[120:123], %[va] offset:30720\n\t" \
    "ds_read_b128 v[124:127], %[va] offset:31232\n\t" \
    "s_mov_b64 exec, -1\n\t" \
    "v_exp_f32 v69, v69\n\t" \
    "v_exp_f32 v70, v70\n\t" \
    "v_exp_f32 v71, v71\n\t" \
    "v_exp_f32 v72, v72\n\t" \
    "v_cvt_pk_bf16_f32 v98, v68, v69\n\t" \
    "v_cvt_pk_bf16_f32 v99, v70, v71\n\t" \
    "v_exp_f32 v73, v73\n\t" \
    "v_exp_f32 v74, v74\n\t" \
    "v_mfma_f32_32x32x16_bf16 v[0:15], v[128:131], v[96:99], v[0:15]\n\t" \
    "v_exp_f32 v75, v75\n\t" \
    "v_exp_f32 v76, v76\n\t" \
    "v_exp_f32 v77, v77\n\t" \
    "v_exp_f32 v78, v78\n\t" \
    "v_exp_f32 v79, v79\n\t" \
    "v_cvt_pk_bf16_f32 v100, v72, v73\n\t" \
    "v_cvt_pk_bf16_f32 v101, v74, v75\n\t" \
    "v_cvt_pk_bf16_f32 v102, v76, v77\n\t" \
    "v_cvt_pk_bf16_f32 v103, v78, v79\n\t" \
    "s_waitcnt lgkmcnt(0)\n\t" \
    "v_mfma_f32_32x32x16_bf16 v[64:79], v[112:115], v[104:107], v[32:47]\n\t" \
    "v_exp_f32 v80, v80\n\t" \
    "v_exp_f32 v81, v81\n\t" \
    "v_exp_f32 v82, v82\n\t" \
    "v_exp_f32 v83, v83\n\t" \
    "v_exp_f32 v84, v84\n\t" \
    "v_cvt_pk_bf16_f32 v96, v80, v81\n\t" \
    "v_cvt_pk_bf16_f32 v97, v82, v83\n\t" \
    "v_mfma_f32_32x32x16_bf16 v[0:15], v[132:135], v[100:103], v[0:15]\n\t" \
    "v_exp_f32 v85, v85\n\t" \
    "v_exp_f32 v86, v86\n\t" \
    "v_exp_f32 v87, v87\n\t" \
    "v_exp_f32 v88, v88\n\t" \
    "v_cvt_pk_bf16_f32 v98, v84, v85\n\t" \
    "v_cvt_pk_bf16_f32 v99, v86, v87\n\t" \
    "v_exp_f32 v89, v89\n\t" \
    "v_exp_f32 v90, v90\n\t" \
    "v_mfma_f32_32x32x16_bf16 v[16:31], v[128:131], v[96:99], v[16:31]\n\t" \
    "v_exp_f32 v91, v91\n\t" \
    "v_exp_f32 v92, v92\n\t" \
    "v_exp_f32 v93, v93\n\t" \
    "v_exp_f32 v94, v94\n\t" \
    "v_exp_f32 v95, v95\n\t" \
    "v_cvt_pk_bf16_f32 v100, v88, v89\n\t" \
    "v_cvt_pk_bf16_f32 v101, v90, v91\n\t" \
    "v_cvt_pk_bf16_f32 v102, v92, v93\n\t" \
    "v_cvt_pk_bf16_f32 v103, v94, v95\n\t" \
    "v_mfma_f32_32x32x16_bf16 v[80:95], v[112:115], v[108:111], v[48:63]\n\t" \
    "ds_read_b128 v[116:119], %[ka] offset:31744\n\t" \
    "v_exp_f32 v64, v64\n\t" \
    "v_exp_f32 v65, v65\n\t" \
    "v_exp_f32 v66, v66\n\t" \
    "v_exp_f32 v67, v67\n\t" \
    "v_exp_f32 v68, v68\n\t" \
    "v_cvt_pk_bf16_f32 v96, v64, v65\n\t" \
    "v_cvt_pk_bf16_f32 v97, v66, v67\n\t" \
    "v_mfma_f32_32x32x16_bf16 v[16:31], v[132:135], v[100:103], v[16:31]\n\t" \
    "s_mov_b64 exec, %[vm]\n\t" \
    "ds_read_b128 v[128:131], %[va] offset:31744\n\t" \
    "ds_read_b128 v[132:135], %[va] offset:32256\n\t" \
    "s_mov_b64 exec, -1\n\t" \
    "v_exp_f32 v69, v69\n\t" \
    "v_exp_f32 v70, v70\n\t" \
    "v_exp_f32 v71, v71\n\t" \
    "v_exp_f32 v72, v72\n\t" \
    "v_cvt_pk_bf16_f32 v98, v68, v69\n\t" \
    "v_cvt_pk_bf16_f32 v99, v70, v71\n\t" \
    "v_exp_f32 v73, v73\n\t" \
    "v_exp_f32 v74, v74\n\t" \
    "v_mfma_f32_32x32x16_bf16 v[0:15], v[120:123], v[96:99], v[0:15]\n\t" \
    "v_exp_f32 v75, v75\n\t" \
    "v_exp_f32 v76, v76\n\t" \
    "v_exp_f32 v77, v77\n\t" \
    "v_exp_f32 v78, v78\n\t" \
    "v_exp_f32 v79, v79\n\t" \
    "v_cvt_pk_bf16_f32 v100, v72, v73\n\t" \
    "v_cvt_pk_bf16_f32 v101, v74, v75\n\t" \
    "v_cvt_pk_bf16_f32 v102, v76, v77\n\t" \
    "v_cvt_pk_bf16_f32 v103, v78, v79\n\t" \
    "s_waitcnt lgkmcnt(0)\n\t" \
    "v_mfma_f32_32x32x16_bf16 v[64:79], v[116:119], v[104:107], v[32:47]\n\t" \
    "v_exp_f32 v80, v80\n\t" \
    "v_exp_f32 v81, v81\n\t" \
    "v_exp_f32 v82, v82\n\t" \
    "v_exp_f32 v83, v83\n\t" \
    "v_exp_f32 v84, v84\n\t" \
    "v_cvt_pk_bf16_f32 v96, v80, v81\n\t" \
    "v_cvt_pk_bf16_f32 v97, v82, v83\n\t" \
    "v_mfma_f32_32x32x16_bf16 v[0:15], v[124:127], v[100:103], v[0:15]\n\t" \
    "v_exp_f32 v85, v85\n\t" \
    "v_exp_f32 v86, v86\n\t" \
    "v_exp_f32 v87, v87\n\t" \
    "v_exp_f32 v88, v88\n\t" \
    "v_cvt_pk_bf16_f32 v98, v84, v85\n\t" \
    "v_cvt_pk_bf16_f32 v99, v86, v87\n\t" \
    "v_exp_f32 v89, v89\n\t" \
    "v_exp_f32 v90, v90\n\t" \
    "v_mfma_f32_32x32x16_bf16 v[16:31], v[120:123], v[96:99], v[16:31]\n\t" \
    "v_exp_f32 v91, v91\n\t" \
    "v_exp_f32 v92, v92\n\t" \
    "v_exp_f32 v93, v93\n\t" \
    "v_exp_f32 v94, v94\n\t" \
    "v_exp_f32 v95, v95\n\t" \
    "v_cvt_pk_bf16_f32 v100, v88, v89\n\t" \
    "v_cvt_pk_bf16_f32 v101, v90, v91\n\t" \
    "v_cvt_pk_bf16_f32 v102, v92, v93\n\t" \
    "v_cvt_pk_bf16_f32 v103, v94, v95\n\t" \
    "v_mfma_f32_32x32x16_bf16 v[80:95], v[116:119], v[108:111], v[48:63]\n\t" \
    "v_exp_f32 v64, v64\n\t" \
    "v_exp_f32 v65, v65\n\t" \
    "v_exp_f32 v66, v66\n\t" \
    "v_exp_f32 v67, v67\n\t" \
    "v_exp_f32 v68, v68\n\t" \
    "v_cvt_pk_bf16_f32 v96, v64, v65\n\t" \
    "v_cvt_pk_bf16_f32 v97, v66, v67\n\t" \
    "v_mfma_f32_32x32x16_bf16 v[16:31], v[124:127], v[100:103], v[16:31]\n\t" \
    "v_exp_f32 v69, v69\n\t" \
    "v_exp_f32 v70, v70\n\t" \
    "v_exp_f32 v71, v71\n\t" \
    "v_exp_f32 v72, v72\n\t" \
    "v_cvt_pk_bf16_f32 v98, v68, v69\n\t" \
    "v_cvt_pk_bf16_f32 v99, v70, v71\n\t" \
    "v_exp_f32 v73, v73\n\t" \
    "v_exp_f32 v74, v74\n\t" \
    "v_mfma_f32_32x32x16_bf16 v[0:15], v[128:131], v[96:99], v[0:15]\n\t" \
    "v_exp_f32 v75, v75\n\t" \
    "v_exp_f32 v76, v76\n\t" \
    "v_exp_f32 v77, v77\n\t" \
    "v_exp_f32 v78, v78\n\t" \
    "v_exp_f32 v79, v79\n\t" \
    "v_cvt_pk_bf16_f32 v100, v72, v73\n\t" \
    "v_cvt_pk_bf16_f32 v101, v74, v75\n\t" \
    "v_cvt_pk_bf16_f32 v102, v76, v77\n\t" \
    "v_cvt_pk_bf16_f32 v103, v78, v79\n\t" \
    "v_exp_f32 v80, v80\n\t" \
    "v_exp_f32 v81, v81\n\t" \
    "v_exp_f32 v82, v82\n\t" \
    "v_exp_f32 v83, v83\n\t" \
    "v_exp_f32 v84, v84\n\t" \
    "v_cvt_pk_bf16_f32 v96, v80, v81\n\t" \
    "v_cvt_pk_bf16_f32 v97, v82, v83\n\t" \
    "v_mfma_f32_32x32x16_bf16 v[0:15], v[132:135], v[100:103], v[0:15]\n\t" \
    "v_exp_f32 v85, v85\n\t" \
    "v_exp_f32 v86, v86\n\t" \
    "v_exp_f32 v87, v87\n\t" \
    "v_exp_f32 v88, v88\n\t" \
    "v_cvt_pk_bf16_f32 v98, v84, v85\n\t" \
    "v_cvt_pk_bf16_f32 v99, v86, v87\n\t" \
    "v_exp_f32 v89, v89\n\t" \
    "v_exp_f32 v90, v90\n\t" \
    "v_mfma_f32_32x32x16_bf16 v[16:31], v[128:131], v[96:99], v[16:31]\n\t" \
    "v_exp_f32 v91, v91\n\t" \
    "v_exp_f32 v92, v92\n\t" \
    "v_exp_f32 v93, v93\n\t" \
    "v_exp_f32 v94, v94\n\t" \
    "v_exp_f32 v95, v95\n\t" \
    "v_cvt_pk_bf16_f32 v100, v88, v89\n\t" \
    "v_cvt_pk_bf16_f32 v101, v90, v91\n\t" \
    "v_cvt_pk_bf16_f32 v102, v92, v93\n\t" \
    "v_cvt_pk_bf16_f32 v103, v94, v95\n\t" \
    "s_nop 1\n\t" \
    "v_mfma_f32_32x32x16_bf16 v[16:31], v[132:135], v[100:103], v[16:31]\n\t" \
    "s_mov_b32 m0, %[sv]\n\t" \
    "s_waitcnt vmcnt(0)\n\t" \
    "s_nop 9\n\t"
#define ATT_CLOB "v32", "v33", "v34", "v35", "v36", "v37", "v38", "v39", "v40", "v41", "v42", "v43", "v44", "v45", "v46", "v47", "v48", "v49", "v50", "v51", "v52", "v53", "v54", "v55", "v56", "v57", "v58", "v59", "v60", "v61", "v62", "v63", "v64", "v65", "v66", "v67", "v68", "v69", "v70", "v71", "v72", "v73", "v74", "v75", "v76", "v77", "v78", "v79", "v80", "v81", "v82", "v83", "v84", "v85", "v86", "v87", "v88", "v89", "v90", "v91", "v92", "v93", "v94", "v95", "v96", "v97", "v98", "v99", "v100", "v101", "v102", "v103", "v112", "v113", "v114", "v115", "v116", "v117", "v118", "v119", "v120", "v121", "v122", "v123", "v124", "v125", "v126", "v127", "v128", "v129", "v130", "v131", "v132", "v133", "v134", "v135", "v136", "v137"
constexpr int K2_XCH = 131072, K2_LDS = K2_XCH + 8 * 64 * 36 + 16;
__device__ __forceinline__ float rowmax16(const f32x16 s, float m) {
#pragma unroll
    for (int r = 0; r < 16; r += 2) m = fmaxf(fmaxf(m, s[r]), s[r + 1]);
    return m;
}
__device__ __forceinline__ float halfmax(float m) {
    auto rr = __builtin_amdgcn_permlane32_swap(__float_as_uint(m), __float_as_uint(m), false, false);
    return fmaxf(__uint_as_float(rr[0]), __uint_as_float(rr[1]));
}
__device__ __forceinline__ int lane_id() {
    int l;
    asm volatile("v_mbcnt_lo_u32_b32 %0, -1, 0\n\tv_mbcnt_hi_u32_b32 %0, -1, %0" : "=v"(l));
    return l;
}
#define hand_off(SYNC, own, f0, f1) do { unsigned vt_; asm volatile(SYNC : "+s"(hstat), [vt] "=&v"(vt_) TSAB : [vs] "v"(own), [v1] "v"(1), \
        [vf] "v"((lane & 16 ? (f1) : (f0)) + (lane & 15) * 128), [sh] "s"(hstat), [w] "s"(w) : "memory", "scc", "vcc"); } while (0)
__global__ __launch_bounds__(768, 3) void k2_rest(char* ws, const char* wo, const char* wg, const char* wu, const char* wd, const char* x, char* out) {
    extern __shared__ __attribute__((aligned(16))) char smem[];
    const lds_ptr lds = (lds_ptr)smem;
    __builtin_amdgcn_sched_barrier(0);
    TSDECL; TSDECL2; TSTAMP(0);
    const unsigned lds0 = (unsigned)(uintptr_t)lds;
    const int w = __builtin_amdgcn_readfirstlane(threadIdx.x >> 6), b = blockIdx.x, xcd = b & 7, bi = b >> 3;
    char *oaf = ws + WS_O, *haf = ws + WS_H, *act = ws + WS_ACT, *hstat = ws + WS_ST;
    {
    const int lane = threadIdx.x & 63, head = xcd * 4 + (bi & 3), qb = b >> 5;
    const int hi = lane >> 5, r32 = lane & 31, qp = w & 3, half = w >> 2;
    LDS_AS unsigned* flag = (LDS_AS unsigned*)(lds + K2_LDS - 16);
    if (w > 8) asm volatile("s_barrier\n\ts_barrier" ::: "memory");
    if (w == 8) {
        *flag = 0;
        const int cg = ((b & 7) * 7 + (bi >> 2)) * 4 + (bi & 3);
        const char* src = b >= 224 ? wg : cg < 112 ? wg + cg * 32 : wu + (cg - 112) * 32;
        unsigned vo = (lane >> 1) * 3584 + (lane & 1) * 16, po = lane * 128, po2 = (bi * 16 + (lane >> 4)) * 3584 + (b & 1) * 1792 + min(lane & 15, 13) * 128;
        unsigned so = (cg * 16 + (lane & 1) * 8 + (lane >> 4) * 4) * 4;
        u32x4 sel;
#pragma unroll
        for (int j = 0; j < 4; ++j) sel[j] = (lane & 15) == 2 * j ? 0x3F80u : (lane & 15) == 2 * j + 1 ? 0x3F800000u : 0u;
        asm volatile("s_setprio 3\n\ts_waitcnt lgkmcnt(0)\n\ts_barrier\n\ts_barrier" : "+v"(vo), "+v"(po), "+v"(po2), "+v"(so), "+v"(sel) :: "memory");
        u32x4 d[16];
        unsigned p0 = 0;
#pragma unroll
        for (int i = 0; i < 16; ++i) asm volatile("global_load_dwordx4 %0, %1, %2" : "=&v"(d[i]) : "v"(vo), "s"(src + i * (32 * 3584)) : "memory");
#pragma unroll
        for (int i = 0; i < 11; ++i)
            asm volatile("global_load_dword %0, %1, %2" : "+v"(p0) : "v"(i < 3 ? po : po2),
                         "s"(i == 0 ? wd + min(b, 223) * 8192 : i < 3 ? wo + bi * 16384 + (i - 1) * 8192 : (i < 7 ? wg : wu) + ((i - 3) & 3) * (4 * 3584)) : "memory");
        po = (bi * 16 + (lane & 15)) * 128;
        asm volatile(CODE_PF("%0", "%1") : "+v"(p0), "+v"(po) :: "memory", "vcc", "scc");
        asm volatile("s_waitcnt vmcnt(0)" : "+v"(d[0]), "+v"(d[1]), "+v"(d[2]), "+v"(d[3]), "+v"(d[4]), "+v"(d[5]), "+v"(d[6]), "+v"(d[7]),
                     "+v"(d[8]), "+v"(d[9]), "+v"(d[10]), "+v"(d[11]), "+v"(d[12]), "+v"(d[13]), "+v"(d[14]), "+v"(d[15]), "+v"(p0));
        f32x4 acc = {};
#pragma unroll
        for (int i = 0; i < 16; ++i) acc = MFMA16(__builtin_bit_cast(bf16x8, sel), __builtin_bit_cast(bf16x8, d[i]), acc);
#pragma unroll
        for (int j = 0; j < 4; ++j) {
            acc[j] += __uint_as_float(__builtin_amdgcn_update_dpp(0u, __float_as_uint(acc[j]), 0x122, 15, 15, false));
            acc[j] += __uint_as_float(__builtin_amdgcn_update_dpp(0u, __float_as_uint(acc[j]), 0x124, 15, 15, false));
            acc[j] += __uint_as_float(__builtin_amdgcn_update_dpp(0u, __float_as_uint(acc[j]), 0x128, 15, 15, false));
        }
        if (b < 224) {
            if ((lane & 46) == 0) ST16(hstat, so + CSUM, 0, acc);
            asm volatile("s_waitcnt vmcnt(0)\n\ts_mov_b64 exec, 1\n\tglobal_store_dword %0, %1, %2" STMOD "\n\ts_mov_b64 exec, -1"
                         :: "v"(FLAGC + (((cg >= 112 ? cg - 112 : cg) >> 3) * 16 + (cg >= 112) * 8 + (cg & 7)) * 128), "v"(1), "s"(hstat) : "memory");
        }
        asm volatile("s_setprio 0");
    }
    if (w >= 8) {
        lds_bar();
        if (__builtin_amdgcn_readfirstlane(*flag)) asm volatile("s_barrier\n\ts_barrier\n\ts_barrier" ::: "memory");
    } else {
    const int g0 = (qp >> 1) * 65536 + (half ? 32768 + (qp & 1) * 4096 : 4096 + (qp & 1) * 40960);
    const int g1 = (qp >> 1) * 65536 + 24576 + (qp & 1) * 4096;
    const unsigned cs1 = (r32 == 16 || r32 == 20) ? 0x3F803F80u : 0u;
    const u32x2 cs = {cs1, cs1};
    f32x2 m = {};
    bf16x8 q0 = {}, q1 = {};
    float o[9];
    const unsigned ka = lds0 + half * 32768 + lane * 16, va = lds0 + 65536 + half * 32768 + (hi * 16 + (r32 & 15)) * 16;
    TSTAMP(1);
    for (int pass = 0;; ++pass) {
        f32x16 o0, o1;
        unsigned sv;
        asm volatile(ATT_ASM
                     : "=&{v[0:15]}"(o0), "=&{v[16:31]}"(o1), "+{v[104:107]}"(q0), "+{v[108:111]}"(q1), "+{v[138:139]}"(m), [sv] "=&s"(sv)
                     : [ka] "v"(ka), [va] "v"(va), [lead] "s"(half), [pass] "s"(pass), [cs] "v"(cs), [l16] "v"(lane * 16),
                       [qo0] "v"(lane * 16 + half * 1024), [qo1] "v"(lane * 16 + 1024 - half * 1024), [kr] "v"(lds0 + 32768 + lane * 16),
                       [qb] "s"(ws + (head * 64 + qb * 8 + qp * 2) * 1024), [kv] "s"(ws + WS_KV + head * 131072), [lds0] "s"(lds0),
                       [g0] "s"(g0), [g1] "s"(g1), [vm] "s"(0x0000FFFF0000FFFFull)
                     : "memory", "scc", ATT_CLOB);
        float chk = 0.f;
#pragma unroll
        for (int r = 0; r < 9; ++r) chk = __builtin_fmaf(o0[r], 0.f, __builtin_fmaf(o1[r], 0.f, chk));
        if (!(chk == 0.f) || !(fmaxf(o0[8], o1[8]) < 1.0e30f)) *flag = 1;
        const f32x16 snd = o1, kp = o0;
        const lds_ptr xw = lds + K2_XCH + w * 2304, xr = lds + K2_XCH + (w ^ 4) * 2304;
        *(LDS_AS f32x4*)(xw + lane * 16) = (f32x4){snd[0], snd[1], snd[2], snd[3]};
        *(LDS_AS f32x4*)(xw + 1024 + lane * 16) = (f32x4){snd[4], snd[5], snd[6], snd[7]};
        *(LDS_AS float*)(xw + 2048 + lane * 4) = snd[8];
        lds_bar();
        const f32x4 r0 = *(LDS_AS f32x4*)(xr + lane * 16), r1 = *(LDS_AS f32x4*)(xr + 1024 + lane * 16);
        const float r2 = *(LDS_AS float*)(xr + 2048 + lane * 4);
        const unsigned bad = __builtin_amdgcn_readfirstlane(*flag);
#pragma unroll
        for (int r = 0; r < 4; ++r) { o[r] = kp[r] + r0[r]; o[4 + r] = kp[4 + r] + r1[r]; }
        o[8] = kp[8] + r2;
        if (pass == 1 || !bad) break;
        float m0 = -INFINITY, m1 = -INFINITY;
        for (int t = 0; t < 64; ++t) {
            const bf16x8 kt = ldsr128(lds + t * 1024 + lane * 16);
            m0 = rowmax16(MFMA32(kt, q0, f32x16{}), m0);
            m1 = rowmax16(MFMA32(kt, q1, f32x16{}), m1);
        }
        m[0] = -halfmax(m0); m[1] = -halfmax(m1);
        lds_bar();
    }
    TSTAMP(2);
    const float rl = __builtin_amdgcn_rcpf(o[8]);
    const int qrow = qb * 256 + qp * 64 + half * 32 + r32;
    ST16(oaf, ((qrow >> 4) * 16 + (head >> 1)) * 1024 + (head & 1) * 512 + hi * 256 + (qrow & 15) * 16, 0,
         ((u32x4){cvtpk(o[0] * rl, o[1] * rl), cvtpk(o[2] * rl, o[3] * rl), cvtpk(o[4] * rl, o[5] * rl), cvtpk(o[6] * rl, o[7] * rl)}));
    }
    }
    const int rt5 = xcd * 4 + (bi & 3), ct5 = bi >> 2;
    const int rt = (xcd >> 1) * 4 + (bi & 3), ct = (xcd & 1) * 7 + (bi >> 2);
    if (w < 4) {
        const int lane = lane_id(), g = lane >> 4;
        const unsigned la = lds0 + w * 16384, lb = lds0 + 65536 + w * 1024, sw = ((lane & 7) ^ (g << 1)) << 4;
        const unsigned vb5 = (w * 8 + (lane >> 3)) * 1024 + ct5 * 128 + sw;
        unsigned va = (rt5 * 4 + w) * 16384 + lane * 16 + 4096, vb = vb5;
        asm volatile(K3_PROD : [va] "+v"(va), [vb] "+v"(vb) : [sa] "s"(oaf), [sb] "s"(wo), [la] "s"(la), [lb] "s"(lb) : "memory", "scc");
        if (bi < 28) {
            unsigned va0 = (rt * 8 + w * 2) * 16384 + lane * 16 + 4096, va1 = va0 + 16384;
            vb = (w * 8 + (lane >> 3)) * 3584 + ct * 256 + sw;
            asm volatile(K4_PROD : [va0] "+v"(va0), [va1] "+v"(va1), [vb] "+v"(vb)
                         : [sa] "s"(haf), [sg] "s"(wg), [su] "s"(wu), [la] "s"(la), [lb] "s"(lb), [sh] "s"(hstat),
                           [vh] "v"((rt * 8 + w * 2 + (lane >> 5)) * 1024 + (lane & 1) * 512 + (lane & 30) * 4), [vc] "v"(CSUM + ((w >> 1) * 1792 + ct * 128 + (w & 1) * 64 + lane) * 4),
                           [wh] "v"(la + (lane >> 1) * 16), [wc] "v"(la + 8192 + lane * 4)
                         : "memory", "scc", K4P_CLOB);
        } else
            asm volatile("s_barrier\n\ts_barrier\n\t" K4_SP :: [bi] "s"(bi), [w] "s"(w), [sp] "s"(wd) : "memory", "scc", "v64", "v65");
        va = (rt5 * 4 + w) * 57344 + lane * 16 + 4096; vb = vb5;
        asm volatile(K5_PROD "code_end:\n\t" : [va] "+v"(va), [vb] "+v"(vb) : [sa] "s"(act), [sb] "s"(wd), [la] "s"(la), [lb] "s"(lb) : "memory", "scc");
        return;
    }
    const int wm = (w - 4) >> 2, wc = w & 3;
    {
        const int lane = lane_id(), g = lane >> 4, r16 = lane & 15;
        hand_off(SYNC32, FLAG2 + b * 128, FLAG2 + xcd * 4096, FLAG2 + xcd * 4096 + 2048);
        TSKEEP(tc_); TSTAMP(0);
        if (wm == 0) {
            unsigned oo = ((rt5 * 4 + wc) * 16 + ct5 * 2) * 1024 + lane * 16, so = ((rt5 * 4 + wc) * 8 + ct5) * 128 + r16 * 8;
            f32x16 acc;
            const unsigned b0 = lds0 + 65536 + (16 * (g >> 1) + 4 * (g & 1) + (r16 >> 2)) * 128 + ((2 * (g & 1) + (r16 >> 3)) << 5) + (lane & 3) * 8;
            asm volatile(K3_PRE BAR0 K3_CONS : "=&{v[0:15]}"(acc), "+v"(oo), "+v"(so), "+s"(haf), "+s"(hstat) TSOUT
                         : [ro] "v"((rt5 * 64 + wc * 16 + r16) * 1024 + ct5 * 128 + g * 8), [rs] "s"(x), [aa] "v"(lds0 + wc * 16384 + lane * 16),
                           [b0] "v"(b0), [b1] "v"(b0 ^ 32), [b2] "v"(b0 ^ 64), [b3] "v"(b0 ^ 96) : "memory", "vcc", G64_CLOB);
            TSTAMP(2);
            unsigned pk[8];
#pragma unroll
            for (int j = 0; j < 8; ++j) pk[j] = cvtpk(acc[2 * j], acc[2 * j + 1]);
            ST16(haf, oo, 0, ((u32x4){pk[0], pk[1], pk[2], pk[3]}));
            ST16(haf, oo, 1024, ((u32x4){pk[4], pk[5], pk[6], pk[7]}));
            float s1 = 0.f, s2 = 0.f;
#pragma unroll
            for (int j = 0; j < 8; ++j) {
                const float h0 = bflo(pk[j]), h1 = bfhi(pk[j]);
                s1 += h0 + h1; s2 = __builtin_fmaf(h0, h0, __builtin_fmaf(h1, h1, s2));
            }
            s1 = xg_sum(s1); s2 = xg_sum(s2);
            if (g == 0) asm volatile("global_store_dwordx2 %0, %1, %2" STMOD :: "v"(so), "v"((f32x2){s1, s2}), "s"(hstat) : "memory");
        } else {
            asm volatile(K3_IDLE ::: "memory");
        }
        hand_off(SYNC32, FLAG3 + (rt5 * 8 + ct5) * 128, FLAG3 + rt * 2048, FLAGC + min(ct, 13) * 2048);
        TSKEEP(ta_); TSTAMP(0);
        if (bi < 28) {
            f32x16 acc[4], st;
            unsigned oo = ((rt * 8 + wm * 4) * 56 + ct * 4 + wc) * 1024 + lane * 16;
            const unsigned aa = lds0 + wm * 32768 + lane * 16;
            const unsigned b0 = lds0 + 65536 + (wc >> 1) * 49152 + (4 * g + (r16 >> 2)) * 128 + ((((wc & 1) * 2) ^ (2 * (g & 1) + (r16 >> 3))) << 5) + (lane & 3) * 8;
            asm volatile(BAR0 K4_CONS : "=&{v[0:15]}"(acc[0]), "=&{v[16:31]}"(acc[1]), "=&{v[32:47]}"(acc[2]), "=&{v[48:63]}"(acc[3]),
                           "=&{v[128:143]}"(st), "+v"(oo), "+s"(act) TSOUT
                         : [aa] "v"(aa), [b0] "v"(b0), [b1] "v"(b0 ^ 32), [wm] "s"(wm), [rh] "v"(lds0 + wm * 32768 + r16 * 16),
                           [rc] "v"(lds0 + 8192 + (wc >> 1) * 16384 + (wc & 1) * 128 + g * 16) : "memory", "scc", "vcc", K4_CLOB);
            TSTAMP(2);
#pragma unroll
            for (int i = 0; i < 4; ++i) {
                float y[8];
#pragma unroll
                for (int r = 0; r < 8; ++r) {
                    const float gv = acc[i][r] * st[4 * i + 1];
                    y[r] = gv * acc[i][8 + r] * __builtin_amdgcn_rcpf(__builtin_fmaf(__builtin_amdgcn_exp2f(gv), st[4 * i + 2], st[4 * i + 2]));
                }
                ST16(act + i * 57344, oo, 0,
                     ((u32x4){cvtpk(y[0], y[1]), cvtpk(y[2], y[3]), cvtpk(y[4], y[5]), cvtpk(y[6], y[7])}));
            }
        } else
            asm volatile(K4_SP :: [bi] "s"(bi), [w] "s"(w), [sp] "s"(wd) : "memory", "scc", "v64", "v65");
    }
    const int lane = lane_id(), g = lane >> 4, r16 = lane & 15;
    hand_off(SYNC16, FLAG5 + (rt * 16 + (xcd & 1) * 8 + ct5) * 128, FLAG5 + (rt5 >> 1) * 2048, 0);
    if (wm) return;
    unsigned oo = (rt5 * 64 + wc * 16 + r16) * 1024 + ct5 * 128 + (g & 1) * 32 + (g >> 1) * 16;
    f32x16 acc;
    const unsigned b0 = lds0 + 65536 + (4 * g + (r16 >> 2)) * 128 + ((2 * (g & 1) + (r16 >> 3)) << 5) + (lane & 3) * 8, c0 = b0 + 49152;
    asm volatile(K5_PRE BAR0 K5_CONS : "=&{v[0:15]}"(acc), "+v"(oo), "+s"(out) TSOUT
                 : [ro] "v"(((rt5 * 4 + wc) * 16 + ct5 * 2) * 1024 + lane * 16), [rs] "s"(haf), [aa] "v"(lds0 + wc * 16384 + lane * 16),
                   [b0] "v"(b0), [b1] "v"(b0 ^ 32), [b2] "v"(b0 ^ 64), [b3] "v"(b0 ^ 96),
                   [c0] "v"(c0), [c1] "v"(c0 ^ 32), [c2] "v"(c0 ^ 64), [c3] "v"(c0 ^ 96) : "memory", "vcc", G64_CLOB);
    TSTAMP(2);
    unsigned pk[8];
#pragma unroll
    for (int j = 0; j < 8; ++j) pk[j] = cvtpk(acc[2 * j], acc[2 * j + 1]);
    ST16(out, oo, 0, swap16(pk[0], pk[1], pk[2], pk[3]));
    ST16(out, oo, 64, swap16(pk[4], pk[5], pk[6], pk[7]));
    TSFLUSH(4, 256); TSPUT(1, tc_); TSPUT(2, ta_); if (bi < 28) TSPUT(3, tb_);
}

extern "C" void kernel_launch(void* const* d_in, const int* in_sizes, int n_in, void* d_out, int out_size, void* d_ws, size_t ws_size,
                              hipStream_t stream) {
    const char* x = (const char*)d_in[0];
    const char* wqkv = (const char*)d_in[1];
    const char* wo = (const char*)d_in[2];
    const char* wg = (const char*)d_in[4];
    const char* wu = (const char*)d_in[5];
    const char* wd = (const char*)d_in[6];
    char* ws = (char*)d_ws;
    (void)hipFuncSetAttribute((const void*)k1_qkv, hipFuncAttributeMaxDynamicSharedMemorySize, LDS_ALL);
    (void)hipFuncSetAttribute((const void*)k2_rest, hipFuncAttributeMaxDynamicSharedMemorySize, LDS_ALL);
    k1_qkv<<<256, 512, LDS_ALL, stream>>>(x, wqkv, ws);
    k2_rest<<<256, 768, LDS_ALL, stream>>>(ws, wo, wg, wu, wd, x, (char*)d_out);
}
```
